```python
import torch

def _softmax(x, axis=-1):
    return torch.softmax(x, dim=axis)
def _gelu(x):
    return torch.nn.functional.gelu(x, approximate="tanh")
def _silu(x):
    return torch.nn.functional.silu(x)
def _sigmoid(x):
    return torch.sigmoid(x)
def _layernorm(x, axis=-1, eps=1e-5):
    m = x.mean(dim=axis, keepdim=True)
    v = x.var(dim=axis, keepdim=True, unbiased=False)
    return (x - m) / torch.sqrt(v + eps)
def _scatter_add(tgt, idx, src, axis=0):
    return tgt.clone().index_add_(axis, idx, src)
def _segment_sum(src, seg, n):
    out = torch.zeros((n,) + src.shape[1:], dtype=src.dtype)
    return out.index_add_(0, seg, src)
def _pad(x, pw):
    flat = tuple(v for pair in reversed(pw) for v in pair)
    return torch.nn.functional.pad(x, flat)

kernel_name = 'interleave_layers__sliding_block_cached__dense_activation_gelu__decode'

def setup_inputs():

    _w = (lambda a: (lambda t: t.to(torch.bfloat16) if t.is_floating_point() else t)(torch.as_tensor(a)))
    def _A_setup():
        B, Sq, D = 4, 1, 576
        Hq, Hkv, Sk, Dh = 32, 16, 8192, 128
        assert Hq % Hkv == 0, (Hq, Hkv)
        return {
            "x": _w((lambda *s: torch.randn(*s, dtype=torch.bfloat16) * 0.05)(B, Sq, D)),
            "k_cache": _w((lambda *s: torch.randn(*s, dtype=torch.bfloat16) * 0.8)(B, Hkv, Sk, Dh)),
            "v_cache": _w((lambda *s: torch.randn(*s, dtype=torch.bfloat16) * 1.6)(B, Hkv, Sk, Dh)),
            "wq": _w((lambda *s: torch.randn(*s, dtype=torch.bfloat16) * 0.8)(D, Hq * Dh) * (D ** -0.5)),
            "wo": _w((lambda *s: torch.randn(*s, dtype=torch.bfloat16) * 1.6)(Hq * Dh, D) * ((Hq * Dh) ** -0.5)),
        }
    _A_in = _A_setup()

    def _B_setup():
        B, S, D, F = 4, 1, 576, 4608
        return {
            "x": _w((lambda *s: torch.randn(*s, dtype=torch.bfloat16) * 0.05)(B, S, D)),
            "w1": _w((lambda *s: torch.randn(*s, dtype=torch.bfloat16) * 0.4)(D, F) * (D ** -0.5)),
            "b1": _w((lambda *s: torch.randn(*s, dtype=torch.bfloat16) * 0.4)(F) * 0.01),
            "w2": _w((lambda *s: torch.randn(*s, dtype=torch.bfloat16) * 0.4)(F, D) * (F ** -0.5)),
            "b2": _w((lambda *s: torch.randn(*s, dtype=torch.bfloat16) * 0.4)(D) * 0.01),
        }
    _B_in = _B_setup()

    return {
        "A_x": _A_in["x"],
        "A_k_cache": _A_in["k_cache"],
        "A_v_cache": _A_in["v_cache"],
        "A_wq": _A_in["wq"],
        "A_wo": _A_in["wo"],
        "B_x": _B_in["x"],
        "B_w1": _B_in["w1"],
        "B_b1": _B_in["b1"],
        "B_w2": _B_in["w2"],
        "B_b2": _B_in["b2"],
    }

def reference(A_x, A_k_cache, A_v_cache, A_wq, A_wo, B_x, B_w1, B_b1, B_w2, B_b2):
    def _A_ref(x, k_cache, v_cache, wq, wo):
        B, Sq, D = x.shape
        Hq, Dh = 32, 128
        Hkv, Sk = k_cache.shape[1], k_cache.shape[2]
        W = min(4096, max(256, Sk // 4))
        rep = Hq // Hkv
        q = (lambda x, *d: x.permute(*d))((x @ wq).reshape(B, Sq, Hq, Dh), 0, 2, 1, 3)
        k = (lambda x, n, axis: torch.repeat_interleave(x, n, dim=axis))(k_cache[:, :, Sk - W:, :], rep, axis=1)
        v = (lambda x, n, axis: torch.repeat_interleave(x, n, dim=axis))(v_cache[:, :, Sk - W:, :], rep, axis=1)
        scores = torch.einsum("bhqd,bhkd->bhqk", q, k) * (Dh ** -0.5)
        p = _softmax(scores, axis=-1)
        o = torch.einsum("bhqk,bhkd->bhqd", p, v)
        return (lambda x, *d: x.permute(*d))(o, 0, 2, 1, 3).reshape(B, Sq, Hq * Dh) @ wo

    def _B_ref(x, w1, b1, w2, b2):
        h = torch.einsum("bsd,df->bsf", x, w1) + b1
        h = _gelu(h)
        return torch.einsum("bsf,fd->bsd", h, w2) + b2

    x = A_x
    x = x + _A_ref(_layernorm(x, axis=-1), A_k_cache, A_v_cache, A_wq, A_wo)
    x = x + _B_ref(_layernorm(x, axis=-1), B_w1, B_b1, B_w2, B_b2)
    x = x + _A_ref(_layernorm(x, axis=-1), A_k_cache, A_v_cache, A_wq, A_wo)
    x = x + _B_ref(_layernorm(x, axis=-1), B_w1, B_b1, B_w2, B_b2)
    return x
```

```cpp
#include <hip/hip_runtime.h>
#include <hip/hip_bf16.h>
#include <stdint.h>

typedef unsigned short u16;
__device__ __forceinline__ float bf2f(u16 b) { return __uint_as_float(((unsigned)b) << 16); }
__device__ __forceinline__ u16 f2bf(float f) { __hip_bfloat16 h = __float2bfloat16(f); return *reinterpret_cast<u16*>(&h); }
__device__ __forceinline__ float rb(float f) { return bf2f(f2bf(f)); }

constexpr int B = 4, D = 576, HQ = 32, HKV = 16, SK = 8192, DH = 128, W = 2048, F = 4608;

__global__ void k_ln(const float* x, float* xn) {
  int b = blockIdx.x, t = threadIdx.x;
  float s = 0.f;
  for (int i = t; i < D; i += 64) s += x[b * D + i];
  for (int o = 32; o; o >>= 1) s += __shfl_xor(s, o);
  float m = s / D;
  float v = 0.f;
  for (int i = t; i < D; i += 64) { float d = x[b * D + i] - m; v += d * d; }
  for (int o = 32; o; o >>= 1) v += __shfl_xor(v, o);
  v /= D;
  float r = rsqrtf(v + 1e-5f);
  for (int i = t; i < D; i += 64) xn[b * D + i] = rb((x[b * D + i] - m) * r);
}
__global__ void k_cvt_in(const u16* xin, float* x) { int i = blockIdx.x * blockDim.x + threadIdx.x; if (i < B * D) x[i] = bf2f(xin[i]); }
__global__ void k_cvt_out(const float* x, u16* o) { int i = blockIdx.x * blockDim.x + threadIdx.x; if (i < B * D) o[i] = f2bf(x[i]); }
template <int MODE>
__global__ void k_gemv(const float* in, const u16* w, const u16* bias, const float* res, float* out, int K, int N) {
  __shared__ float red[4][4][64];
  int n = blockIdx.x * 64 + (threadIdx.x & 63), kw = threadIdx.x >> 6;
  float a[4] = {0, 0, 0, 0};
  for (int k = kw; k < K; k += 4) { float wv = bf2f(w[(size_t)k * N + n]);
    for (int b = 0; b < 4; ++b) a[b] += in[b * K + k] * wv; }
  for (int b = 0; b < 4; ++b) red[kw][b][threadIdx.x & 63] = a[b];
  __syncthreads();
  int b = kw; float s = red[0][b][threadIdx.x & 63] + red[1][b][threadIdx.x & 63] + red[2][b][threadIdx.x & 63] + red[3][b][threadIdx.x & 63];
  s = rb(s);
  if (MODE == 1 || MODE == 3) s = rb(s + bf2f(bias[n]));
  if (MODE == 1) { float u = 0.7978845608028654f * (s + 0.044715f * s * s * s); s = rb(0.5f * s * (1.f + tanhf(u))); }
  if (MODE == 2 || MODE == 3) s = rb(res[b * N + n] + s);
  out[b * N + n] = s;
}
__global__ void k_attn(const float* q, const u16* kc, const u16* vc, float* o) {
  __shared__ float sc[W]; __shared__ float red[256]; __shared__ float qs[DH];
  int h = blockIdx.x, b = blockIdx.y, t = threadIdx.x;
  const u16* kp = kc + ((size_t)(b * HKV + h / 2) * SK + (SK - W)) * DH;
  const u16* vp = vc + ((size_t)(b * HKV + h / 2) * SK + (SK - W)) * DH;
  if (t < DH) qs[t] = q[b * HQ * DH + h * DH + t];
  __syncthreads();
  float mx = -1e30f;
  for (int j = t; j < W; j += 256) { float s = 0; for (int d = 0; d < DH; ++d) s += qs[d] * bf2f(kp[(size_t)j * DH + d]); s = rb(rb(s) * 0.08838834764831845f); sc[j] = s; mx = fmaxf(mx, s); }
  red[t] = mx; __syncthreads();
  for (int o2 = 128; o2; o2 >>= 1) { if (t < o2) red[t] = fmaxf(red[t], red[t + o2]); __syncthreads(); }
  mx = red[0]; __syncthreads();
  float sm = 0; for (int j = t; j < W; j += 256) { float e = __expf(sc[j] - mx); sc[j] = e; sm += e; }
  red[t] = sm; __syncthreads();
  for (int o2 = 128; o2; o2 >>= 1) { if (t < o2) red[t] += red[t + o2]; __syncthreads(); }
  sm = red[0]; __syncthreads();
  int d = t & 127, half = t >> 7; float acc = 0;
  for (int j = half; j < W; j += 2) acc += rb(sc[j] / sm) * bf2f(vp[(size_t)j * DH + d]);
  red[t] = acc; __syncthreads();
  if (t < DH) o[b * HQ * DH + h * DH + t] = rb(red[t] + red[t + 128]);
}

extern "C" void kernel_launch(void* const* d_in, const int* in_sizes, int n_in, void* d_out, int out_size, void* d_ws, size_t ws_size, hipStream_t stream) {
  const u16* x = (const u16*)d_in[0]; const u16* kc = (const u16*)d_in[1]; const u16* vc = (const u16*)d_in[2];
  const u16* wq = (const u16*)d_in[3]; const u16* wo = (const u16*)d_in[4];
  const u16* w1 = (const u16*)d_in[6]; const u16* b1 = (const u16*)d_in[7]; const u16* w2 = (const u16*)d_in[8]; const u16* b2 = (const u16*)d_in[9];
  float* ws = (float*)d_ws;
  float* xa = ws, *xb = ws + 4096, *xn = ws + 8192, *q = ws + 16384, *o = ws + 49152, *h = ws + 81920;
  k_cvt_in<<<9, 256, 0, stream>>>(x, xa);
  for (int l = 0; l < 2; ++l) {
    k_ln<<<4, 64, 0, stream>>>(xa, xn);
    k_gemv<0><<<4096 / 64, 256, 0, stream>>>(xn, wq, nullptr, nullptr, q, D, 4096);
    k_attn<<<dim3(HQ, B), 256, 0, stream>>>(q, kc, vc, o);
    k_gemv<2><<<D / 64, 256, 0, stream>>>(o, wo, nullptr, xa, xb, 4096, D);
    k_ln<<<4, 64, 0, stream>>>(xb, xn);
    k_gemv<1><<<F / 64, 256, 0, stream>>>(xn, w1, b1, nullptr, h, D, F);
    k_gemv<3><<<D / 64, 256, 0, stream>>>(h, w2, b2, xb, xa, F, D);
  }
  k_cvt_out<<<9, 256, 0, stream>>>(xa, (u16*)d_out);
}
```

```cpp
#include <hip/hip_runtime.h>
#include <stdint.h>

#define STAMPS 0

typedef unsigned short u16;
typedef unsigned long long u64;
typedef unsigned v4u __attribute__((ext_vector_type(4)));
typedef unsigned v2u __attribute__((ext_vector_type(2)));
typedef float f4 __attribute__((ext_vector_type(4)));
typedef float f2 __attribute__((ext_vector_type(2)));
typedef __bf16 bf8 __attribute__((ext_vector_type(8)));
typedef short s4v __attribute__((ext_vector_type(4)));
#define LAS __attribute__((address_space(3)))
#define DI __device__ __forceinline__
typedef __amdgpu_buffer_rsrc_t rsrc_t;

constexpr int DM = 576, SK = 8192, WIN = 2048, DH = 128, FF = 4608, QN = 4096;
constexpr unsigned SENT = 0xFFFFFFFFu;
constexpr int AUX_NT = 2, AUX_SC1 = 16;

constexpr int WS_EPOCH = 0, WS_FLAGS = 4096, WS_X = 8192;
constexpr int X1_MSG = 5376, X2_MSG = 1152, X4_MSG = 3968;
constexpr int X1_OFF = 0, X2_OFF = X1_OFF + 144 * X1_MSG, X3_OFF = X2_OFF + 256 * X2_MSG, X4_OFF = X3_OFF + 144 * 1024,
              X5_OFF = X4_OFF + 216 * X4_MSG, XL_BYTES = X5_OFF + 216 * 1024;
constexpr int XL_STRIDE = 0x240000;
static_assert(XL_BYTES <= XL_STRIDE, "layer stride");
constexpr int WS_USED = WS_X + 2 * XL_STRIDE;
constexpr int WS_STAMP = 8 << 20;

constexpr int L_W = 0;
constexpr int L_AO = 131072;
constexpr int L_RD = L_AO + 8192;
constexpr int L_XB = L_RD + 8192;
constexpr int L_ST = L_XB + 1024;
constexpr int L_QB = L_ST + 256;
constexpr int L_ML = L_QB + 512;
constexpr int L_GO = L_ML + 128;
constexpr int L_BYTES = L_GO + 64;
constexpr int XBS = 144;

#define NK0 4
#define D_X1 10
#define D_X2 10
#define D_X3 10
#define D_X4 10
#define D_X5 10
constexpr int SPIN_CAP = 1 << 18;

struct Params {
  const u16 *x, *kc, *vc, *wq, *wo, *w1, *b1, *w2, *b2;
  u16* out;
  unsigned char* ws;
};

DI f4 mfma(v4u a, v4u b, f4 c) { return __builtin_amdgcn_mfma_f32_16x16x32_bf16(__builtin_bit_cast(bf8, a), __builtin_bit_cast(bf8, b), c, 0, 0, 0); }
DI float asf(unsigned u) { return __builtin_bit_cast(float, u); }
DI unsigned asu(float f) { return __builtin_bit_cast(unsigned, f); }
template <int C> DI unsigned dppu(unsigned x) { return (unsigned)__builtin_amdgcn_mov_dpp((int)x, C, 0xf, 0xf, true); }
template <int C> DI float dppf(float x) { return asf(dppu<C>(asu(x))); }
constexpr int XOR1 = 0xB1, XOR2 = 0x4E, ROR4 = 0x124, ROR8 = 0x128;
DI float add16(float x) { auto s = __builtin_amdgcn_permlane16_swap(asu(x), asu(x), false, false); return asf(s[0]) + asf(s[1]); }
DI float add32(float x) { auto s = __builtin_amdgcn_permlane32_swap(asu(x), asu(x), false, false); return asf(s[0]) + asf(s[1]); }
DI float max16(float x) { auto s = __builtin_amdgcn_permlane16_swap(asu(x), asu(x), false, false); return fmaxf(asf(s[0]), asf(s[1])); }
DI float max32(float x) { auto s = __builtin_amdgcn_permlane32_swap(asu(x), asu(x), false, false); return fmaxf(asf(s[0]), asf(s[1])); }
DI float sum8_16_32(float x) { x += dppf<ROR8>(x); x = add16(x); return add32(x); }
typedef __bf16 bf2 __attribute__((ext_vector_type(2)));
DI unsigned pk(float lo, float hi) { bf2 v = {(__bf16)lo, (__bf16)hi}; return __builtin_bit_cast(unsigned, v); }
DI float rbf(float x) { return (float)(__bf16)x; }
DI float blo(unsigned u) { return asf(u << 16); }
DI float bhi(unsigned u) { return asf(u & 0xffff0000u); }
DI f4 f4of(v4u v) { return __builtin_bit_cast(f4, v); }
DI v4u clean(f4 v) { v4u u = __builtin_bit_cast(v4u, v); u.x = min(u.x, SENT - 1); u.y = min(u.y, SENT - 1); u.z = min(u.z, SENT - 1); u.w = min(u.w, SENT - 1); return u; }
DI unsigned mx4(v4u v) { return max(max(v.x, v.y), max(v.z, v.w)); }
DI v4u ldx(rsrc_t r, int voff, int soff) { return __builtin_amdgcn_raw_buffer_load_b128(r, voff, soff, AUX_SC1); }
DI v2u ldx2(rsrc_t r, int voff, int soff) { return __builtin_amdgcn_raw_buffer_load_b64(r, voff, soff, AUX_SC1); }
DI void stx(rsrc_t r, v4u v, int voff, int soff) { __builtin_amdgcn_raw_buffer_store_b128(v, r, voff, soff, AUX_SC1); }
DI void stx2(rsrc_t r, v2u v, int voff, int soff) { __builtin_amdgcn_raw_buffer_store_b64(v, r, voff, soff, AUX_SC1); }
DI void bar() { asm volatile("s_waitcnt lgkmcnt(0)\n\ts_barrier" ::: "memory"); }
DI rsrc_t mkrs(const void* p, unsigned bytes) { return __builtin_amdgcn_make_buffer_rsrc((void*)p, (short)0, (int)bytes, 0x00020000); }
DI void dma(rsrc_t r, int voff, int soff, unsigned ldsaddr) {
  unsigned keep;
  asm volatile("s_nop 4\n\ts_mov_b32 %0, m0\n\ts_mov_b32 m0, %1\n\ts_nop 0\n\tbuffer_load_dwordx4 %2, %3, %4 offen nt lds\n\ts_mov_b32 m0, %0"
               : "=&s"(keep) : "s"(ldsaddr), "v"(voff), "s"(r), "s"(soff) : "memory");
}
DI v4u lds16(LAS unsigned char* L, int off) { return *(LAS v4u*)(L + off); }
DI v4u trfrag(LAS unsigned char* L, int off, int step) {
  s4v a = __builtin_amdgcn_ds_read_tr16_b64_v4i16((LAS s4v*)(L + off));
  s4v b = __builtin_amdgcn_ds_read_tr16_b64_v4i16((LAS s4v*)(L + off + step));
  v2u x = __builtin_bit_cast(v2u, a), y = __builtin_bit_cast(v2u, b);
  return v4u{x.x, x.y, y.x, y.y};
}
DI bool anybad(unsigned m) { return __builtin_amdgcn_ballot_w64(m == SENT) != 0; }
#define OPAQUE() asm volatile("" ::: "memory")
#define STAMP(i) do {} while (0)
#define COUNT(i, v) do {} while (0)

__global__ void __launch_bounds__(512, 2) fused_fwd(Params p) {
  extern __shared__ __attribute__((aligned(16))) unsigned char smem[];
  LAS unsigned char* L = (LAS unsigned char*)smem;
  const int tid = threadIdx.x, lane = tid & 63, w = __builtin_amdgcn_readfirstlane(tid >> 6);
  const int c = blockIdx.x, xc = c & 7, j = c >> 3, grp = j >> 4, u = j & 15, g = 2 * xc + grp;
  STAMP(0);
  const int ab = u >> 2, as = u & 3;
  const bool hasP1 = u <= 8, hasP3 = u >= 7;
  const int r1 = u < 8 ? xc : 8, gq1 = u < 8 ? 8 * grp + u : g, n3 = u - 7;
  const int fi = 14 * grp + (u <= 6 ? u : u - 2);
  const bool hasF = (u != 7) && (u != 8) && (fi < 27);
  const int n4 = fi < 24 ? xc : 8, f4i = fi < 24 ? fi : 3 * xc + fi - 24, f5 = 3 * xc + fi / 9, n5 = fi % 9;
  const bool hasT = hasF || hasP1;
  const int sl = hasF ? n4 : r1;
  const bool isOut = (j == 0) || (c == 64);
  const rsrc_t rsW = mkrs(p.ws, WS_USED);
  const int myX1 = WS_X + X1_OFF + (gq1 * 9 + r1) * X1_MSG, myX2 = WS_X + X2_OFF + (g * 16 + u) * X2_MSG, myX3 = WS_X + X3_OFF + (n3 * 16 + g) * 1024,
            myX4 = WS_X + X4_OFF + (f4i * 9 + n4) * X4_MSG, myX5 = WS_X + X5_OFF + (n5 * 24 + f5) * 1024;
  if (w == 0) __builtin_amdgcn_s_setprio(3);

  {
    const v4u S = {SENT, SENT, SENT, SENT};
    int a0, a1, a2, a3, n0, n1, n2, n3c;
    if (w == 0) { a0 = myX2; a1 = myX2 + 1024; a2 = myX2 + XL_STRIDE; a3 = a2 + 1024; n0 = 64; n1 = 8; n2 = 64; n3c = 8; }
    else if (w == 1) { a0 = myX3; a1 = myX3 + XL_STRIDE; a2 = myX5; a3 = myX5 + XL_STRIDE; n0 = n1 = hasP3 ? 64 : 0; n2 = n3c = hasF ? 64 : 0; }
    else if (w < 4) { a0 = myX4 + (w - 2) * XL_STRIDE; a1 = a0 + 1024; a2 = a0 + 2048; a3 = a0 + 3072; n0 = n1 = n2 = hasF ? 64 : 0; n3c = hasF ? 56 : 0; }
    else { a0 = myX1 + ((w - 4) >> 1) * XL_STRIDE + (w & 1) * 3072; a1 = a0 + 1024; a2 = a0 + 2048; a3 = a0; n0 = n1 = hasP1 ? 64 : 0; n2 = hasP1 ? ((w & 1) ? 8 : 64) : 0; n3c = 0; }
    if (lane < n0) stx(rsW, S, a0 + lane * 16, 0);
    if (lane < n1) stx(rsW, S, a1 + lane * 16, 0);
    if (lane < n2) stx(rsW, S, a2 + lane * 16, 0);
    if (lane < n3c) stx(rsW, S, a3 + lane * 16, 0);
  }
  OPAQUE();
  const v2u ep = ldx2(rsW, 0, WS_EPOCH);
  const int tb = (lane >> 1) & 3, th = lane & 1;
  v2u x0 = {0, 0}, b2v = {0, 0}, b1v = {0, 0};
  if (hasT) {
    x0 = *(const v2u*)(p.x + tb * DM + 64 * sl + 8 * w + 4 * th);
    b2v = *(const v2u*)(p.b2 + 64 * sl + 8 * w + 4 * th);
  }
  if (hasF && w < 6) b1v = *(const v2u*)(p.b1 + 192 * f5 + 32 * w + 8 * (lane >> 4) + 4 * ((lane >> 2) & 1));
  const unsigned ldsW = (unsigned)(size_t)(L + L_W) + w * 16384;
  const int c64 = ((lane & 3) ^ (2 * (lane >> 5))) * 16;
  if (hasP1) {
    const rsrc_t rq = mkrs(p.wq, DM * QN * 2);
    const int vo = (lane >> 2) * (QN * 2) + c64;
#pragma unroll
    for (int i = 0; i < 4; ++i) dma(rq, vo, (64 * r1 + 16 * i) * (QN * 2) + gq1 * 512 + w * 64, ldsW + i * 1024);
  }
  const size_t kvbase = ((size_t)(ab * 16 + g) * SK + (SK - WIN) + as * 512 + w * 64) * DH;
  const rsrc_t rsK = mkrs(p.kc + kvbase, 16384), rsV = mkrs(p.vc + kvbase, 16384);
  const int rot = (w >> 1) + 4 * (j & 3) + 2 * xc;
  const int kvo = ((lane & 15) >> 2) * 256 + (4 * (lane >> 4) + (lane & 3)) * 16, vvo = lane * 16;
  v4u K[16], V[16];
#pragma unroll
  for (int i = 0; i < NK0; ++i) K[i] = __builtin_amdgcn_raw_buffer_load_b128(rsK, kvo, ((i + rot) & 15) * 1024, AUX_NT);
  asm volatile("s_waitcnt vmcnt(%0)" ::"i"(NK0) : "memory");

  float xr[4];
  xr[0] = blo(x0.x); xr[1] = bhi(x0.x); xr[2] = blo(x0.y); xr[3] = bhi(x0.y);
  auto track_finish = [&]() {
    float s1 = (xr[0] + xr[1]) + (xr[2] + xr[3]), s2 = (xr[0] * xr[0] + xr[1] * xr[1]) + (xr[2] * xr[2] + xr[3] * xr[3]);
    s1 += dppf<XOR1>(s1); s2 += dppf<XOR1>(s2);
    if (lane < 8) {
      *(LAS v2u*)(L + L_XB + tb * XBS + (8 * w + 4 * th) * 2) = v2u{pk(xr[0], xr[1]), pk(xr[2], xr[3])};
      if (th == 0) *(LAS f2*)(L + L_ST + (w * 4 + tb) * 8) = f2{s1, s2};
    }
  };
  if (tid < 72) *(LAS unsigned*)(L + L_XB + 4 * XBS + tid * 4) = tid < 36 ? 0x3F803F80u : 0u;
  if (tid == 0) *(LAS unsigned*)(L + L_GO) = 0u;
  track_finish();
  bar();
  STAMP(1);
  const u64 tag = (((u64)ep.y << 32) | ep.x) * 6364136223846793005ull + 1442695040888963407ull;
  if (w == 0 && lane == 0) { const u64 f = tag + (u64)c * 0x9E3779B97F4A7C15ull; stx2(rsW, v2u{(unsigned)f, (unsigned)(f >> 32)}, 0, WS_FLAGS + c * 8); }

  const int col = lane & 15, hq = lane >> 4;
  const int xb0 = L_XB + ((col < 4) ? col : (col == 8 ? 4 : 5)) * XBS + hq * 16;
  const int xb1 = L_XB + ((col >= 4 && col < 8) ? col - 4 : (col == 12 ? 4 : 5)) * XBS + hq * 16;
  const int q4 = (lane & 15) >> 2, pp = lane & 3;
  const int a64 = L_W + w * 16384 + hq * 512 + q4 * 64 + pp * 8;
  const int a128 = L_W + w * 16384 + hq * 1024 + q4 * 128 + pp * 8;
  const int z128 = ((q4 >> 1) & 1) | ((hq & 1) << 1);
  auto two_tile = [&](int tile) -> f4 {
    const int s0 = (hq & 1) * 32, s1 = 32 - s0, base = a64 + tile * 4096;
    const v4u A00 = trfrag(L, base + s0, 256), A01 = trfrag(L, base + s0 + 2048, 256), A10 = trfrag(L, base + s1, 256), A11 = trfrag(L, base + s1 + 2048, 256);
    const v4u B00 = lds16(L, xb0), B01 = lds16(L, xb0 + 64), B10 = lds16(L, xb1), B11 = lds16(L, xb1 + 64);
    f4 d0 = mfma(A00, B00, f4{0, 0, 0, 0}), d1 = mfma(A10, B10, f4{0, 0, 0, 0});
    d0 = mfma(A01, B01, d0); d1 = mfma(A11, B11, d1);
    return d0 + d1;
  };
  auto pub_stats = [&](int off) {
    f2 s = *(LAS f2*)(L + L_ST + (lane & 31) * 8);
    s.x += dppf<ROR4>(s.x); s.y += dppf<ROR4>(s.y); s.x += dppf<ROR8>(s.x); s.y += dppf<ROR8>(s.y);
    s.x = add16(s.x); s.y = add16(s.y);
    if (lane < 4) stx(rsW, clean(f4{s.x, s.y, 0.f, 0.f}), lane * 16, off);
  };
  auto do_p1 = [&](int lay) {
    const f4 d = two_tile(0);
    const int t = (col >> 2) & 1, nb = (32 * w + 16 * t + 4 * hq) * 4;
    const int so = myX1 + lay * XL_STRIDE;
    if (col < 8) stx(rsW, clean(d), (col & 3) * 1024 + nb, so);
    if (lay == 0 && (col & 11) == 8) stx(rsW, clean(d), 4096 + nb, so);
    if (w == 7) pub_stats(so + 5120);
  };
  if (hasP1) do_p1(0);
  STAMP(2);
#pragma unroll
  for (int i = NK0; i < 16; ++i) K[i] = __builtin_amdgcn_raw_buffer_load_b128(rsK, kvo, ((i + rot) & 15) * 1024, AUX_NT);
#pragma unroll
  for (int i = 0; i < 16; ++i) V[i] = __builtin_amdgcn_raw_buffer_load_b128(rsV, vvo, ((i + rot) & 15) * 1024, AUX_NT);
  OPAQUE();
  STAMP(3);
  if (w == 0) {
    u64 e[4];
#pragma unroll
    for (int k = 0; k < 4; ++k) e[k] = tag + (u64)(lane * 2 + (k & 1) + (k >> 1) * 128) * 0x9E3779B97F4A7C15ull;
    int spin = 0;
    for (;; ++spin) {
      OPAQUE();
      const v4u a = ldx(rsW, lane * 16, WS_FLAGS), b = ldx(rsW, lane * 16, WS_FLAGS + 1024);
      const bool ok = a.x == (unsigned)e[0] && a.y == (unsigned)(e[0] >> 32) && a.z == (unsigned)e[1] && a.w == (unsigned)(e[1] >> 32) &&
                      b.x == (unsigned)e[2] && b.y == (unsigned)(e[2] >> 32) && b.z == (unsigned)e[3] && b.w == (unsigned)(e[3] >> 32);
      if (__builtin_amdgcn_ballot_w64(!ok) == 0 || spin > SPIN_CAP) break;
      __builtin_amdgcn_s_sleep(8);
    }
    COUNT(40, spin);
    if (c == 0 && lane == 0) stx2(rsW, v2u{(unsigned)tag, (unsigned)(tag >> 32)}, 0, WS_EPOCH);
    if (lane == 0) *(volatile LAS unsigned*)(L + L_GO) = 1u;
    __builtin_amdgcn_s_setprio(0);
    STAMP(4);
  }
  {
    const bool o1 = lane & 1, o2 = lane & 2;
#pragma unroll
    for (int T = 0; T < 4; ++T)
#pragma unroll
      for (int d = 0; d < 4; ++d) {
        const unsigned m0 = K[4 * T][d], m1 = K[4 * T + 1][d], m2 = K[4 * T + 2][d], m3 = K[4 * T + 3][d];
        const unsigned s0 = dppu<XOR1>(m0), s1 = dppu<XOR1>(m1), s2 = dppu<XOR1>(m2), s3 = dppu<XOR1>(m3);
        const unsigned n0 = o1 ? s1 : m0, n1 = o1 ? m1 : s0, n2 = o1 ? s3 : m2, n3_ = o1 ? m3 : s2;
        const unsigned t0 = dppu<XOR2>(n0), t1 = dppu<XOR2>(n1), t2 = dppu<XOR2>(n2), t3 = dppu<XOR2>(n3_);
        K[4 * T][d] = o2 ? t2 : n0; K[4 * T + 2][d] = o2 ? n2 : t0;
        K[4 * T + 1][d] = o2 ? t3 : n1; K[4 * T + 3][d] = o2 ? n3_ : t1;
      }
  }
  asm volatile("" : "+v"(K[0]), "+v"(K[1]), "+v"(K[2]), "+v"(K[3]), "+v"(K[4]), "+v"(K[5]), "+v"(K[6]), "+v"(K[7]), "+v"(K[8]), "+v"(K[9]), "+v"(K[10]), "+v"(K[11]), "+v"(K[12]), "+v"(K[13]), "+v"(K[14]), "+v"(K[15]));
  for (int spin = 0; *(volatile LAS unsigned*)(L + L_GO) == 0u && spin < (SPIN_CAP << 4); ++spin) __builtin_amdgcn_s_sleep(1);
  STAMP(5);

  float csq[4] = {0, 0, 0, 0}, cs1[4] = {0, 0, 0, 0};
  unsigned pm[4];
#pragma unroll
  for (int r = 0; r < 4; ++r) pm[r] = (((lane & 7) >> 1) == r) ? ((lane & 1) ? 0xFFFF0000u : 0x0000FFFFu) : 0u;
  const unsigned tm0 = ((lane >> 2) & 3) == 0 ? ~0u : 0u, tm1 = ((lane >> 2) & 3) == 1 ? ~0u : 0u, tm2 = ((lane >> 2) & 3) == 2 ? ~0u : 0u, tm3 = ((lane >> 2) & 3) == 3 ? ~0u : 0u;
  const int rdw = (2 * ((lane >> 2) & 3) + (hq >> 1)) * 128 + (lane & 3) * 32 + (hq & 1) * 16;
  auto four_tile = [&](int tile, v4u B) -> f4 {
    const int base = a128 + tile * 4096;
    const v4u A0 = trfrag(L, base + (0 ^ z128) * 32, 512), A1 = trfrag(L, base + (1 ^ z128) * 32, 512), A2 = trfrag(L, base + (2 ^ z128) * 32, 512), A3 = trfrag(L, base + (3 ^ z128) * 32, 512);
    f4 d0 = mfma(A0, B & tm0, f4{0, 0, 0, 0}), d1 = mfma(A1, B & tm1, f4{0, 0, 0, 0});
    d0 = mfma(A2, B & tm2, d0); d1 = mfma(A3, B & tm3, d1);
    return d0 + d1;
  };
  auto reduce_pub = [&](int nsrc, int so) {
    f4 v = *(LAS f4*)(L + L_RD + (lane >> 3) * 1024 + w * 128 + (lane & 7) * 16);
    if ((lane >> 3) >= nsrc) v = f4{0, 0, 0, 0};
    v.x = sum8_16_32(v.x); v.y = sum8_16_32(v.y); v.z = sum8_16_32(v.z); v.w = sum8_16_32(v.w);
    if (lane < 8) stx(rsW, clean(v), w * 128 + lane * 16, so);
  };
  constexpr float CS = 0.08838834764831845f * 1.4426950408889634f;

  for (int lay = 0; lay < 2; ++lay) {
    const int xo = WS_X + lay * XL_STRIDE;
    {
      const int so = xo + X1_OFF + g * 9 * X1_MSG;
      const int vo = (lane >> 3) * X1_MSG + ab * 1024 + w * 128 + (lane & 7) * 16;
      const bool st = lane >= 16 && lane < 25;
      const int vo1 = st ? (lane - 16) * X1_MSG + 5120 + ab * 16 : 8 * X1_MSG + ab * 1024 + w * 128 + (lane & 7) * 16;
      if (lay == 1) __builtin_amdgcn_s_sleep(D_X1);
      v4u d0, d1, e0 = {0, 0, 0, 0}, e1 = {0, 0, 0, 0};
      int spin = 0;
      for (;; ++spin) {
        OPAQUE();
        d0 = ldx(rsW, vo, so); d1 = ldx(rsW, vo1, so);
        if (lay == 0) { e0 = ldx(rsW, (lane >> 3) * X1_MSG + 4096 + w * 128 + (lane & 7) * 16, so); e1 = ldx(rsW, 8 * X1_MSG + 4096 + w * 128 + (lane & 7) * 16, so); }
        if (!anybad(max(max(mx4(d0), mx4(d1)), max(mx4(e0), mx4(e1)))) || spin > SPIN_CAP) break;
      }
      OPAQUE();
      COUNT(41 + lay * 8, spin);
      STAMP(6 + lay * 16);
      if (lay == 0) {
        asm volatile("" ::"v"(V[0]), "v"(V[1]), "v"(V[2]), "v"(V[3]), "v"(V[4]), "v"(V[5]), "v"(V[6]), "v"(V[7]), "v"(V[8]), "v"(V[9]), "v"(V[10]), "v"(V[11]), "v"(V[12]), "v"(V[13]), "v"(V[14]), "v"(V[15]) : "memory");
        if (hasP3) {
          const rsrc_t ro = mkrs(p.wo, QN * DM * 2);
#pragma unroll
          for (int i = 0; i < 4; ++i)
            dma(ro, (lane >> 3) * (DM * 2) + ((((lane & 7) >> 1) ^ (((lane >> 4) & 1) | ((i & 1) << 1))) * 32) + (lane & 1) * 16, (256 * g + 32 * w + 8 * i) * (DM * 2) + n3 * 128, ldsW + 4096 + i * 1024);
        }
        const f4 a = f4of(e0), b = lane < 8 ? f4of(e1) : f4{0, 0, 0, 0};
#pragma unroll
        for (int k = 0; k < 4; ++k) csq[k] = sum8_16_32(a[k] + b[k]);
      }
      float s1 = st ? asf(d1.x) : 0.f, s2 = st ? asf(d1.y) : 0.f;
      s1 += dppf<XOR1>(s1); s2 += dppf<XOR1>(s2); s1 += dppf<XOR2>(s1); s2 += dppf<XOR2>(s2);
      s1 += dppf<ROR4>(s1); s2 += dppf<ROR4>(s2); s1 += dppf<ROR8>(s1); s2 += dppf<ROR8>(s2);
      s1 = asf(__builtin_amdgcn_readlane(asu(s1), 16)); s2 = asf(__builtin_amdgcn_readlane(asu(s2), 16));
      const float mu = s1 * (1.f / DM), rstd = __builtin_amdgcn_rsqf(fmaxf(s2 * (1.f / DM) - mu * mu, 0.f) + 1e-5f);
      const f4 a = f4of(d0), b = lane < 8 ? f4of(d1) : f4{0, 0, 0, 0};
      float q[4];
#pragma unroll
      for (int k = 0; k < 4; ++k) q[k] = (sum8_16_32(a[k] + b[k]) - mu * csq[k]) * rstd;
      if (lane < 8) *(LAS v2u*)(L + L_QB + (32 * w + 4 * lane) * 2) = v2u{pk(q[0], q[1]), pk(q[2], q[3])};
    }
    bar();
    STAMP(7 + lay * 16);
    {
      const int qa = L_QB + ((lane >> 3) & 1) * 256 + hq * 64;
      v4u Q[4];
#pragma unroll
      for (int t = 0; t < 4; ++t) Q[t] = lds16(L, qa + t * 16);
      f4 S[4];
#pragma unroll
      for (int T = 0; T < 4; ++T) S[T] = mfma(K[4 * T], Q[0], f4{0, 0, 0, 0});
#pragma unroll
      for (int t = 1; t < 4; ++t)
#pragma unroll
        for (int T = 0; T < 4; ++T) S[T] = mfma(K[4 * T + t], Q[t], S[T]);
      float m = fmaxf(fmaxf(S[0][0], S[0][1]), fmaxf(S[0][2], S[0][3]));
#pragma unroll
      for (int T = 1; T < 4; ++T) m = fmaxf(m, fmaxf(fmaxf(S[T][0], S[T][1]), fmaxf(S[T][2], S[T][3])));
      m = max32(max16(m));
      const float mc = m * CS;
      float ls = 0.f;
      f4 O = {0, 0, 0, 0};
#pragma unroll
      for (int T = 0; T < 4; ++T)
#pragma unroll
        for (int r = 0; r < 4; ++r) {
          const float e = __builtin_amdgcn_exp2f(S[T][r] * CS - mc);
          ls += e;
          const unsigned pd = pk(e, e);
          O = mfma(v4u{pd & pm[0], pd & pm[1], pd & pm[2], pd & pm[3]}, V[4 * T + r], O);
        }
      ls = add32(add16(ls));
      *(LAS f4*)(L + L_AO + w * 1024 + ((lane >> 5) * 128 + 8 * col + 4 * (hq & 1)) * 4) = O;
      if ((lane & 55) == 0) *(LAS f2*)(L + L_ML + (w * 2 + (lane >> 3)) * 8) = f2{mc, ls};
    }
    bar();
    STAMP(8 + lay * 16);
    {
      f4 v = *(LAS f4*)(L + L_AO + (lane >> 3) * 1024 + w * 128 + (lane & 7) * 16);
      const f2 ml = *(LAS f2*)(L + L_ML + ((lane >> 3) * 2 + (w >> 2)) * 8);
      float M = fmaxf(ml.x, dppf<ROR8>(ml.x)); M = max32(max16(M));
      const float f = __builtin_amdgcn_exp2f(ml.x - M);
      v.x = sum8_16_32(v.x * f); v.y = sum8_16_32(v.y * f); v.z = sum8_16_32(v.z * f); v.w = sum8_16_32(v.w * f);
      const int so = myX2 + lay * XL_STRIDE;
      if (lane < 8) stx(rsW, clean(v), w * 128 + lane * 16, so);
      if ((w & 3) == 0) { const float Ls = sum8_16_32(ml.y * f); if (lane == 0) stx2(rsW, v2u{min(asu(M), SENT - 1), min(asu(Ls), SENT - 1)}, 1024 + (w >> 2) * 64, so); }
    }
    STAMP(9 + lay * 16);
    auto dma_w1 = [&]() {
      if (w < 6) { const rsrc_t r = mkrs(p.w1, DM * FF * 2);
#pragma unroll
        for (int i = 0; i < 4; ++i) dma(r, (lane >> 2) * (FF * 2) + c64, (64 * n4 + 16 * i) * (FF * 2) + f4i * 384 + w * 64, ldsW + 8192 + i * 1024); }
    };
    auto dma_w2 = [&]() {
      if (w < 6) { const rsrc_t r = mkrs(p.w2, FF * DM * 2);
#pragma unroll
        for (int i = 0; i < 4; ++i)
          dma(r, (lane >> 3) * (DM * 2) + ((((lane & 7) >> 1) ^ (((lane >> 4) & 1) | ((i & 1) << 1))) * 32) + (lane & 1) * 16, (192 * f5 + 32 * w + 8 * i) * (DM * 2) + n5 * 128, ldsW + 12288 + i * 1024); }
    };
    if (lay == 0 && hasF && !hasP3) { dma_w1(); dma_w2(); }
    if (hasP3) {
      const int so = xo + X2_OFF + g * 16 * X2_MSG;
      const int mo = ((lane & 3) * 4 + ((lane >> 2) & 3)) * X2_MSG;
      __builtin_amdgcn_s_sleep(D_X2);
      v4u d0, d1; v2u ml;
      int spin = 0;
      for (;; ++spin) {
        OPAQUE();
        d0 = ldx(rsW, mo + w * 128 + hq * 32, so); d1 = ldx(rsW, mo + w * 128 + hq * 32 + 16, so); ml = ldx2(rsW, mo + 1024 + (w >> 2) * 64, so);
        if (!anybad(max(max(mx4(d0), mx4(d1)), max(ml.x, ml.y))) || spin > SPIN_CAP) break;
      }
      OPAQUE();
      COUNT(42 + lay * 8, spin);
      STAMP(10 + lay * 16);
      if (lay == 0 && hasF) dma_w1();
      const float m = asf(ml.x);
      float M = fmaxf(m, dppf<ROR4>(m)); M = fmaxf(M, dppf<ROR8>(M));
      float f = __builtin_amdgcn_exp2f(m - M);
      float Ls = f * asf(ml.y); Ls += dppf<ROR4>(Ls); Ls += dppf<ROR8>(Ls);
      f *= __builtin_amdgcn_rcpf(Ls);
      float o[8];
#pragma unroll
      for (int k = 0; k < 4; ++k) { o[k] = asf(d0[k]) * f; o[4 + k] = asf(d1[k]) * f; }
#pragma unroll
      for (int k = 0; k < 8; ++k) { o[k] += dppf<ROR4>(o[k]); o[k] += dppf<ROR8>(o[k]); }
      const f4 d = four_tile(1, v4u{pk(o[0], o[1]), pk(o[2], o[3]), pk(o[4], o[5]), pk(o[6], o[7])});
      *(LAS f4*)(L + L_RD + w * 1024 + rdw) = d;
      bar();
      STAMP(11 + lay * 16);
      reduce_pub(8, myX3 + lay * XL_STRIDE);
    }
    if (!hasT) { if (lay == 1) return; continue; }
    {
      const int so = xo + X3_OFF + sl * 16 * 1024, vo = (lane >> 3) * 1024 + w * 128 + (lane & 7) * 16;
      __builtin_amdgcn_s_sleep(D_X3);
      v4u d0, d1;
      int spin = 0;
      for (;; ++spin) {
        OPAQUE();
        d0 = ldx(rsW, vo, so); d1 = ldx(rsW, vo + 8192, so);
        if (!anybad(max(mx4(d0), mx4(d1))) || spin > SPIN_CAP) break;
        if (!hasP3) __builtin_amdgcn_s_sleep(4);
      }
      OPAQUE();
      COUNT(43 + lay * 8, spin);
      STAMP(12 + lay * 16);
      if (lay == 0 && hasF && hasP3) dma_w2();
#pragma unroll
      for (int k = 0; k < 4; ++k) xr[k] = rbf(xr[k] + rbf(sum8_16_32(asf(d0[k]) + asf(d1[k]))));
      track_finish();
    }
    bar();
    STAMP(13 + lay * 16);
    if (hasF) {
      const int sp = myX4 + lay * XL_STRIDE;
      if (w < 6) {
        const f4 d = two_tile(2);
        const int t = (col >> 2) & 1;
        if (col < 8) stx(rsW, clean(d), w * 512 + (4 * t + hq) * 64 + (col & 3) * 16, sp);
        if (lay == 0 && (col & 11) == 8) stx(rsW, clean(d), 3072 + w * 128 + (4 * t + hq) * 16, sp);
      } else if (w == 6) pub_stats(sp + 3840);
      STAMP(14 + lay * 16);
      if (w < 6) {
        const int so = xo + X4_OFF + f5 * 9 * X4_MSG;
        const int gq = (lane >> 3) & 1, hf = (lane >> 2) & 1;
        const int vo = gq * X4_MSG + w * 512 + (2 * hq + hf) * 64 + (lane & 3) * 16;
        const int vo4 = 8 * X4_MSG + w * 512 + (2 * hq + hf) * 64 + (lane & 3) * 16;
        const int pn = lane >> 2, vs = (pn < 9 ? pn : 8) * X4_MSG + 3840 + (lane & 3) * 16;
        const int ve = 3072 + w * 128 + (2 * hq + hf) * 16;
        __builtin_amdgcn_s_sleep(D_X4);
        v4u d[5], e0 = {0, 0, 0, 0}, e1 = {0, 0, 0, 0}; v2u st;
        int spin = 0;
        for (;; ++spin) {
          OPAQUE();
#pragma unroll
          for (int i = 0; i < 4; ++i) d[i] = ldx(rsW, vo + 2 * i * X4_MSG, so);
          d[4] = ldx(rsW, vo4, so);
          st = ldx2(rsW, vs, so);
          if (lay == 0) { e0 = ldx(rsW, (4 * gq + (lane & 3)) * X4_MSG + ve, so); e1 = ldx(rsW, 8 * X4_MSG + ve, so); }
          const unsigned mm = max(max(max(mx4(d[0]), mx4(d[1])), max(mx4(d[2]), mx4(d[3]))), max(max(mx4(d[4]), max(st.x, st.y)), max(mx4(e0), mx4(e1))));
          if (!anybad(mm) || spin > SPIN_CAP) break;
        }
        OPAQUE();
        COUNT(44 + lay * 8, spin);
        STAMP(15 + lay * 16);
        if (lay == 0) {
#pragma unroll
          for (int k = 0; k < 4; ++k) { float a = asf(e0[k]); a += dppf<XOR1>(a); a += dppf<XOR2>(a); a += dppf<ROR8>(a); cs1[k] = a + asf(e1[k]); }
        }
        float s1 = pn < 9 ? asf(st.x) : 0.f, s2 = pn < 9 ? asf(st.y) : 0.f;
        s1 += dppf<ROR4>(s1); s2 += dppf<ROR4>(s2); s1 = sum8_16_32(s1); s2 = sum8_16_32(s2);
        const float mu = s1 * (1.f / DM), rstd = __builtin_amdgcn_rsqf(fmaxf(s2 * (1.f / DM) - mu * mu, 0.f) + 1e-5f);
        const float bb[4] = {blo(b1v.x), bhi(b1v.x), blo(b1v.y), bhi(b1v.y)};
        float h[4];
#pragma unroll
        for (int k = 0; k < 4; ++k) {
          float a = (asf(d[0][k]) + asf(d[1][k])) + (asf(d[2][k]) + asf(d[3][k]));
          a += dppf<ROR8>(a);
          a += asf(d[4][k]);
          const float v = rbf(rbf((a - mu * cs1[k]) * rstd) + bb[k]);
          const float tt = v * (1.f + 0.044715f * v * v) * (-2.f * 0.7978845608028654f * 1.4426950408889634f);
          h[k] = v * __builtin_amdgcn_rcpf(1.f + __builtin_amdgcn_exp2f(tt));
        }
        const unsigned p0 = pk(h[0], h[1]), p1 = pk(h[2], h[3]), o0 = dppu<ROR4>(p0), o1 = dppu<ROR4>(p1);
        const f4 dd = four_tile(3, hf ? v4u{o0, o1, p0, p1} : v4u{p0, p1, o0, o1});
        *(LAS f4*)(L + L_RD + w * 1024 + rdw) = dd;
      }
      bar();
      STAMP(16 + lay * 16);
      reduce_pub(6, myX5 + lay * XL_STRIDE);
    }
    if (lay == 1 && !isOut) return;
    {
      const int so = xo + X5_OFF + sl * 24 * 1024, vo = (lane >> 3) * 1024 + w * 128 + (lane & 7) * 16;
      __builtin_amdgcn_s_sleep(D_X5);
      v4u d0, d1, d2;
      int spin = 0;
      for (;; ++spin) {
        OPAQUE();
        d0 = ldx(rsW, vo, so); d1 = ldx(rsW, vo + 8192, so); d2 = ldx(rsW, vo + 16384, so);
        if (!anybad(max(max(mx4(d0), mx4(d1)), mx4(d2))) || spin > SPIN_CAP) break;
        if (!hasF) __builtin_amdgcn_s_sleep(4);
      }
      OPAQUE();
      COUNT(45 + lay * 8, spin);
      STAMP(17 + lay * 16);
      const float bb[4] = {blo(b2v.x), bhi(b2v.x), blo(b2v.y), bhi(b2v.y)};
#pragma unroll
      for (int k = 0; k < 4; ++k) xr[k] = rbf(xr[k] + rbf(rbf(sum8_16_32((asf(d0[k]) + asf(d1[k])) + asf(d2[k]))) + bb[k]));
      if (lay == 1) {
        if (lane < 8) *(v2u*)(p.out + tb * DM + 64 * sl + 8 * w + 4 * th) = v2u{pk(xr[0], xr[1]), pk(xr[2], xr[3])};
        STAMP(18 + lay * 16);
        return;
      }
      track_finish();
    }
    bar();
    STAMP(18 + lay * 16);
    if (hasP1) do_p1(1);
    STAMP(19 + lay * 16);
  }
}

extern "C" void kernel_launch(void* const* d_in, const int* in_sizes, int n_in, void* d_out, int out_size, void* d_ws, size_t ws_size, hipStream_t stream) {
  Params p;
  p.x = (const u16*)d_in[0]; p.kc = (const u16*)d_in[1]; p.vc = (const u16*)d_in[2]; p.wq = (const u16*)d_in[3]; p.wo = (const u16*)d_in[4];
  p.w1 = (const u16*)d_in[6]; p.b1 = (const u16*)d_in[7]; p.w2 = (const u16*)d_in[8]; p.b2 = (const u16*)d_in[9];
  p.out = (u16*)d_out; p.ws = (unsigned char*)d_ws;
  (void)hipFuncSetAttribute((const void*)fused_fwd, hipFuncAttributeMaxDynamicSharedMemorySize, L_BYTES);
  hipLaunchKernelGGL(fused_fwd, dim3(256), dim3(512), L_BYTES, stream, p);
}
```

```cpp
#include <hip/hip_runtime.h>
#include <stdint.h>

#define STAMPS 0

typedef unsigned short u16;
typedef unsigned long long u64;
typedef unsigned v4u __attribute__((ext_vector_type(4)));
typedef unsigned v8u __attribute__((ext_vector_type(8)));
typedef unsigned v2u __attribute__((ext_vector_type(2)));
typedef float f4 __attribute__((ext_vector_type(4)));
typedef float f2 __attribute__((ext_vector_type(2)));
typedef __bf16 bf2 __attribute__((ext_vector_type(2)));
typedef __bf16 bf8 __attribute__((ext_vector_type(8)));
typedef __bf16 bf16v __attribute__((ext_vector_type(16)));
typedef short s4v __attribute__((ext_vector_type(4)));
#define LAS __attribute__((address_space(3)))
#define DI __device__ __forceinline__
typedef __amdgpu_buffer_rsrc_t rsrc_t;

constexpr int DM = 576, SK = 8192, WIN = 2048, DH = 128, FF = 4608, QN = 4096;
constexpr unsigned SENT = 0xFFFFFFFFu;
constexpr int AUX_NT = 2, AUX_SC1 = 16;

constexpr int WS_EPOCH = 0, WS_FLAGS = 4096, WS_X = 8192;
constexpr int X1_MSG = 5376, X2_MSG = 640, X4_MSG = 3968;
constexpr int X1_OFF = 0, X2_OFF = X1_OFF + 144 * X1_MSG, X3_OFF = X2_OFF + 256 * X2_MSG, X4_OFF = X3_OFF + 144 * 512,
              X5_OFF = X4_OFF + 216 * X4_MSG, XL_BYTES = X5_OFF + 216 * 512;
constexpr int XL_STRIDE = 0x200000;
static_assert(XL_BYTES <= XL_STRIDE, "layer stride");
constexpr int WS_USED = WS_X + 2 * XL_STRIDE;
constexpr int WS_STAMP = 8 << 20;

constexpr int L_WQ = 0;
constexpr int L_WO = 32768;
constexpr int L_W2 = 65536;
constexpr int L_W1 = 90112;
constexpr int L_PA = 114688;
constexpr int L_AO = L_PA + 8 * 2176;
constexpr int L_OB = L_AO + 4096;
constexpr int L_HB = L_OB + 4 * 528;
constexpr int L_XB = L_HB + 4 * 400;
constexpr int L_QP = L_XB + 6 * 144;
constexpr int L_ML = L_QP + 544;
constexpr int L_LN0 = L_ML + 128;
constexpr int L_TAG = L_LN0 + 32;
constexpr int L_BYTES = L_TAG + 16;
constexpr int XBS = 144, OBS = 528, HBS = 400;
static_assert(L_BYTES <= 160 * 1024 && L_OB % 16 == 0 && L_HB % 16 == 0 && L_XB % 16 == 0 && L_QP % 16 == 0 && L_ML % 16 == 0, "lds");

#define D_X1 18
#define D_X2 18
#define D_X3 20
#define D_X4 12
#define D_X5 20
#define XPOS 1
#define WND 4
#define WND0 6
#define KPRE 2
#define PREFI 0
#define WMODE 0
constexpr int SPIN_CAP = 1 << 18;

struct Params {
  const u16 *x, *kc, *vc, *wq, *wo, *w1, *b1, *w2, *b2;
  u16* out;
  unsigned char* ws;
};

DI f4 mfma(v4u a, v4u b, f4 c) { return __builtin_amdgcn_mfma_f32_16x16x32_bf16(__builtin_bit_cast(bf8, a), __builtin_bit_cast(bf8, b), c, 0, 0, 0); }
DI f4 smf(v4u a, v8u b, f4 c, int idx) { return __builtin_amdgcn_smfmac_f32_16x16x64_bf16(__builtin_bit_cast(bf8, a), __builtin_bit_cast(bf16v, b), c, idx, 0, 0); }
DI v8u cat(v4u a, v4u b) { return __builtin_shufflevector(a, b, 0, 1, 2, 3, 4, 5, 6, 7); }
DI float asf(unsigned u) { return __builtin_bit_cast(float, u); }
DI unsigned asu(float f) { return __builtin_bit_cast(unsigned, f); }
template <int C> DI unsigned dppu(unsigned x) { return (unsigned)__builtin_amdgcn_mov_dpp((int)x, C, 0xf, 0xf, true); }
template <int C> DI float dppf(float x) { return asf(dppu<C>(asu(x))); }
constexpr int XOR1 = 0xB1, XOR2 = 0x4E, ROR4 = 0x124, ROR8 = 0x128;
DI float add16(float x) { auto s = __builtin_amdgcn_permlane16_swap(asu(x), asu(x), false, false); return asf(s[0]) + asf(s[1]); }
DI float add32(float x) { auto s = __builtin_amdgcn_permlane32_swap(asu(x), asu(x), false, false); return asf(s[0]) + asf(s[1]); }
DI float vmax(float a, float b) { float r; asm("v_max_f32 %0, %1, %2" : "=v"(r) : "v"(a), "v"(b)); return r; }
DI float max16(float x) { auto s = __builtin_amdgcn_permlane16_swap(asu(x), asu(x), false, false); return vmax(asf(s[0]), asf(s[1])); }
DI float max32(float x) { auto s = __builtin_amdgcn_permlane32_swap(asu(x), asu(x), false, false); return vmax(asf(s[0]), asf(s[1])); }
DI unsigned pk(float lo, float hi) { bf2 v = {(__bf16)lo, (__bf16)hi}; return __builtin_bit_cast(unsigned, v); }
DI float rbf(float x) { return (float)(__bf16)x; }
DI float blo(unsigned u) { return asf(u << 16); }
DI float bhi(unsigned u) { return asf(u & 0xffff0000u); }
DI unsigned cl(unsigned u) { return min(u, SENT - 1); }
DI v4u split4(f4 d) {
  const unsigned h0 = pk(d.x, d.y), h1 = pk(d.z, d.w);
  return v4u{cl(h0), cl(h1), cl(pk(d.x - blo(h0), d.y - bhi(h0))), cl(pk(d.z - blo(h1), d.w - bhi(h1)))};
}
DI v2u pack4(f4 d) { return v2u{cl(pk(d.x, d.y)), cl(pk(d.z, d.w))}; }
DI unsigned mx4(v4u v) { return max(max(v.x, v.y), max(v.z, v.w)); }
DI v4u ldx(rsrc_t r, int voff, int soff) { return __builtin_amdgcn_raw_buffer_load_b128(r, voff, soff, AUX_SC1); }
DI v2u ldx2(rsrc_t r, int voff, int soff) { return __builtin_amdgcn_raw_buffer_load_b64(r, voff, soff, AUX_SC1); }
DI void stx(rsrc_t r, v4u v, int voff, int soff) { __builtin_amdgcn_raw_buffer_store_b128(v, r, voff, soff, AUX_SC1); }
DI void stx2(rsrc_t r, v2u v, int voff, int soff) { __builtin_amdgcn_raw_buffer_store_b64(v, r, voff, soff, AUX_SC1); }
DI void bar() { asm volatile("s_waitcnt lgkmcnt(0)\n\ts_barrier" ::: "memory"); }
DI void drain() { asm volatile("s_waitcnt vmcnt(0)" ::: "memory"); }
DI rsrc_t mkrs(const void* p, unsigned bytes) { return __builtin_amdgcn_make_buffer_rsrc((void*)p, (short)0, (int)bytes, 0x00020000); }
DI void dma(rsrc_t r, int voff, int soff, unsigned ldsaddr) {
  unsigned keep;
  asm volatile("s_nop 4\n\ts_mov_b32 %0, m0\n\ts_mov_b32 m0, %1\n\ts_nop 0\n\tbuffer_load_dwordx4 %2, %3, %4 offen nt lds\n\ts_mov_b32 m0, %0"
               : "=&s"(keep) : "s"(ldsaddr), "v"(voff), "s"(r), "s"(soff) : "memory");
}
typedef LAS unsigned char* lptr;
DI v4u lds16(lptr L, int off) { return *(LAS v4u*)(L + off); }
#define SB() __builtin_amdgcn_sched_barrier(0)
DI float dot2(unsigned a, unsigned b, float c) { return __builtin_amdgcn_fdot2_f32_bf16(__builtin_bit_cast(bf2, a), __builtin_bit_cast(bf2, b), c, false); }
DI v4u trfrag(LAS unsigned char* L, int off, int step) {
  s4v a = __builtin_amdgcn_ds_read_tr16_b64_v4i16((LAS s4v*)(L + off));
  s4v b = __builtin_amdgcn_ds_read_tr16_b64_v4i16((LAS s4v*)(L + off + step));
  v2u x = __builtin_bit_cast(v2u, a), y = __builtin_bit_cast(v2u, b);
  return v4u{x.x, x.y, y.x, y.y};
}
DI bool any(bool b) { return __builtin_amdgcn_ballot_w64(b) != 0; }
#define OPAQUE() asm volatile("" ::: "memory")
#define Z4 f4{0.f, 0.f, 0.f, 0.f}
#define FRESH() int lane = lane0; asm volatile("" : "+v"(lane)); const int col = lane & 15, hq = lane >> 4, q4 = col >> 2, pp = lane & 3; (void)col; (void)hq; (void)q4; (void)pp
#define SEL8() const unsigned one = (lane & 1) ? 0x3F800000u : 0x00003F80u; \
  const v4u sel8 = {((col & 7) >> 1) == 0 ? one : 0u, ((col & 7) >> 1) == 1 ? one : 0u, ((col & 7) >> 1) == 2 ? one : 0u, ((col & 7) >> 1) == 3 ? one : 0u}
#define A128() const int z128 = ((q4 >> 1) & 1) | ((hq & 1) << 1), a128 = hq * 1024 + q4 * 128 + pp * 8
#define STAMP(i) do {} while (0)
#define STAMPW(ww, i) do {} while (0)
#define COUNT(i, v) do {} while (0)

#define PIN4(a) asm volatile("" : "+v"(a[0]), "+v"(a[1]), "+v"(a[2]), "+v"(a[3]))
#define PINI(x) asm volatile("" : "+v"(x))
#define TKO (32 * (w & 1) + 8 * q4 + 4 * (hq & 1))
#define HFO (32 * w + 16 * (hq >> 1) + 4 * q4 + 2 * (hq & 1))
#define KLOAD(i) K[i] = __builtin_amdgcn_raw_buffer_load_b128(rsK, kvo, (((i) + rot) & 15) * 1024, AUX_NT)
#define VLOAD(i) V[i] = __builtin_amdgcn_raw_buffer_load_b128(rsV, vvo, (((i) + rot) & 15) * 1024, AUX_NT)

__global__ void __launch_bounds__(512, 2) fused_fwd(Params p) {
  extern __shared__ __attribute__((aligned(16))) unsigned char smem[];
  const lptr L = (lptr)smem;
  const int tid = threadIdx.x, lane0 = tid & 63, lane = lane0, w = __builtin_amdgcn_readfirstlane(tid >> 6);
  const int c = blockIdx.x, xc = c & 7, j = c >> 3, grp = j >> 4, u = j & 15, g = 2 * xc + grp;
  const int ab = u >> 2, as = u & 3;
  const int col = lane & 15, hq = lane >> 4, q4 = col >> 2, pp = lane & 3;
  const size_t kvbase = ((size_t)(ab * 16 + g) * SK + (SK - WIN) + as * 512 + w * 64) * DH;
  const rsrc_t rsK = mkrs(p.kc + kvbase, 16384), rsV = mkrs(p.vc + kvbase, 16384);
  const int rot = (w >> 1) + 4 * (j & 3) + 2 * xc;
  const int kvo = q4 * 256 + (4 * hq + pp) * 16, vvo = lane * 16;
  v4u K[16], V[16];
#pragma unroll
  for (int i = 0; i < KPRE; ++i) KLOAD(i);
  OPAQUE(); SB();
  STAMPW(0, 0);
  if (w == 0) COUNT(60, __builtin_amdgcn_s_memtime());
  const rsrc_t rsW = mkrs(p.ws, WS_USED);
  v2u ep = {0, 0}; v4u xv[5], pf = {0, 0, 0, 0};
  int wa = w, wb = w, wc = w;
  asm volatile("" : "+s"(wa), "+s"(wb), "+s"(wc), "+v"(ep), "+v"(pf));
  if (wa == 7) ep = ldx2(rsW, 0, WS_EPOCH);
  OPAQUE();
  if (wb == 6) {
    const u16* xq = p.x + hq * DM + col * 8;
#pragma unroll
    for (int k = 0; k < 4; ++k) xv[k] = *(const v4u*)(xq + 128 * k);
    xv[4] = *(const v4u*)(xq + 512 - (col & 8) * 8);
  }
  OPAQUE();
  if (wc == 5 && PREFI) {
    u64 pc; unsigned len;
    asm volatile("s_getpc_b64 %0\n.Lfwd_pc:\n\ts_mov_b32 %1, .Lfwd_end-.Lfwd_pc" : "=s"(pc), "=s"(len));
    const unsigned off = j * 1024 + lane * 16;
    if (off + 16 <= len) pf = *(const __attribute__((address_space(1))) v4u*)((pc & ~15ull) + off);
  }
  OPAQUE(); SB();
  const bool hasP1 = u <= 8, hasP3 = u >= 7;
  const int r1 = u < 8 ? xc : 8, gq1 = u < 8 ? 8 * grp + u : g, n3 = hasP3 ? u - 7 : 0;
  const unsigned ldsb = (unsigned)(size_t)L;
  auto dma128 = [&](int ln, rsrc_t r, int rowbytes, int soff, unsigned lds, int i) {
    dma(r, (ln >> 3) * rowbytes + ((((ln & 7) >> 1) ^ (((ln >> 4) & 1) | ((i & 1) << 1))) * 32) + (ln & 1) * 16, soff + 8 * i * rowbytes, lds + i * 1024);
  };
  if (hasP1) {
    const rsrc_t rq = mkrs(p.wq, DM * QN * 2);
#pragma unroll
    for (int i = 0; i < 4; ++i) dma128(lane, rq, QN * 2, (64 * r1 + 32 * (w & 1)) * (QN * 2) + gq1 * 512 + (w >> 1) * 128, ldsb + L_WQ + w * 4096, i);
  }
  OPAQUE(); SB();
  const int fi0 = 14 * grp + (u <= 6 ? u : u - 2);
  const bool hasF = (u != 7) && (u != 8) && (fi0 < 27);
  const int fi = fi0 < 27 ? fi0 : 0;
  const int n4 = fi < 24 ? xc : 8, f4i = fi < 24 ? fi : 3 * xc + fi - 24, f5 = 3 * xc + fi / 9, n5 = fi % 9;
  const int sl = u == 8 ? 8 : (hasF ? n4 : xc);
  const bool isOut = (j == 0) || (c == 64);
  const int myX1 = WS_X + X1_OFF + (gq1 * 9 + r1) * X1_MSG, myX2 = WS_X + X2_OFF + (g * 16 + u) * X2_MSG, myX3 = WS_X + X3_OFF + (n3 * 16 + g) * 512,
            myX4 = WS_X + X4_OFF + (f4i * 9 + n4) * X4_MSG, myX5 = WS_X + X5_OFF + (n5 * 24 + f5) * 512;
  constexpr float CS = 0.08838834764831845f * 1.4426950408889634f;
  {
    const v4u SENT4 = {SENT, SENT, SENT, SENT};
    const int lo = w & 1, ql = w >> 1;
    const int ca0 = (ql < 2 ? myX4 + ql * XL_STRIDE : myX1 + XL_STRIDE) + (ql == 3 ? 5120 : lo * 2048);
    const int on = (ql < 2 ? hasF : hasP1) ? 64 : 0;
    const int cn0 = ql == 3 ? (lo ? on >> 3 : 0) : on, cn1 = ql == 3 ? 0 : ((ql < 2 && lo && on) ? 56 : on);
    const int ca2 = (ql == 0 ? myX5 : (ql == 1 ? myX2 : myX3)) + lo * XL_STRIDE;
    const int cn2 = ql == 0 ? (hasF ? 32 : 0) : (ql == 1 ? 40 : ((ql == 2 && hasP3) ? 32 : 0));
    if (lane < cn0) stx(rsW, SENT4, lane * 16, ca0);
    if (lane < cn1) stx(rsW, SENT4, lane * 16, ca0 + 1024);
    if (lane < cn2) stx(rsW, SENT4, lane * 16, ca2);
  }
  OPAQUE();
#pragma unroll
  for (int i = KPRE; i < WND0; ++i) KLOAD(i);
  OPAQUE();
  const u64 tiss = STAMPS ? __builtin_amdgcn_s_memrealtime() : 0;
  {
    const v4u z = {0, 0, 0, 0};
    const lptr pa = L + L_PA + w * 2176 + lane * 16;
    *(LAS v4u*)pa = z; *(LAS v4u*)(pa + 1024) = z; if (lane < 8) *(LAS v4u*)(pa + 2048) = z;
  }
  if (w == 6) {
    const int ks = r1 >> 1;
    const v4u xs = ks == 0 ? xv[0] : (ks == 1 ? xv[1] : (ks == 2 ? xv[2] : (ks == 3 ? xv[3] : xv[4])));
    if ((col >> 3) == (r1 & 1)) *(LAS v4u*)(L + L_XB + hq * XBS + (col & 7) * 16) = xs;
    if (col >= 8) xv[4] = v4u{0, 0, 0, 0};
    float s1 = 0.f, s2 = 0.f;
#pragma unroll
    for (int k = 0; k < 5; ++k)
#pragma unroll
      for (int d = 0; d < 4; ++d) { s1 = dot2(xv[k][d], 0x3F803F80u, s1); s2 = dot2(xv[k][d], xv[k][d], s2); }
    s1 += dppf<XOR1>(s1); s2 += dppf<XOR1>(s2); s1 += dppf<XOR2>(s1); s2 += dppf<XOR2>(s2);
    s1 += dppf<ROR4>(s1); s2 += dppf<ROR4>(s2); s1 += dppf<ROR8>(s1); s2 += dppf<ROR8>(s2);
    const float mu = s1 * (1.f / DM), rs = __builtin_amdgcn_rsqf(fmaxf(s2 * (1.f / DM) - mu * mu, 0.f) + 1e-5f) * CS;
    if (col == 0) *(LAS f2*)(L + L_LN0 + hq * 8) = f2{mu, rs};
  } else if (w == 7) {
    if (lane < 36) { *(LAS unsigned*)(L + L_XB + 4 * XBS + lane * 4) = 0x3F803F80u; *(LAS unsigned*)(L + L_XB + 5 * XBS + lane * 4) = 0u; }
    if (lane < 2) *(LAS v4u*)(L + L_QP + 256 + lane * 272) = v4u{0, 0, 0, 0};
    asm volatile("" : "+v"(ep));
    const u64 t = ((((u64)ep.y << 32) | ep.x) * 6364136223846793005ull + 1442695040888963407ull);
    if (lane == 0) *(LAS v2u*)(L + L_TAG) = v2u{(unsigned)t, (unsigned)(t >> 32)};
  } else if (w == 5) asm volatile("" ::"v"(pf));
  asm volatile("s_waitcnt vmcnt(%0)" ::"i"(WND0 - KPRE) : "memory");
  const u64 tw8 = STAMPS ? __builtin_amdgcn_s_memrealtime() : 0;
  bar();
  if (w == 0) { COUNT(35, tiss); COUNT(38, tw8); }
  if (w == 7) { COUNT(39, tw8); }
  const v2u tgw = *(LAS v2u*)(L + L_TAG);
  const u64 tag = ((u64)tgw.y << 32) | tgw.x;
  const unsigned tgx = tgw.x & 0xBFFFBFFFu, tgy = tgw.y & 0xBFFFBFFFu;
  if (w == 7 && lane == 0) { const u64 f = tag + (u64)c * 0x9E3779B97F4A7C15ull; stx2(rsW, v2u{(unsigned)f, (unsigned)(f >> 32)}, 0, WS_FLAGS + c * 8); }
  STAMPW(7, 1);
  if (hasP1) {
    A128();
    const int base = L_WQ + (w >> 1) * 8192 + a128, t0 = 2 * (w & 1);
    const v4u A0 = trfrag(L, base + (t0 ^ z128) * 32, 512), A1 = trfrag(L, base + (t0 ^ z128) * 32 + 4096, 512);
    const v4u A2 = trfrag(L, base + ((t0 + 1) ^ z128) * 32, 512), A3 = trfrag(L, base + ((t0 + 1) ^ z128) * 32 + 4096, 512);
    const v4u xf0 = lds16(L, L_XB + pp * XBS + hq * 16), xf1 = lds16(L, L_XB + pp * XBS + hq * 16 + 64);
    const f2 ln = *(LAS f2*)(L + L_LN0 + pp * 8);
    const unsigned m0 = q4 == 0 ? ~0u : 0u, m1 = q4 == 1 ? ~0u : 0u, o0 = m0 & 0x3F803F80u, o1 = m1 & 0x3F803F80u;
    f4 d = mfma(A0, xf0 & m0, Z4); d = mfma(A1, xf1 & m0, d); d = mfma(A2, xf0 & m1, d); d = mfma(A3, xf1 & m1, d);
    f4 e = mfma(A0, v4u{o0, o0, o0, o0}, Z4); e = mfma(A1, v4u{o0, o0, o0, o0}, e); e = mfma(A2, v4u{o1, o1, o1, o1}, e); e = mfma(A3, v4u{o1, o1, o1, o1}, e);
    d = (d - ln.x * e) * ln.y;
    const int ch = (8 * w + 4 * q4 + hq) * 16;
    if (col < 8) stx(rsW, v4u{pk(d.x, d.y), tgx, pk(d.z, d.w), tgy}, pp * 1024 + ch, myX1);
    if ((col & 11) == 0) stx(rsW, split4(e), 4096 + ch, myX1);
    STAMPW(0, 2);
  }
#define WIN() asm volatile("s_waitcnt vmcnt(%0)" ::"i"(WND - 1) : "memory")
#pragma unroll
  for (int i = WND0; i < 16; ++i) { WIN(); KLOAD(i); }
#pragma unroll
  for (int i = 0; i < XPOS; ++i) { WIN(); VLOAD(i); }
  const int x1so = WS_X + X1_OFF + g * 9 * X1_MSG, x1v = hq * X1_MSG + (16 * (w & 3) + col) * 16 + ab * 1024;
  v4u g0 = {0, 0, 0, 0}, g1 = g0, g2 = g0, fa = g0, fb = g0;
  WIN();
  if (w >= 4) { g0 = ldx(rsW, x1v, x1so); g1 = ldx(rsW, x1v + 4 * X1_MSG, x1so); g2 = ldx(rsW, x1v + (8 - hq) * X1_MSG, x1so); }
  if (w == 3) { fa = ldx(rsW, lane * 16, WS_FLAGS); fb = ldx(rsW, lane * 16, WS_FLAGS + 1024); }
  v8u K8[8], V8[8];
  auto transp = [&](int T) {
    const bool o1 = lane & 1, o2 = lane & 2;
#pragma unroll
    for (int d = 0; d < 4; ++d) {
      const unsigned m0 = K[4 * T][d], m1 = K[4 * T + 1][d], m2 = K[4 * T + 2][d], m3 = K[4 * T + 3][d];
      const unsigned s0 = dppu<XOR1>(m0), s1 = dppu<XOR1>(m1), s2 = dppu<XOR1>(m2), s3 = dppu<XOR1>(m3);
      const unsigned n0 = o1 ? s1 : m0, n1 = o1 ? m1 : s0, n2 = o1 ? s3 : m2, n3_ = o1 ? m3 : s2;
      const unsigned t0 = dppu<XOR2>(n0), t1 = dppu<XOR2>(n1), t2 = dppu<XOR2>(n2), t3 = dppu<XOR2>(n3_);
      K8[2 * T][d] = o2 ? t2 : n0; K8[2 * T + 1][d] = o2 ? n2 : t0;
      K8[2 * T][4 + d] = o2 ? t3 : n1; K8[2 * T + 1][4 + d] = o2 ? n3_ : t1;
    }
    asm volatile("" : "+v"(K8[2 * T]), "+v"(K8[2 * T + 1]));
  };
#define QPW(ww) (L_QP + (q4 & 1) * 272 + (((ww) >> 1) * 8 + 2 * ((col >> 1) & 1) + ((ww) & 1)) * 16 + (2 * (col >> 3) + (col & 1)) * 4)
#pragma unroll
  for (int i = XPOS; i < 16; ++i) {
    WIN(); VLOAD(i);
    OPAQUE();
    SB();
    if (i >= XPOS + 1 && i < XPOS + 5) transp(i - XPOS - 1);
    SB();
    if (i == XPOS + 6) {
      if (w >= 4) {
        const unsigned one = (lane & 1) ? 0x3F800000u : 0x00003F80u;
        const v4u selT = {(col & 2) ? 0u : one, 0u, (col & 2) ? one : 0u, 0u}, selT0 = hq == 0 ? selT : v4u{0, 0, 0, 0};
        int spin = 0;
        asm volatile("" : "+v"(g0), "+v"(g1), "+v"(g2));
        for (; any(g0.y != tgx || g0.w != tgy || g1.y != tgx || g1.w != tgy || g2.y != tgx || g2.w != tgy) && spin < SPIN_CAP; ++spin) {
          OPAQUE();
          g0 = ldx(rsW, x1v, x1so); g1 = ldx(rsW, x1v + 4 * X1_MSG, x1so); g2 = ldx(rsW, x1v + (8 - hq) * X1_MSG, x1so);
          asm volatile("" : "+v"(g0), "+v"(g1), "+v"(g2));
        }
        f4 q = mfma(selT, g0, Z4); q = mfma(selT, g1, q); q = mfma(selT0, g2, q);
        if (hq == 0) { LAS unsigned* qp = (LAS unsigned*)(L + QPW(w & 3)); qp[0] = pk(q.x, q.y); qp[16] = pk(q.z, q.w); }
        if (w == 4) COUNT(41, spin);
        STAMPW(4, 6);
      } else if (w == 3) {
        u64 e[4];
#pragma unroll
        for (int k = 0; k < 4; ++k) e[k] = tag + (u64)(lane * 2 + (k & 1) + (k >> 1) * 128) * 0x9E3779B97F4A7C15ull;
        int spin = 0;
        asm volatile("" : "+v"(fa), "+v"(fb));
        for (;; ++spin) {
          const bool ok = fa.x == (unsigned)e[0] && fa.y == (unsigned)(e[0] >> 32) && fa.z == (unsigned)e[1] && fa.w == (unsigned)(e[1] >> 32) &&
                          fb.x == (unsigned)e[2] && fb.y == (unsigned)(e[2] >> 32) && fb.z == (unsigned)e[3] && fb.w == (unsigned)(e[3] >> 32);
          if (!any(!ok) || spin > SPIN_CAP) break;
          OPAQUE();
          fa = ldx(rsW, lane * 16, WS_FLAGS); fb = ldx(rsW, lane * 16, WS_FLAGS + 1024);
          asm volatile("" : "+v"(fa), "+v"(fb));
        }
        COUNT(40, spin);
        if (c == 0 && lane == 0) stx2(rsW, v2u{(unsigned)tag, (unsigned)(tag >> 32)}, 0, WS_EPOCH);
        STAMP(4);
      }
      OPAQUE();
    }
  }
#pragma unroll
  for (int m = 0; m < 8; ++m) V8[m] = cat(V[2 * m], V[2 * m + 1]);
  STAMPW(0, 3);

  v2u xp = {0, 0}, b2v = {0, 0}; unsigned b1v = 0;
  f4 csq = Z4; f2 cs1 = {0.f, 0.f};
  auto unp = [](v2u v) { return f4{blo(v.x), bhi(v.x), blo(v.y), bhi(v.y)}; };
  auto pk4 = [](f4 v) { return v2u{pk(v.x, v.y), pk(v.z, v.w)}; };
  auto dma_w1 = [&](int ln) {
    const rsrc_t r = mkrs(p.w1, DM * FF * 2);
#pragma unroll
    for (int i = 0; i < 4; ++i) dma(r, (ln >> 2) * (FF * 2) + ((ln & 3) ^ (2 * (ln >> 5))) * 16, (64 * n4 + 16 * i) * (FF * 2) + f4i * 384 + w * 64, ldsb + L_W1 + w * 4096 + i * 1024);
  };
  auto dma_w2 = [&](int ln) {
    const rsrc_t r = mkrs(p.w2, FF * DM * 2);
#pragma unroll
    for (int i = 0; i < 4; ++i) dma128(ln, r, DM * 2, (192 * f5 + 32 * w) * (DM * 2) + n5 * 128, ldsb + L_W2 + w * 4096, i);
  };
  auto dma_wo = [&](int ln) {
    const rsrc_t ro = mkrs(p.wo, QN * DM * 2);
#pragma unroll
    for (int i = 0; i < 4; ++i) dma128(ln, ro, DM * 2, (256 * g + 32 * w) * (DM * 2) + n3 * 128, ldsb + L_WO + w * 4096, i);
  };

  for (int lay = 0; lay < 2; ++lay) {
    const int xo = WS_X + lay * XL_STRIDE;
    lptr qa[4], pw, pr, aow, mlw, mbr, mmr; int qidx, pidx, mst; v4u pm; f4 S = Z4, O = Z4;
    {
      FRESH();
#pragma unroll
      for (int T = 0; T < 4; ++T) qa[T] = L + (q4 == T ? L_QP + (pp * 4 + hq) * 16 : L_QP + 256);
      qidx = (lane & 1) ? 0xEEEE : 0x4444; pidx = pp * 0x1111;
      pw = L + L_PA + w * 2176 + (2 * hq + (pp >> 1)) * 272 + (2 * (pp & 1) + (q4 >> 1)) * 64 + (q4 & 1) * 8;
      pr = L + L_PA + w * 2176 + (hq * 4 + (col >> 3) * 2 + (q4 & 1)) * 16;
      aow = L + L_AO + w * 512 + ((lane >> 5) * 128 + 8 * col + 4 * (hq & 1)) * 2;
      mlw = L + L_ML + (w * 2 + (lane >> 5)) * 8;
      mbr = L + L_AO + (w & 1) * 256 + hq * 512 + col * 16; mmr = L + L_ML + (w & 1) * 8 + hq * 16;
      mst = (w & 1) * 256 + col * 16 + hq * 8;
      const unsigned hm = (lane & 1) ? 0xFFFF0000u : 0xFFFFu;
#pragma unroll
      for (int k = 0; k < 4; ++k) pm[k] = ((col & 7) >> 1) == k ? hm : 0u;
      PIN4(qa); PINI(pw); PINI(pr); PINI(aow); PINI(mlw); PINI(mbr); PINI(mmr); PINI(qidx); PINI(pidx); PINI(mst); PINI(pm); PINI(S); PINI(O);
    }
    if (lay == 1 && w >= 4) {
      FRESH();
      const unsigned one = (lane & 1) ? 0x3F800000u : 0x00003F80u;
      v4u sel4 = {(col & 2) ? 0u : one, (col & 2) ? one : 0u, (col & 2) ? 0u : one, (col & 2) ? one : 0u};
      v4u sel40 = hq == 0 ? sel4 : v4u{0, 0, 0, 0};
      const int so = xo + X1_OFF + g * 9 * X1_MSG;
      int v0 = hq * X1_MSG + (16 * (w - 4) + col) * 16 + ab * 1024, v1 = v0 + 4 * X1_MSG, v2 = v0 + (8 - hq) * X1_MSG, vs = (col < 9 ? col : 8) * X1_MSG + 5120 + ab * 8;
      float sm = col < 9 ? (1.f / DM) : 0.f;
      lptr qw = L + QPW(w - 4);
      PINI(sel4); PINI(sel40); PINI(v0); PINI(v1); PINI(v2); PINI(vs); PINI(qw); PINI(sm);
      __builtin_amdgcn_s_sleep(D_X1);
      f4 d; float rs;
      int spin = 0;
      for (;; ++spin) {
        OPAQUE();
        const v2u st = ldx2(rsW, vs, so);
        const v4u d0 = ldx(rsW, v0, so), d1 = ldx(rsW, v1, so), d2 = ldx(rsW, v2, so);
        SB();
        float s1 = asf(st.x) * sm, s2 = asf(st.y) * sm;
        s1 += dppf<XOR1>(s1); s2 += dppf<XOR1>(s2); s1 += dppf<XOR2>(s1); s2 += dppf<XOR2>(s2);
        s1 += dppf<ROR4>(s1); s2 += dppf<ROR4>(s2); s1 += dppf<ROR8>(s1); s2 += dppf<ROR8>(s2);
        rs = __builtin_amdgcn_rsqf(fmaxf(s2 - s1 * s1, 0.f) + 1e-5f) * CS;
        d = -s1 * csq;
        SB();
        d = mfma(sel4, d0, d); d = mfma(sel4, d1, d); d = mfma(sel40, d2, d);
        if (!any(d.x != d.x)) break;
        if (!any(max(max(max(mx4(d0), mx4(d1)), mx4(d2)), max(st.x, st.y)) == SENT) || spin > SPIN_CAP) break;
      }
      OPAQUE();
      if (w == 4) COUNT(49, spin);
      STAMPW(4, 22);
      const f4 q = d * rs;
      if (lane0 < 16) { LAS unsigned* qp = (LAS unsigned*)qw; qp[0] = pk(q.x, q.y); qp[16] = pk(q.z, q.w); }
    }
    bar();
    STAMPW(0, 7 + lay * 16);
    v4u AF[8];
    {
#pragma unroll
      for (int T = 0; T < 4; ++T) { AF[2 * T] = lds16(qa[T], 0); AF[2 * T + 1] = lds16(qa[T], 272); }
      SB();
#pragma unroll
      for (int m = 0; m < 8; ++m) S = smf(AF[m], K8[m], S, qidx);
      const float s0 = S.x + S.y, s1 = S.z + S.w;
      auto sw = __builtin_amdgcn_permlane32_swap(asu(s0), asu(s1), false, false);
      float mx;
      asm("v_max_f32 %0, %1, %2\n\ts_nop 1\n\tv_max_f32_dpp %0, %0, %0 quad_perm:[1,0,3,2] row_mask:0xf bank_mask:0xf\n\ts_nop 1\n\t"
          "v_max_f32_dpp %0, %0, %0 quad_perm:[2,3,0,1] row_mask:0xf bank_mask:0xf\n\ts_nop 1\n\tv_max_f32_dpp %0, %0, %0 row_ror:4 row_mask:0xf bank_mask:0xf\n\ts_nop 1\n\t"
          "v_max_f32_dpp %0, %0, %0 row_ror:8 row_mask:0xf bank_mask:0xf\n\ts_nop 1\n\tv_max_f32_dpp %0, %0, %0 row_bcast:15 row_mask:0xa bank_mask:0xf\n\ts_nop 0"
          : "=&v"(mx) : "v"(asf(sw[0])), "v"(asf(sw[1])));
      const float m0 = asf(__builtin_amdgcn_readlane(asu(mx), 31)), m1 = asf(__builtin_amdgcn_readlane(asu(mx), 63));
      const float p0 = __builtin_amdgcn_exp2f(s0 - m0), p1 = __builtin_amdgcn_exp2f(s1 - m1);
      const unsigned pb = pk(p0, p1);
      *(LAS u16*)(pw) = (u16)pb; *(LAS u16*)(pw + 20) = (u16)pb; *(LAS u16*)(pw + 32) = (u16)(pb >> 16); *(LAS u16*)(pw + 52) = (u16)(pb >> 16);
#pragma unroll
      for (int m = 0; m < 8; ++m) AF[m] = lds16(pr, m * 272);
      SB();
      auto sl2 = __builtin_amdgcn_permlane32_swap(asu(p0), asu(p1), false, false);
      float ls = asf(sl2[0]) + asf(sl2[1]);
      ls += dppf<XOR1>(ls); ls += dppf<XOR2>(ls); ls += dppf<ROR4>(ls); ls += dppf<ROR8>(ls);
      ls += asf((unsigned)__builtin_amdgcn_update_dpp(0, (int)asu(ls), 0x142, 0xa, 0xf, false));
      if ((lane0 & 31) == 31) *(LAS f2*)mlw = f2{mx, ls};
      SB();
#pragma unroll
      for (int m = 0; m < 8; ++m) O = smf(AF[m], V8[m], O, pidx);
      if (lay == 0) {
        if (WMODE == 0 && hasP3) { FRESH(); dma_wo(lane); }
      }
      *(LAS v2u*)aow = pk4(O);
    }
    bar();
    STAMPW(0, 8 + lay * 16);
    if (w >= 6) {
      const f2 ml0 = *(LAS f2*)mmr, ml1 = *(LAS f2*)(mmr + 64);
      const v4u B0 = lds16(mbr, 0), B1 = lds16(mbr, 2048);
      SB();
      const float M = max32(max16(vmax(ml0.x, ml1.x)));
      const float f0 = __builtin_amdgcn_exp2f(ml0.x - M), f1 = __builtin_amdgcn_exp2f(ml1.x - M);
      const unsigned h0 = pk(f0, f0), h1 = pk(f1, f1);
      f4 o = mfma(pm & h0, B0, Z4); o = mfma(pm & h1, B1, o);
      const float Ls = add32(add16(blo(h0) * ml0.y + blo(h1) * ml1.y));
      const int so = myX2 + lay * XL_STRIDE;
      if (lane0 == 0) stx2(rsW, v2u{cl(asu(M)), cl(asu(Ls))}, 512 + (w & 1) * 8, so);
      if (lane0 < 32) stx2(rsW, pack4(o), mst, so);
      STAMPW(6, 9 + lay * 16);
    }
    if (lay == 0 && w >= 4) {
      FRESH();
      if (w >= 6) { xp = *(const v2u*)(p.x + pp * DM + 64 * sl + TKO); b2v = *(const v2u*)(p.b2 + 64 * sl + TKO); }
      if (w < 6) __builtin_amdgcn_s_sleep(D_X2 + 2);
      if (WMODE == 1 && hasP3) dma_wo(lane);
      if (hasF && w < 6) { dma_w1(lane); if (!hasP3) dma_w2(lane); }
    }
    lptr p3a = L, p3b = L; int ymsg = 0;
    if (w < 4) {
      FRESH(); A128();
      const int so = xo + X2_OFF + (g * 16 + w * 4) * X2_MSG;
      int vm = hq * X2_MSG + 512 + (col >> 3) * 8, vd = hq * X2_MSG + col * 16;
      p3a = L + L_WO + a128 + (w ^ z128) * 32; p3b = L + L_OB + pp * OBS + hq * 16; ymsg = (4 * (2 * w + (hq >> 1)) + pp) * 16 + (hq & 1) * 8;
      PINI(vm); PINI(vd); PINI(p3a); PINI(p3b); PINI(ymsg);
      int spin = 0;
      if (hasP3) {
        v4u pm0 = col < 8 ? pm : v4u{0, 0, 0, 0}, pm1 = col < 8 ? v4u{0, 0, 0, 0} : pm;
        lptr ow = L + L_OB + w * OBS + ((hq >> 1) * 128 + 8 * col + 4 * (hq & 1)) * 2;
        PINI(pm0); PINI(pm1); PINI(ow);
        if (lay == 1) {
#pragma unroll
          for (int k = 0; k < 8; ++k) AF[k] = trfrag(p3a, k * 4096, 512);
        }
        __builtin_amdgcn_s_sleep(D_X2);
        f4 o;
        for (;; ++spin) {
          OPAQUE();
          const v2u ml = ldx2(rsW, vm, so);
          const v4u d0 = ldx(rsW, vd, so), d1 = ldx(rsW, vd + 256, so);
          SB();
          const float m = asf(ml.x), M = max32(max16(m));
          const float f = __builtin_amdgcn_exp2f(m - M);
          const float Ls = add32(add16(f * asf(ml.y)));
          const float fs = f * __builtin_amdgcn_rcpf(Ls);
          const unsigned gg = pk(fs, fs);
          o = mfma(pm0 & gg, d0, Z4); o = mfma(pm1 & gg, d1, o);
          if (!any(o.x != o.x)) break;
          if (!any(max(max(mx4(d0), mx4(d1)), max(ml.x, ml.y)) == SENT) || spin > SPIN_CAP) break;
        }
        OPAQUE();
        *(LAS v2u*)ow = pk4(o);
      } else {
        __builtin_amdgcn_s_sleep(D_X2);
        for (;; ++spin) {
          OPAQUE();
          const v2u ml = ldx2(rsW, vm, so);
          if (!any(max(ml.x, ml.y) == SENT) || spin > SPIN_CAP) break;
        }
      }
      if (lay == 0) {
        if (WMODE == 1 && hasP3) { dma_wo(lane); drain(); }
        if (hasF) { dma_w1(lane); if (!hasP3) dma_w2(lane); }
      }
      if (w == 0) COUNT(42 + lay * 8, spin);
      STAMPW(0, 10 + lay * 16);
    } else if (lay == 0 && hasP3) { if (w >= 6 || !hasF) drain(); else asm volatile("s_waitcnt vmcnt(4)" ::: "memory"); }
    bar();
    STAMPW(0, 11 + lay * 16);
    if (w < 4 && hasP3) {
      v4u B[8];
#pragma unroll
      for (int k = 0; k < 8; ++k) B[k] = lds16(p3b, k * 64);
      if (lay == 0) {
#pragma unroll
        for (int k = 0; k < 8; ++k) AF[k] = trfrag(p3a, k * 4096, 512);
      }
      SB();
      f4 d = Z4;
#pragma unroll
      for (int k = 0; k < 8; ++k) d = mfma(AF[k], B[k], d);
      if ((lane0 & 15) < 4) stx2(rsW, pack4(d), ymsg, myX3 + lay * XL_STRIDE);
      STAMPW(0, 20 + lay * 16);
    }
    lptr xb0, xb1; int st4 = 0;
    {
      FRESH();
      xb0 = L + L_XB + ((col < 4) ? col : 5) * XBS + hq * 16; xb1 = L + L_XB + ((col >= 4 && col < 8) ? col - 4 : 5) * XBS + hq * 16;
      PINI(xb0); PINI(xb1);
      if (w < 6) {
        if (hasF) {
          if (lay == 0) { b1v = *(const unsigned*)(p.b1 + 192 * f5 + HFO); drain(); if (hasP3) dma_w2(lane); }
          const int s0 = (hq & 1) * 32, base = L_W1 + w * 4096 + hq * 512 + q4 * 64 + pp * 8;
          AF[0] = trfrag(L, base + s0, 256); AF[1] = trfrag(L, base + s0 + 2048, 256); AF[2] = trfrag(L, base + 32 - s0, 256); AF[3] = trfrag(L, base + 32 - s0 + 2048, 256);
          st4 = w * 512 + (16 * q4 + 4 * hq + pp) * 16;
          PINI(st4); PIN4(AF);
          if (lay == 0) {
            const unsigned o0 = col == 0 ? 0x3F803F80u : 0u, o1 = col == 4 ? 0x3F803F80u : 0u;
            f4 e = mfma(AF[0], v4u{o0, o0, o0, o0}, Z4); e = mfma(AF[1], v4u{o0, o0, o0, o0}, e);
            e = mfma(AF[2], v4u{o1, o1, o1, o1}, e); e = mfma(AF[3], v4u{o1, o1, o1, o1}, e);
            if ((col & 11) == 0) stx(rsW, split4(e), 3072 + w * 128 + (col + hq) * 16, myX4);
          }
        }
      } else {
        SEL8();
        v4u s8 = sel8;
        const int so = xo + X3_OFF + sl * 16 * 512;
        int v0 = hq * 512 + (w - 6) * 256 + col * 16, v1 = v0 + 4096;
        lptr xw = L + L_XB + pp * XBS + TKO * 2;
        f4 xr = unp(xp);
        PINI(s8); PINI(v0); PINI(v1); PINI(xw); PINI(xr);
        __builtin_amdgcn_s_sleep(D_X3);
        f4 y;
        int spin = 0;
        for (;; ++spin) {
          OPAQUE();
          const v4u d0 = ldx(rsW, v0, so), d1 = ldx(rsW, v0 + 2048, so), d2 = ldx(rsW, v1, so), d3 = ldx(rsW, v1 + 2048, so);
          SB();
          y = mfma(s8, d0, Z4); y = mfma(s8, d1, y); y = mfma(s8, d2, y); y = mfma(s8, d3, y);
          if (!any(y.x != y.x)) break;
          if (!any(max(max(mx4(d0), mx4(d1)), max(mx4(d2), mx4(d3))) == SENT) || spin > SPIN_CAP) break;
        }
        OPAQUE();
        if (w == 6) COUNT(43 + lay * 8, spin);
        STAMPW(6, 12 + lay * 16);
        xp = pk4(xr + unp(pk4(y)));
        if (lane0 < 32) *(LAS v2u*)xw = xp;
      }
    }
    lptr sra, srb;
    { FRESH(); sra = L + L_XB + (pp == 0 ? 4 : (pp == 1 ? q4 : 5)) * XBS + hq * 16; srb = L + L_XB + pp * XBS + hq * 16; PINI(sra); PINI(srb); }
    auto stats_lds = [&](int off) {
      const v4u a0 = lds16(sra, 0), a1 = lds16(sra, 64), b0 = lds16(srb, 0), b1 = lds16(srb, 64);
      SB();
      f4 d = mfma(a0, b0, Z4); d = mfma(a1, b1, d);
      if ((lane0 & 15) == (lane0 >> 4)) stx2(rsW, v2u{cl(asu(d.x)), cl(asu(d.y))}, (lane0 & 15) * 8, off);
    };
    auto two_tile = [&]() {
      const v4u b00 = lds16(xb0, 0), b01 = lds16(xb0, 64), b10 = lds16(xb1, 0), b11 = lds16(xb1, 64);
      SB();
      f4 d = mfma(AF[0], b00, Z4); d = mfma(AF[1], b01, d); d = mfma(AF[2], b10, d); d = mfma(AF[3], b11, d);
      return split4(d);
    };
    bar();
    STAMPW(0, 13 + lay * 16);
    lptr p5a = L, p5b = L;
    if (w < 6) {
      const int so = xo + X4_OFF + f5 * 9 * X4_MSG;
      int spin = 0;
      if (hasF) {
        { const v4u r = two_tile(); if ((lane0 & 15) < 8) stx(rsW, r, st4, myX4 + lay * XL_STRIDE); }
        STAMPW(0, 14 + lay * 16);
        FRESH(); A128();
        const int pn = lane >> 2;
        const int vv = 2 * (q4 & 1) + (col & 1);
        const unsigned on = ((col >> 3) == (hq >> 1) && (col & 2) == 0) ? ((vv & 1) ? 0x3F800000u : 0x3F80u) : 0u;
        v4u selh = {(vv & 2) ? 0u : on, (vv & 2) ? on : 0u, (vv & 2) ? 0u : on, (vv & 2) ? on : 0u};
        v4u selh0 = (hq & 1) ? v4u{0, 0, 0, 0} : selh;
        int vs = (pn < 9 ? pn : 8) * X4_MSG + 3840 + pp * 8;
        float sm = pn < 9 ? (1.f / DM) : 0.f;
        lptr hw = L + L_HB + pp * HBS + HFO * 2;
        int vo[5];
#pragma unroll
        for (int i = 0; i < 5; ++i) vo[i] = (i < 4 ? (hq & 1) + 2 * i : 8) * X4_MSG + w * 512 + (16 * (hq >> 1) + col) * 16;
        p5a = L + L_W2 + a128 + ((w & 3) ^ z128) * 32; p5b = L + L_HB + pp * HBS + hq * 16;
        f2 bb = {blo(b1v), bhi(b1v)};
        PINI(selh); PINI(selh0); PINI(vs); PINI(sm); PINI(hw); PIN4(vo); PINI(vo[4]); PINI(p5a); PINI(p5b); PINI(bb);
        if (lay == 0) {
          const int ve = 3072 + w * 128 + (4 * (hq >> 1) + q4) * 16;
          for (int sp2 = 0;; ++sp2) {
            OPAQUE();
            const v4u e0 = ldx(rsW, (4 * (hq & 1) + pp) * X4_MSG + ve, so), e1 = ldx(rsW, 8 * X4_MSG + ve, so);
            f4 e = mfma(selh, e0, Z4); const f4 e8 = mfma(selh0, e1, Z4);
            e.x += dppf<XOR1>(e.x); e.y += dppf<XOR1>(e.y); e.x += dppf<XOR2>(e.x); e.y += dppf<XOR2>(e.y);
            cs1 = f2{e.x + e8.x, e.y + e8.y};
            if (!any(max(mx4(e0), mx4(e1)) == SENT) || sp2 > SPIN_CAP) break;
          }
        } else {
          if (w < 4) {
#pragma unroll
            for (int k = 0; k < 6; ++k) AF[k] = trfrag(p5a, k * 4096, 512);
          }
          __builtin_amdgcn_s_sleep(D_X4);
        }
        f4 a; float rstd; f2 t;
        for (;; ++spin) {
          OPAQUE();
          const v2u st = ldx2(rsW, vs, so);
          const v4u d0 = ldx(rsW, vo[0], so), d1 = ldx(rsW, vo[1], so), d2 = ldx(rsW, vo[2], so), d3 = ldx(rsW, vo[3], so), d4 = ldx(rsW, vo[4], so);
          SB();
          float s1 = asf(st.x) * sm, s2 = asf(st.y) * sm;
          s1 += dppf<ROR4>(s1); s2 += dppf<ROR4>(s2); s1 += dppf<ROR8>(s1); s2 += dppf<ROR8>(s2);
          s1 = add32(add16(s1)); s2 = add32(add16(s2));
          rstd = __builtin_amdgcn_rsqf(fmaxf(s2 - s1 * s1, 0.f) + 1e-5f);
          t = bb - (s1 * rstd) * cs1;
          SB();
          a = mfma(selh, d0, Z4); a = mfma(selh, d1, a); a = mfma(selh, d2, a); a = mfma(selh, d3, a); a = mfma(selh0, d4, a);
          if (!any(a.x != a.x || t.x != t.x)) break;
          if (!any(max(max(max(mx4(d0), mx4(d1)), max(mx4(d2), mx4(d3))), max(mx4(d4), max(st.x, st.y))) == SENT) || spin > SPIN_CAP) break;
        }
        OPAQUE();
        STAMPW(0, 15 + lay * 16);
        const f2 v = f2{a.x, a.y} * rstd + t;
        const f2 e = v * (v * v * (-2.f * 0.7978845608028654f * 1.4426950408889634f * 0.044715f) + (-2.f * 0.7978845608028654f * 1.4426950408889634f));
        const float h0 = v.x * __builtin_amdgcn_rcpf(1.f + __builtin_amdgcn_exp2f(e.x)), h1 = v.y * __builtin_amdgcn_rcpf(1.f + __builtin_amdgcn_exp2f(e.y));
        *(LAS unsigned*)hw = pk(h0, h1);
      } else if (w == 0) {
        FRESH();
        const int pn = lane >> 2, vs = (pn < 9 ? pn : 8) * X4_MSG + 3840 + pp * 8;
        __builtin_amdgcn_s_sleep(D_X4 + 8);
        for (;; ++spin) {
          OPAQUE();
          const v2u st = ldx2(rsW, vs, so);
          if (!any(max(st.x, st.y) == SENT) || spin > SPIN_CAP) break;
        }
      }
      if (w == 0) COUNT(44 + lay * 8, spin);
    } else if (w == 6 && hasF) stats_lds(myX4 + lay * XL_STRIDE + 3840);
    bar();
    STAMPW(0, 16 + lay * 16);
    if (w < 4 && hasF) {
      v4u B[6];
#pragma unroll
      for (int k = 0; k < 6; ++k) B[k] = lds16(p5b, k * 64);
      if (lay == 0) {
#pragma unroll
        for (int k = 0; k < 6; ++k) AF[k] = trfrag(p5a, k * 4096, 512);
      }
      SB();
      f4 d = Z4;
#pragma unroll
      for (int k = 0; k < 6; ++k) d = mfma(AF[k], B[k], d);
      if ((lane0 & 15) < 4) stx2(rsW, pack4(d), ymsg, myX5 + lay * XL_STRIDE);
      STAMPW(0, 21 + lay * 16);
    }
    if (lay == 1 && (!isOut || w < 6)) return;
    int st1;
    {
      FRESH(); A128();
      if (lay == 0) {
        if (w >= 4) {
          const int ve = hq * X1_MSG + (16 * (w - 4) + col) * 16 + 4096;
          const v4u e0 = ldx(rsW, ve, x1so), e1 = ldx(rsW, ve + 4 * X1_MSG, x1so), e2 = ldx(rsW, ve + (8 - hq) * X1_MSG, x1so);
          const unsigned one = (lane & 1) ? 0x3F800000u : 0x00003F80u;
          const v4u sel4 = {(col & 2) ? 0u : one, (col & 2) ? one : 0u, (col & 2) ? 0u : one, (col & 2) ? one : 0u};
          csq = mfma(sel4, e0, Z4); csq = mfma(sel4, e1, csq); csq = mfma(hq == 0 ? sel4 : v4u{0, 0, 0, 0}, e2, csq);
        }
        if (hasP1) {
          const int base = L_WQ + (w >> 1) * 8192 + a128, t0 = 2 * (w & 1);
          AF[0] = trfrag(L, base + (t0 ^ z128) * 32, 512); AF[1] = trfrag(L, base + (t0 ^ z128) * 32 + 4096, 512);
          AF[2] = trfrag(L, base + ((t0 + 1) ^ z128) * 32, 512); AF[3] = trfrag(L, base + ((t0 + 1) ^ z128) * 32 + 4096, 512);
          PIN4(AF);
        }
      }
      st1 = pp * 1024 + (8 * w + 4 * q4 + hq) * 16;
      PINI(st1);
      if (w >= 6) {
        SEL8();
        v4u s8 = sel8;
        const int so = xo + X5_OFF + sl * 24 * 512;
        int v0 = hq * 512 + (w - 6) * 256 + col * 16, v1 = v0 + 4096, v2 = v0 + 8192;
        lptr xw = L + L_XB + pp * XBS + TKO * 2;
        f4 xr = unp(xp), bb = unp(b2v);
        PINI(s8); PINI(v0); PINI(v1); PINI(v2); PINI(xw); PINI(xr); PINI(bb);
        if (lay == 1) __builtin_amdgcn_s_sleep(D_X5);
        f4 y;
        int spin = 0;
        for (;; ++spin) {
          OPAQUE();
          const v4u d0 = ldx(rsW, v0, so), d1 = ldx(rsW, v0 + 2048, so), d2 = ldx(rsW, v1, so), d3 = ldx(rsW, v1 + 2048, so), d4 = ldx(rsW, v2, so), d5 = ldx(rsW, v2 + 2048, so);
          SB();
          y = mfma(s8, d0, Z4); y = mfma(s8, d1, y); y = mfma(s8, d2, y); y = mfma(s8, d3, y); y = mfma(s8, d4, y); y = mfma(s8, d5, y);
          if (!any(y.x != y.x)) break;
          if (!any(max(max(max(mx4(d0), mx4(d1)), max(mx4(d2), mx4(d3))), max(mx4(d4), mx4(d5))) == SENT) || spin > SPIN_CAP) break;
        }
        OPAQUE();
        if (w == 6) COUNT(45 + lay * 8, spin);
        STAMPW(6, 17 + lay * 16);
        xp = pk4(xr + unp(pk4(unp(pk4(y)) + bb)));
        if (lay == 1) {
          if (hq < 2) *(v2u*)(p.out + pp * DM + 64 * sl + TKO) = xp;
          asm volatile(".Lfwd_end:");
          STAMPW(6, 34);
          if (w == 6) COUNT(61, __builtin_amdgcn_s_memtime());
          return;
        }
        if (lane0 < 32) *(LAS v2u*)xw = xp;
      }
    }
    bar();
    STAMPW(0, 18);
    if (hasP1) {
      const v4u r = two_tile();
      if ((lane0 & 15) < 8) stx(rsW, r, st1, myX1 + XL_STRIDE);
      if (w == 3) stats_lds(myX1 + XL_STRIDE + 5120);
    }
    STAMPW(0, 19);
  }
}

extern "C" void kernel_launch(void* const* d_in, const int* in_sizes, int n_in, void* d_out, int out_size, void* d_ws, size_t ws_size, hipStream_t stream) {
  Params p;
  p.x = (const u16*)d_in[0]; p.kc = (const u16*)d_in[1]; p.vc = (const u16*)d_in[2]; p.wq = (const u16*)d_in[3]; p.wo = (const u16*)d_in[4];
  p.w1 = (const u16*)d_in[6]; p.b1 = (const u16*)d_in[7]; p.w2 = (const u16*)d_in[8]; p.b2 = (const u16*)d_in[9];
  p.out = (u16*)d_out; p.ws = (unsigned char*)d_ws;
  (void)hipFuncSetAttribute((const void*)fused_fwd, hipFuncAttributeMaxDynamicSharedMemorySize, L_BYTES);
  hipLaunchKernelGGL(fused_fwd, dim3(256), dim3(512), L_BYTES, stream, p);
}
```

```cpp
#include <hip/hip_runtime.h>
#include <stdint.h>

#define STAMPS 0

typedef unsigned short u16;
typedef unsigned long long u64;
typedef unsigned v4u __attribute__((ext_vector_type(4)));
typedef unsigned v8u __attribute__((ext_vector_type(8)));
typedef unsigned v2u __attribute__((ext_vector_type(2)));
typedef float f4 __attribute__((ext_vector_type(4)));
typedef float f2 __attribute__((ext_vector_type(2)));
typedef __bf16 bf2 __attribute__((ext_vector_type(2)));
typedef __bf16 bf8 __attribute__((ext_vector_type(8)));
typedef __bf16 bf16v __attribute__((ext_vector_type(16)));
typedef short s4v __attribute__((ext_vector_type(4)));
#define LAS __attribute__((address_space(3)))
#define DI __device__ __forceinline__
typedef __amdgpu_buffer_rsrc_t rsrc_t;

constexpr int DM = 576, SK = 8192, WIN = 2048, DH = 128, FF = 4608, QN = 4096;
constexpr unsigned SENT = 0xFFFFFFFFu;
constexpr int AUX_NT = 2, AUX_SC1 = 16;

constexpr int WS_EPOCH = 0, WS_FLAGS = 4096, WS_X = 8192;
constexpr int X1_MSG = 5376, X2_MSG = 640, X4_MSG = 3968;
constexpr int X1_OFF = 0, X2_OFF = X1_OFF + 144 * X1_MSG, X3_OFF = X2_OFF + 256 * X2_MSG, X4_OFF = X3_OFF + 144 * 512,
              X5_OFF = X4_OFF + 216 * X4_MSG, XL_BYTES = X5_OFF + 216 * 512;
constexpr int XL_STRIDE = 0x200000;
static_assert(XL_BYTES <= XL_STRIDE, "layer stride");
constexpr int WS_USED = WS_X + 2 * XL_STRIDE;
constexpr int WS_STAMP = 8 << 20;

constexpr int L_WQ = 0;
constexpr int L_WO = 32768;
constexpr int L_W2 = 65536;
constexpr int L_W1 = 90112;
constexpr int L_PA = 114688;
constexpr int L_AO = L_PA + 8 * 2176;
constexpr int L_OB = L_AO + 4096;
constexpr int L_HB = L_OB + 4 * 528;
constexpr int L_XB = L_HB + 4 * 400;
constexpr int L_QP = L_XB + 6 * 144;
constexpr int L_ML = L_QP + 544;
constexpr int L_LN0 = L_ML + 128;
constexpr int L_TAG = L_LN0 + 32;
constexpr int L_CNT = L_TAG + 8;
constexpr int L_BYTES = L_TAG + 16;
constexpr int XBS = 144, OBS = 528, HBS = 400;
static_assert(L_BYTES <= 160 * 1024 && L_OB % 16 == 0 && L_HB % 16 == 0 && L_XB % 16 == 0 && L_QP % 16 == 0 && L_ML % 16 == 0, "lds");

#define D_X1 18
#define D_X2 18
#define D_X3 20
#define D_X4 12
#define D_X5 20
#define XPOS 4
#define WND 8
#define WND0 6
#define KY 8
#define KO 4
#define PREFI 1
#define WMODE 0
#define EXP_NOCLR 0
#define EXP_NOWQ 0
#define EXP_NOX1 0
constexpr int SPIN_CAP = 1 << 18;

struct Params {
  const u16 *x, *kc, *vc, *wq, *wo, *w1, *b1, *w2, *b2;
  u16* out;
  unsigned char* ws;
};

DI f4 mfma(v4u a, v4u b, f4 c) { return __builtin_amdgcn_mfma_f32_16x16x32_bf16(__builtin_bit_cast(bf8, a), __builtin_bit_cast(bf8, b), c, 0, 0, 0); }
DI f4 smf(v4u a, v8u b, f4 c, int idx) { return __builtin_amdgcn_smfmac_f32_16x16x64_bf16(__builtin_bit_cast(bf8, a), __builtin_bit_cast(bf16v, b), c, idx, 0, 0); }
DI v8u cat(v4u a, v4u b) { return __builtin_shufflevector(a, b, 0, 1, 2, 3, 4, 5, 6, 7); }
DI float asf(unsigned u) { return __builtin_bit_cast(float, u); }
DI unsigned asu(float f) { return __builtin_bit_cast(unsigned, f); }
template <int C> DI unsigned dppu(unsigned x) { return (unsigned)__builtin_amdgcn_mov_dpp((int)x, C, 0xf, 0xf, true); }
template <int C> DI float dppf(float x) { return asf(dppu<C>(asu(x))); }
constexpr int XOR1 = 0xB1, XOR2 = 0x4E, ROR4 = 0x124, ROR8 = 0x128;
DI float add16(float x) { auto s = __builtin_amdgcn_permlane16_swap(asu(x), asu(x), false, false); return asf(s[0]) + asf(s[1]); }
DI float add32(float x) { auto s = __builtin_amdgcn_permlane32_swap(asu(x), asu(x), false, false); return asf(s[0]) + asf(s[1]); }
DI float vmax(float a, float b) { float r; asm("v_max_f32 %0, %1, %2" : "=v"(r) : "v"(a), "v"(b)); return r; }
DI float max16(float x) { auto s = __builtin_amdgcn_permlane16_swap(asu(x), asu(x), false, false); return vmax(asf(s[0]), asf(s[1])); }
DI float max32(float x) { auto s = __builtin_amdgcn_permlane32_swap(asu(x), asu(x), false, false); return vmax(asf(s[0]), asf(s[1])); }
DI unsigned pk(float lo, float hi) { bf2 v = {(__bf16)lo, (__bf16)hi}; return __builtin_bit_cast(unsigned, v); }
DI float rbf(float x) { return (float)(__bf16)x; }
DI float blo(unsigned u) { return asf(u << 16); }
DI float bhi(unsigned u) { return asf(u & 0xffff0000u); }
DI unsigned cl(unsigned u) { return min(u, SENT - 1); }
DI v4u split4(f4 d) {
  const unsigned h0 = pk(d.x, d.y), h1 = pk(d.z, d.w);
  return v4u{cl(h0), cl(h1), cl(pk(d.x - blo(h0), d.y - bhi(h0))), cl(pk(d.z - blo(h1), d.w - bhi(h1)))};
}
DI v2u pack4(f4 d) { return v2u{cl(pk(d.x, d.y)), cl(pk(d.z, d.w))}; }
DI unsigned mx4(v4u v) { return max(max(v.x, v.y), max(v.z, v.w)); }
DI v4u ldx(rsrc_t r, int voff, int soff) { return __builtin_amdgcn_raw_buffer_load_b128(r, voff, soff, AUX_SC1); }
DI v2u ldx2(rsrc_t r, int voff, int soff) { return __builtin_amdgcn_raw_buffer_load_b64(r, voff, soff, AUX_SC1); }
DI void stx(rsrc_t r, v4u v, int voff, int soff) { __builtin_amdgcn_raw_buffer_store_b128(v, r, voff, soff, AUX_SC1); }
DI void stx2(rsrc_t r, v2u v, int voff, int soff) { __builtin_amdgcn_raw_buffer_store_b64(v, r, voff, soff, AUX_SC1); }
DI void bar() { asm volatile("s_waitcnt lgkmcnt(0)\n\ts_barrier" ::: "memory"); }
DI void drain() { asm volatile("s_waitcnt vmcnt(0)" ::: "memory"); }
DI rsrc_t mkrs(const void* p, unsigned bytes) { return __builtin_amdgcn_make_buffer_rsrc((void*)p, (short)0, (int)bytes, 0x00020000); }
DI void dma(rsrc_t r, int voff, int soff, unsigned ldsaddr) {
  unsigned keep;
  asm volatile("s_nop 4\n\ts_mov_b32 %0, m0\n\ts_mov_b32 m0, %1\n\ts_nop 0\n\tbuffer_load_dwordx4 %2, %3, %4 offen nt lds\n\ts_mov_b32 m0, %0"
               : "=&s"(keep) : "s"(ldsaddr), "v"(voff), "s"(r), "s"(soff) : "memory");
}
typedef LAS unsigned char* lptr;
DI v4u lds16(lptr L, int off) { return *(LAS v4u*)(L + off); }
#define SB() __builtin_amdgcn_sched_barrier(0)
DI float dot2(unsigned a, unsigned b, float c) { return __builtin_amdgcn_fdot2_f32_bf16(__builtin_bit_cast(bf2, a), __builtin_bit_cast(bf2, b), c, false); }
DI v4u trfrag(LAS unsigned char* L, int off, int step) {
  s4v a = __builtin_amdgcn_ds_read_tr16_b64_v4i16((LAS s4v*)(L + off));
  s4v b = __builtin_amdgcn_ds_read_tr16_b64_v4i16((LAS s4v*)(L + off + step));
  v2u x = __builtin_bit_cast(v2u, a), y = __builtin_bit_cast(v2u, b);
  return v4u{x.x, x.y, y.x, y.y};
}
DI bool any(bool b) { return __builtin_amdgcn_ballot_w64(b) != 0; }
#define OPAQUE() asm volatile("" ::: "memory")
#define Z4 f4{0.f, 0.f, 0.f, 0.f}
#define FRESH() int lane = lane0; asm volatile("" : "+v"(lane)); const int col = lane & 15, hq = lane >> 4, q4 = col >> 2, pp = lane & 3; (void)col; (void)hq; (void)q4; (void)pp
#define SEL8() const unsigned one = (lane & 1) ? 0x3F800000u : 0x00003F80u; \
  const v4u sel8 = {((col & 7) >> 1) == 0 ? one : 0u, ((col & 7) >> 1) == 1 ? one : 0u, ((col & 7) >> 1) == 2 ? one : 0u, ((col & 7) >> 1) == 3 ? one : 0u}
#define A128() const int z128 = ((q4 >> 1) & 1) | ((hq & 1) << 1), a128 = hq * 1024 + q4 * 128 + pp * 8
#define STAMP(i) do {} while (0)
#define STAMPW(ww, i) do {} while (0)
#define COUNT(i, v) do {} while (0)

#define PIN4(a) asm volatile("" : "+v"(a[0]), "+v"(a[1]), "+v"(a[2]), "+v"(a[3]))
#define PINI(x) asm volatile("" : "+v"(x))
#define TKO (32 * (w & 1) + 8 * q4 + 4 * (hq & 1))
#define HFO (32 * w + 16 * (hq >> 1) + 4 * q4 + 2 * (hq & 1))
#define KLOAD(i) K[i] = __builtin_amdgcn_raw_buffer_load_b128(rsK, kvo, __builtin_amdgcn_readfirstlane((((i) + rot) & 15) * 1024), AUX_NT)
#define VLOAD(i) V[i] = __builtin_amdgcn_raw_buffer_load_b128(rsV, vvo, __builtin_amdgcn_readfirstlane((((i) + rot) & 15) * 1024), AUX_NT)

__global__ void __launch_bounds__(512, 2) fused_fwd(Params p) {
  extern __shared__ __attribute__((aligned(16))) unsigned char smem[];
  const lptr L = (lptr)smem;
  const int tid = threadIdx.x, lane0 = tid & 63, lane = lane0, w = __builtin_amdgcn_readfirstlane(tid >> 6);
  const int c = blockIdx.x, xc = c & 7, j = c >> 3, grp = j >> 4, u = j & 15, g = 2 * xc + grp;
  u64 tt[8] = {};
#define TT(i) do { if (STAMPS) { SB(); tt[i] = __builtin_amdgcn_s_memrealtime(); asm volatile("s_waitcnt lgkmcnt(0)" : "+s"(tt[i])); SB(); } } while (0)
  TT(0);
  const int ab = u >> 2, as = u & 3;
  const int col = lane & 15, hq = lane >> 4, q4 = col >> 2, pp = lane & 3;
  const size_t kvbase = ((size_t)(ab * 16 + g) * SK + (SK - WIN) + as * 512 + w * 64) * DH;
  const rsrc_t rsK = mkrs(p.kc + kvbase, 16384), rsV = mkrs(p.vc + kvbase, 16384);
  const int rot = __builtin_amdgcn_readfirstlane((w >> 1) + 4 * (j & 3) + 2 * xc);
  const int kvo = q4 * 256 + (4 * hq + pp) * 16, vvo = lane * 16;
  v4u K[16], V[16];
  const bool hasP1 = u <= 8, hasP3 = u >= 7;
  const int r1 = u < 8 ? xc : 8, gq1 = u < 8 ? 8 * grp + u : g, n3 = (u + 9) & 15;
  const unsigned ldsb = (unsigned)(size_t)L;
  auto dma128 = [&](int ln, rsrc_t r, int rowbytes, int soff, unsigned lds, int i) {
    dma(r, (ln >> 3) * rowbytes + ((((ln & 7) >> 1) ^ (((ln >> 4) & 1) | ((i & 1) << 1))) * 32) + (ln & 1) * 16, soff + 8 * i * rowbytes, lds + i * 1024);
  };
  const rsrc_t rsW = mkrs(p.ws, WS_USED);
  constexpr float CS = 0.08838834764831845f * 1.4426950408889634f;
#define ROLES() \
  const int fi0 = 14 * grp + (u <= 6 ? u : u - 2); \
  const bool hasF = (u != 7) && (u != 8) && (fi0 < 27); \
  const int fi = fi0 < 27 ? fi0 : 0; \
  const int n4 = fi < 24 ? xc : 8, f4i = fi < 24 ? fi : 3 * xc + fi - 24, f5 = 3 * xc + fi / 9, n5 = fi % 9; \
  const int myX1 = WS_X + X1_OFF + (gq1 * 9 + r1) * X1_MSG, myX2 = WS_X + X2_OFF + (g * 16 + u) * X2_MSG, myX3 = WS_X + X3_OFF + (n3 * 16 + g) * 512, \
            myX4 = WS_X + X4_OFF + (f4i * 9 + n4) * X4_MSG, myX5 = WS_X + X5_OFF + (n5 * 24 + f5) * 512
#define QPW(ww) (L_QP + (q4 & 1) * 272 + (((ww) >> 1) * 8 + 2 * ((col >> 1) & 1) + ((ww) & 1)) * 16 + (2 * (col >> 3) + (col & 1)) * 4)
  const int x1so = WS_X + X1_OFF + g * 9 * X1_MSG;
  u64 tag = 0; v4u csw = {0, 0, 0, 0};
  if (w >= 4) {
    KLOAD(0); KLOAD(1);
    OPAQUE(); SB();
    if (w == 7) {
      if (lane < 36) { *(LAS unsigned*)(L + L_XB + 4 * XBS + lane * 4) = 0x3F803F80u; *(LAS unsigned*)(L + L_XB + 5 * XBS + lane * 4) = 0u; }
      if (lane < 2) { *(LAS v4u*)(L + L_QP + 256 + lane * 272) = v4u{0, 0, 0, 0}; *(LAS unsigned*)(L + L_CNT + lane * 4) = 0u; }
    }
    bar();
    KLOAD(2); KLOAD(3);
  } else {
    if (w == 3) {
      const v4u SENT4 = {SENT, SENT, SENT, SENT};
      if (lane < 40) stx(rsW, SENT4, lane * 16, WS_X + X2_OFF + (g * 16 + u) * X2_MSG);
      if (hasP3 && lane < 32) stx(rsW, SENT4, lane * 16, WS_X + X3_OFF + (n3 * 16 + g) * 512);
    }
    OPAQUE();
    if (hasP1 && !EXP_NOWQ) {
      const rsrc_t rq = mkrs(p.wq, DM * QN * 2);
#pragma unroll
      for (int i = 0; i < 8; ++i) dma128(lane, rq, QN * 2, 64 * r1 * (QN * 2) + gq1 * 512 + w * 128, ldsb + L_WQ + w * 8192, i);
    }
    OPAQUE(); SB();
    TT(1);
    const u64 clk0 = STAMPS ? __builtin_amdgcn_s_memtime() : 0;
    v2u ep = ldx2(rsW, 0, WS_EPOCH);
    v4u xv[5] = {}, pf = {0, 0, 0, 0};
    int wb = w, wc = w;
    asm volatile("" : "+s"(wb), "+s"(wc), "+v"(pf), "+v"(xv[0]), "+v"(xv[1]), "+v"(xv[2]), "+v"(xv[3]), "+v"(xv[4]));
    if (wb == 1 && hasP1) {
      const rsrc_t rx = mkrs(p.x, 4 * DM * 2);
      const int xo_ = (hq * DM + col * 8) * 2;
#pragma unroll
      for (int k = 0; k < 4; ++k) xv[k] = __builtin_amdgcn_raw_buffer_load_b128(rx, xo_ + 256 * k, 0, 0);
      if (col < 8) xv[4] = __builtin_amdgcn_raw_buffer_load_b128(rx, xo_ + 1024, 0, 0);
    }
    OPAQUE();
    bar();
    TT(2);
    if (wc == 3 && PREFI) {
      u64 pc; unsigned len;
      asm volatile("s_getpc_b64 %0\n.Lfwd_pc:\n\ts_mov_b32 %1, .Lfwd_end-.Lfwd_pc" : "=s"(pc), "=s"(len));
      const unsigned off = j * 1024 + lane * 16;
      if (off + 16 <= len) pf = *(const __attribute__((address_space(1))) v4u*)((pc & ~15ull) + off);
    }
    OPAQUE();
    ROLES();
    OPAQUE();
#pragma unroll
    for (int i = 0; i < 4; ++i) KLOAD(i);
    OPAQUE();
    TT(3);
    asm volatile("" : "+v"(ep), "+v"(xv[0]), "+v"(xv[1]), "+v"(xv[2]), "+v"(xv[3]), "+v"(xv[4]));
    tag = ((((u64)ep.y << 32) | ep.x) * 6364136223846793005ull + 1442695040888963407ull);
    asm volatile("s_waitcnt vmcnt(4)" : "+v"(pf)::"memory");
    TT(4);
    if (w == 3 && lane == 0) { const u64 f = tag + (u64)c * 0x9E3779B97F4A7C15ull; stx2(rsW, v2u{(unsigned)f, (unsigned)(f >> 32)}, 0, WS_FLAGS + c * 8); }
    if (w == 1 && hasP1) {
      const int ks = r1 >> 1;
      const v4u xs = ks == 0 ? xv[0] : (ks == 1 ? xv[1] : (ks == 2 ? xv[2] : (ks == 3 ? xv[3] : xv[4])));
      if ((col >> 3) == (r1 & 1)) *(LAS v4u*)(L + L_XB + hq * XBS + (col & 7) * 16) = xs;
      float s1 = 0.f, s2 = 0.f;
#pragma unroll
      for (int k = 0; k < 5; ++k)
#pragma unroll
        for (int d = 0; d < 4; ++d) { s1 = dot2(xv[k][d], 0x3F803F80u, s1); s2 = dot2(xv[k][d], xv[k][d], s2); }
      s1 += dppf<XOR1>(s1); s2 += dppf<XOR1>(s2); s1 += dppf<XOR2>(s1); s2 += dppf<XOR2>(s2);
      s1 += dppf<ROR4>(s1); s2 += dppf<ROR4>(s2); s1 += dppf<ROR8>(s1); s2 += dppf<ROR8>(s2);
      const float mu = s1 * (1.f / DM), rs = __builtin_amdgcn_rsqf(fmaxf(s2 * (1.f / DM) - mu * mu, 0.f) + 1e-5f) * CS;
      if (col == 0) *(LAS f2*)(L + L_LN0 + hq * 8) = f2{mu, rs};
      OPAQUE();
      if (lane == 0) *(volatile LAS unsigned*)(L + L_CNT + 4) = 1u;
    }
    if (hasP1) {
      A128();
      for (int spin = 0; *(volatile LAS unsigned*)(L + L_CNT + 4) == 0u && spin < (SPIN_CAP << 4); ++spin) __builtin_amdgcn_s_sleep(1);
      OPAQUE();
      TT(5);
      const unsigned tgx = (unsigned)tag & 0xBFFFBFFFu, tgy = (unsigned)(tag >> 32) & 0xBFFFBFFFu;
      const v4u xf0 = lds16(L, L_XB + pp * XBS + hq * 16), xf1 = lds16(L, L_XB + pp * XBS + hq * 16 + 64);
      const f2 ln = *(LAS f2*)(L + L_LN0 + pp * 8);
      v4u A[8];
#pragma unroll
      for (int t = 0; t < 4; ++t) { const int base = L_WQ + w * 8192 + a128 + (t ^ z128) * 32; A[2 * t] = trfrag(L, base, 512); A[2 * t + 1] = trfrag(L, base + 4096, 512); }
      SB();
      f4 d = Z4, e = Z4;
#pragma unroll
      for (int t = 0; t < 4; ++t) {
        const unsigned m = q4 == t ? ~0u : 0u, o = m & 0x3F803F80u;
        d = mfma(A[2 * t], xf0 & m, d); e = mfma(A[2 * t], v4u{o, o, o, o}, e);
        d = mfma(A[2 * t + 1], xf1 & m, d); e = mfma(A[2 * t + 1], v4u{o, o, o, o}, e);
      }
      d = (d - ln.x * e) * ln.y;
      const int ch = (16 * w + 4 * q4 + hq) * 16;
      if (!EXP_NOX1) stx(rsW, v4u{pk(d.x, d.y), tgx, pk(d.z, d.w), tgy}, pp * 1024 + ch, myX1);
      csw = split4(e);
    }
    TT(6);
    tt[7] = clk0;
  }
  OPAQUE(); SB();
#define WIN() do { if (WND) asm volatile("s_waitcnt vmcnt(%0)" ::"i"(WND - 1) : "memory"); } while (0)
#pragma unroll
  for (int i = 4; i < 16; ++i) { if (i >= WND) WIN(); KLOAD(i); SB(); }
  {
    const v4u z = {0, 0, 0, 0};
    const lptr pa = L + L_PA + w * 2176 + lane * 16;
    *(LAS v4u*)pa = z; *(LAS v4u*)(pa + 1024) = z; if (lane < 8) *(LAS v4u*)(pa + 2048) = z;
  }
  v8u K8[8], V8[8];
  auto transp = [&](int T) {
    const bool o1 = lane & 1, o2 = lane & 2;
#pragma unroll
    for (int d = 0; d < 4; ++d) {
      const unsigned m0 = K[4 * T][d], m1 = K[4 * T + 1][d], m2 = K[4 * T + 2][d], m3 = K[4 * T + 3][d];
      const unsigned s0 = dppu<XOR1>(m0), s1 = dppu<XOR1>(m1), s2 = dppu<XOR1>(m2), s3 = dppu<XOR1>(m3);
      const unsigned n0 = o1 ? s1 : m0, n1 = o1 ? m1 : s0, n2 = o1 ? s3 : m2, n3_ = o1 ? m3 : s2;
      const unsigned t0 = dppu<XOR2>(n0), t1 = dppu<XOR2>(n1), t2 = dppu<XOR2>(n2), t3 = dppu<XOR2>(n3_);
      K8[2 * T][d] = o2 ? t2 : n0; K8[2 * T + 1][d] = o2 ? n2 : t0;
      K8[2 * T][4 + d] = o2 ? t3 : n1; K8[2 * T + 1][4 + d] = o2 ? n3_ : t1;
    }
    asm volatile("" : "+v"(K8[2 * T]), "+v"(K8[2 * T + 1]));
  };
  const int oob = w < 4 ? 0 : 0x40000000;
  const int x1v = hq * X1_MSG + (16 * (w & 3) + col) * 16 + ab * 1024 + oob;
  v4u g0, g1, g2, fa;
#pragma unroll
  for (int i = 0; i < 16; ++i) {
    WIN(); VLOAD(i); SB();
    if (i == XPOS) {
      g0 = ldx(rsW, x1v, x1so); g1 = ldx(rsW, x1v + 4 * X1_MSG, x1so); g2 = ldx(rsW, x1v + (8 - hq) * X1_MSG, x1so);
      fa = ldx(rsW, lane * 16 + (w & 1) * 1024 + oob, WS_FLAGS);
      OPAQUE(); SB();
    }
    if (i == 2 || i == 4 || i == 6 || i == 10) { transp(i == 10 ? 3 : i / 2 - 1); SB(); }
  }
#pragma unroll
  for (int m = 0; m < 8; ++m) V8[m] = cat(V[2 * m], V[2 * m + 1]);
  OPAQUE(); SB();
  {
    ROLES();
    const v4u SENT4 = {SENT, SENT, SENT, SENT};
    if (w >= 4 && hasF) { if (lane < (w == 7 ? 56 : 64)) stx(rsW, SENT4, lane * 16, myX4 + (w - 4) * 1024); if (w == 4 && lane < 32) stx(rsW, SENT4, lane * 16, myX5); }
    if (w < 4 && hasP1 && pp == 0) stx(rsW, csw, 4096 + (16 * w + 4 * q4 + hq) * 16, myX1);
  }
  OPAQUE(); SB();
  STAMPW(0, 3); STAMPW(7, 4);
  if (w == 0) { COUNT(0, tt[0]); COUNT(60, tt[7]); if (hasP1) { COUNT(1, tt[1]); COUNT(2, tt[2]); COUNT(5, tt[3]); COUNT(35, tt[4]); COUNT(38, tt[5]); COUNT(39, tt[6]); } }
  asm volatile("" : "+v"(g0), "+v"(g1), "+v"(g2), "+v"(fa));
  if (w < 4) {
    const unsigned tgx = (unsigned)tag & 0xBFFFBFFFu, tgy = (unsigned)(tag >> 32) & 0xBFFFBFFFu;
    if (w == 0 && lane == 0) *(LAS v2u*)(L + L_TAG) = v2u{(unsigned)tag, (unsigned)(tag >> 32)};
    const unsigned one = (lane & 1) ? 0x3F800000u : 0x00003F80u;
    const v4u selT = {(col & 2) ? 0u : one, 0u, (col & 2) ? one : 0u, 0u}, selT0 = hq == 0 ? selT : v4u{0, 0, 0, 0};
    const u64 e0 = tag + (u64)(lane * 2 + (w & 1) * 128) * 0x9E3779B97F4A7C15ull, e1 = e0 + 0x9E3779B97F4A7C15ull;
    int spin = 0;
#pragma nounroll
    for (; spin < SPIN_CAP; ++spin) {
      if (EXP_NOX1 || !any(g0.y != tgx || g0.w != tgy || g1.y != tgx || g1.w != tgy || g2.y != tgx || g2.w != tgy ||
               fa.x != (unsigned)e0 || fa.y != (unsigned)(e0 >> 32) || fa.z != (unsigned)e1 || fa.w != (unsigned)(e1 >> 32))) break;
      OPAQUE();
      g0 = ldx(rsW, x1v, x1so); g1 = ldx(rsW, x1v + 4 * X1_MSG, x1so); g2 = ldx(rsW, x1v + (8 - hq) * X1_MSG, x1so);
      fa = ldx(rsW, lane * 16 + (w & 1) * 1024, WS_FLAGS);
      asm volatile("" : "+v"(g0), "+v"(g1), "+v"(g2), "+v"(fa));
    }
    f4 q = mfma(selT, g0, Z4); q = mfma(selT, g1, q); q = mfma(selT0, g2, q);
    if (hq == 0) { LAS unsigned* qp = (LAS unsigned*)(L + QPW(w)); qp[0] = pk(q.x, q.y); qp[16] = pk(q.z, q.w); }
    if (w == 0) COUNT(41, spin);
    STAMPW(0, 6);
  }

  v2u xp = {0, 0}, b2v = {0, 0}; unsigned b1v = 0;
  f4 csq = Z4; f2 cs1 = {0.f, 0.f};
  auto unp = [](v2u v) { return f4{blo(v.x), bhi(v.x), blo(v.y), bhi(v.y)}; };
  auto pk4 = [](f4 v) { return v2u{pk(v.x, v.y), pk(v.z, v.w)}; };
  const int w_out = w, u_out = u, xc_out = xc, grp_out = grp;
  for (int lay = 0; lay < 2; ++lay) {
    int w = w_out, u = u_out, xc = xc_out, grp = grp_out;
    asm volatile("" : "+s"(w), "+s"(u), "+s"(xc), "+s"(grp));
    const int g = 2 * xc + grp, ab = u >> 2;
    const bool hasP1 = u <= 8, hasP3 = u >= 7;
    const int r1 = u < 8 ? xc : 8, gq1 = u < 8 ? 8 * grp + u : g, n3 = (u + 9) & 15;
    ROLES();
    const int sl = u == 8 ? 8 : (hasF ? n4 : xc);
    const bool isOut = grp == 0 && (u == 0 || (u == 8 && xc == 0));
  auto dma_w1 = [&](int ln) {
    const rsrc_t r = mkrs(p.w1, DM * FF * 2);
#pragma unroll
    for (int i = 0; i < 4; ++i) dma(r, (ln >> 2) * (FF * 2) + ((ln & 3) ^ (2 * (ln >> 5))) * 16, (64 * n4 + 16 * i) * (FF * 2) + f4i * 384 + w * 64, ldsb + L_W1 + w * 4096 + i * 1024);
  };
  auto dma_w2 = [&](int ln) {
    const rsrc_t r = mkrs(p.w2, FF * DM * 2);
#pragma unroll
    for (int i = 0; i < 4; ++i) dma128(ln, r, DM * 2, (192 * f5 + 32 * w) * (DM * 2) + n5 * 128, ldsb + L_W2 + w * 4096, i);
  };
  auto dma_wo = [&](int ln) {
    const rsrc_t ro = mkrs(p.wo, QN * DM * 2);
#pragma unroll
    for (int i = 0; i < 4; ++i) dma128(ln, ro, DM * 2, (256 * g + 32 * w) * (DM * 2) + n3 * 128, ldsb + L_WO + w * 4096, i);
  };

    const int xo = WS_X + lay * XL_STRIDE;
    lptr qa[4], pw, pr, aow, mlw, mbr, mmr; int qidx, pidx, mst; v4u pm; f4 S = Z4, O = Z4;
    {
      FRESH();
#pragma unroll
      for (int T = 0; T < 4; ++T) qa[T] = L + (q4 == T ? L_QP + (pp * 4 + hq) * 16 : L_QP + 256);
      qidx = (lane & 1) ? 0xEEEE : 0x4444; pidx = pp * 0x1111;
      pw = L + L_PA + w * 2176 + (2 * hq + (pp >> 1)) * 272 + (2 * (pp & 1) + (q4 >> 1)) * 64 + (q4 & 1) * 8;
      pr = L + L_PA + w * 2176 + (hq * 4 + (col >> 3) * 2 + (q4 & 1)) * 16;
      aow = L + L_AO + w * 512 + ((lane >> 5) * 128 + 8 * col + 4 * (hq & 1)) * 2;
      mlw = L + L_ML + (w * 2 + (lane >> 5)) * 8;
      mbr = L + L_AO + (w & 1) * 256 + hq * 512 + col * 16; mmr = L + L_ML + (w & 1) * 8 + hq * 16;
      mst = (w & 1) * 256 + col * 16 + hq * 8;
      const unsigned hm = (lane & 1) ? 0xFFFF0000u : 0xFFFFu;
#pragma unroll
      for (int k = 0; k < 4; ++k) pm[k] = ((col & 7) >> 1) == k ? hm : 0u;
      PIN4(qa); PINI(pw); PINI(pr); PINI(aow); PINI(mlw); PINI(mbr); PINI(mmr); PINI(qidx); PINI(pidx); PINI(mst); PINI(pm); PINI(S); PINI(O);
    }
    if (lay == 1 && w >= 4) {
      FRESH();
      const unsigned one = (lane & 1) ? 0x3F800000u : 0x00003F80u;
      v4u sel4 = {(col & 2) ? 0u : one, (col & 2) ? one : 0u, (col & 2) ? 0u : one, (col & 2) ? one : 0u};
      v4u sel40 = hq == 0 ? sel4 : v4u{0, 0, 0, 0};
      const int so = xo + X1_OFF + g * 9 * X1_MSG;
      int v0 = hq * X1_MSG + (16 * (w - 4) + col) * 16 + ab * 1024, v1 = v0 + 4 * X1_MSG, v2 = v0 + (8 - hq) * X1_MSG, vs = (col < 9 ? col : 8) * X1_MSG + 5120 + ab * 8;
      float sm = col < 9 ? (1.f / DM) : 0.f;
      lptr qw = L + QPW(w - 4);
      PINI(sel4); PINI(sel40); PINI(v0); PINI(v1); PINI(v2); PINI(vs); PINI(qw); PINI(sm);
      __builtin_amdgcn_s_sleep(D_X1);
      f4 d; float rs;
      int spin = 0;
      for (;; ++spin) {
        OPAQUE();
        const v2u st = ldx2(rsW, vs, so);
        const v4u d0 = ldx(rsW, v0, so), d1 = ldx(rsW, v1, so), d2 = ldx(rsW, v2, so);
        SB();
        float s1 = asf(st.x) * sm, s2 = asf(st.y) * sm;
        s1 += dppf<XOR1>(s1); s2 += dppf<XOR1>(s2); s1 += dppf<XOR2>(s1); s2 += dppf<XOR2>(s2);
        s1 += dppf<ROR4>(s1); s2 += dppf<ROR4>(s2); s1 += dppf<ROR8>(s1); s2 += dppf<ROR8>(s2);
        rs = __builtin_amdgcn_rsqf(fmaxf(s2 - s1 * s1, 0.f) + 1e-5f) * CS;
        d = -s1 * csq;
        SB();
        d = mfma(sel4, d0, d); d = mfma(sel4, d1, d); d = mfma(sel40, d2, d);
        if (!any(d.x != d.x)) break;
        if (!any(max(max(max(mx4(d0), mx4(d1)), mx4(d2)), max(st.x, st.y)) == SENT) || spin > SPIN_CAP) break;
      }
      OPAQUE();
      if (w == 4) COUNT(49, spin);
      STAMPW(4, 22);
      const f4 q = d * rs;
      if (lane0 < 16) { LAS unsigned* qp = (LAS unsigned*)qw; qp[0] = pk(q.x, q.y); qp[16] = pk(q.z, q.w); }
    }
    bar();
    STAMPW(0, 7 + lay * 16);
    v4u AF[8];
    {
#pragma unroll
      for (int T = 0; T < 4; ++T) { AF[2 * T] = lds16(qa[T], 0); AF[2 * T + 1] = lds16(qa[T], 272); }
      SB();
#pragma unroll
      for (int m = 0; m < 8; ++m) S = smf(AF[m], K8[m], S, qidx);
      const float s0 = S.x + S.y, s1 = S.z + S.w;
      auto sw = __builtin_amdgcn_permlane32_swap(asu(s0), asu(s1), false, false);
      float mx;
      asm("v_max_f32 %0, %1, %2\n\ts_nop 1\n\tv_max_f32_dpp %0, %0, %0 quad_perm:[1,0,3,2] row_mask:0xf bank_mask:0xf\n\ts_nop 1\n\t"
          "v_max_f32_dpp %0, %0, %0 quad_perm:[2,3,0,1] row_mask:0xf bank_mask:0xf\n\ts_nop 1\n\tv_max_f32_dpp %0, %0, %0 row_ror:4 row_mask:0xf bank_mask:0xf\n\ts_nop 1\n\t"
          "v_max_f32_dpp %0, %0, %0 row_ror:8 row_mask:0xf bank_mask:0xf\n\ts_nop 1\n\tv_max_f32_dpp %0, %0, %0 row_bcast:15 row_mask:0xa bank_mask:0xf\n\ts_nop 0"
          : "=&v"(mx) : "v"(asf(sw[0])), "v"(asf(sw[1])));
      const float m0 = asf(__builtin_amdgcn_readlane(asu(mx), 31)), m1 = asf(__builtin_amdgcn_readlane(asu(mx), 63));
      const float p0 = __builtin_amdgcn_exp2f(s0 - m0), p1 = __builtin_amdgcn_exp2f(s1 - m1);
      const unsigned pb = pk(p0, p1);
      *(LAS u16*)(pw) = (u16)pb; *(LAS u16*)(pw + 20) = (u16)pb; *(LAS u16*)(pw + 32) = (u16)(pb >> 16); *(LAS u16*)(pw + 52) = (u16)(pb >> 16);
#pragma unroll
      for (int m = 0; m < 8; ++m) AF[m] = lds16(pr, m * 272);
      SB();
      auto sl2 = __builtin_amdgcn_permlane32_swap(asu(p0), asu(p1), false, false);
      float ls = asf(sl2[0]) + asf(sl2[1]);
      ls += dppf<XOR1>(ls); ls += dppf<XOR2>(ls); ls += dppf<ROR4>(ls); ls += dppf<ROR8>(ls);
      ls += asf((unsigned)__builtin_amdgcn_update_dpp(0, (int)asu(ls), 0x142, 0xa, 0xf, false));
      if ((lane0 & 31) == 31) *(LAS f2*)mlw = f2{mx, ls};
      SB();
#pragma unroll
      for (int m = 0; m < 8; ++m) O = smf(AF[m], V8[m], O, pidx);
      if (lay == 0) {
        drain();
        if (WMODE == 0 && hasP3) { FRESH(); dma_wo(lane); }
      }
      *(LAS v2u*)aow = pk4(O);
    }
    bar();
    STAMPW(0, 8 + lay * 16);
    if (w >= 6) {
      const f2 ml0 = *(LAS f2*)mmr, ml1 = *(LAS f2*)(mmr + 64);
      const v4u B0 = lds16(mbr, 0), B1 = lds16(mbr, 2048);
      SB();
      const float M = max32(max16(vmax(ml0.x, ml1.x)));
      const float f0 = __builtin_amdgcn_exp2f(ml0.x - M), f1 = __builtin_amdgcn_exp2f(ml1.x - M);
      const unsigned h0 = pk(f0, f0), h1 = pk(f1, f1);
      f4 o = mfma(pm & h0, B0, Z4); o = mfma(pm & h1, B1, o);
      const float Ls = add32(add16(blo(h0) * ml0.y + blo(h1) * ml1.y));
      const int so = myX2 + lay * XL_STRIDE;
      if (lane0 == 0) stx2(rsW, v2u{cl(asu(M)), cl(asu(Ls))}, 512 + (w & 1) * 8, so);
      if (lane0 < 32) stx2(rsW, pack4(o), mst, so);
      STAMPW(6, 9 + lay * 16);
    }
    if (lay == 0 && w >= 4) {
      FRESH();
      if (w >= 6) { xp = *(const v2u*)(p.x + pp * DM + 64 * sl + TKO); b2v = *(const v2u*)(p.b2 + 64 * sl + TKO); }
      if (w < 6) {
        const v4u SENT4 = {SENT, SENT, SENT, SENT};
        const int lo = lane * 16, a0 = (w == 4 ? myX4 : myX1) + XL_STRIDE;
        __builtin_amdgcn_s_sleep(D_X2 + 2);
        if (w == 4 ? hasF : hasP1) {
          stx(rsW, SENT4, lo, a0); stx(rsW, SENT4, lo, a0 + 1024); stx(rsW, SENT4, lo, a0 + 2048);
          if (w == 5) stx(rsW, SENT4, lo, a0 + 3072);
          if (lane < 8) stx(rsW, SENT4, lo, a0 + (w == 4 ? 3840 : 5120));
        }
        if (w == 4) { if (lane < 40) stx(rsW, SENT4, lo, myX2 + XL_STRIDE); if (hasF && lane < 32) stx(rsW, SENT4, lo, myX5 + XL_STRIDE); }
        else if (hasP3 && lane < 32) stx(rsW, SENT4, lo, myX3 + XL_STRIDE);
      }
      if (WMODE == 1 && hasP3) dma_wo(lane);
      if (hasF && w < 6) { dma_w1(lane); if (!hasP3) dma_w2(lane); }
    }
    lptr p3a = L, p3b = L; int ymsg = 0;
    if (w < 4) {
      FRESH(); A128();
      const int so = xo + X2_OFF + (g * 16 + w * 4) * X2_MSG;
      int vm = hq * X2_MSG + 512 + (col >> 3) * 8, vd = hq * X2_MSG + col * 16;
      p3a = L + L_WO + a128 + (w ^ z128) * 32; p3b = L + L_OB + pp * OBS + hq * 16; ymsg = (4 * (2 * w + (hq >> 1)) + pp) * 16 + (hq & 1) * 8;
      PINI(vm); PINI(vd); PINI(p3a); PINI(p3b); PINI(ymsg);
      int spin = 0;
      if (hasP3) {
        v4u pm0 = col < 8 ? pm : v4u{0, 0, 0, 0}, pm1 = col < 8 ? v4u{0, 0, 0, 0} : pm;
        lptr ow = L + L_OB + w * OBS + ((hq >> 1) * 128 + 8 * col + 4 * (hq & 1)) * 2;
        PINI(pm0); PINI(pm1); PINI(ow);
        if (lay == 1) {
#pragma unroll
          for (int k = 0; k < 8; ++k) AF[k] = trfrag(p3a, k * 4096, 512);
        }
        __builtin_amdgcn_s_sleep(D_X2);
        f4 o;
        for (;; ++spin) {
          OPAQUE();
          const v2u ml = ldx2(rsW, vm, so);
          const v4u d0 = ldx(rsW, vd, so), d1 = ldx(rsW, vd + 256, so);
          SB();
          const float m = asf(ml.x), M = max32(max16(m));
          const float f = __builtin_amdgcn_exp2f(m - M);
          const float Ls = add32(add16(f * asf(ml.y)));
          const float fs = f * __builtin_amdgcn_rcpf(Ls);
          const unsigned gg = pk(fs, fs);
          o = mfma(pm0 & gg, d0, Z4); o = mfma(pm1 & gg, d1, o);
          if (!any(o.x != o.x)) break;
          if (!any(max(max(mx4(d0), mx4(d1)), max(ml.x, ml.y)) == SENT) || spin > SPIN_CAP) break;
        }
        OPAQUE();
        *(LAS v2u*)ow = pk4(o);
      } else {
        __builtin_amdgcn_s_sleep(D_X2);
        for (;; ++spin) {
          OPAQUE();
          const v2u ml = ldx2(rsW, vm, so);
          if (!any(max(ml.x, ml.y) == SENT) || spin > SPIN_CAP) break;
        }
      }
      if (lay == 0) {
        if (WMODE == 1 && hasP3) { dma_wo(lane); drain(); }
        if (hasF) { dma_w1(lane); if (!hasP3) dma_w2(lane); }
      }
      if (w == 0) COUNT(42 + lay * 8, spin);
      STAMPW(0, 10 + lay * 16);
    } else if (lay == 0) {
      if (w >= 6 || !hasF) drain(); else if (hasP3) asm volatile("s_waitcnt vmcnt(4)" ::: "memory"); else asm volatile("s_waitcnt vmcnt(8)" ::: "memory");
    }
    bar();
    STAMPW(0, 11 + lay * 16);
    if (w < 4 && hasP3) {
      v4u B[8];
#pragma unroll
      for (int k = 0; k < 8; ++k) B[k] = lds16(p3b, k * 64);
      if (lay == 0) {
#pragma unroll
        for (int k = 0; k < 8; ++k) AF[k] = trfrag(p3a, k * 4096, 512);
      }
      SB();
      f4 d = Z4;
#pragma unroll
      for (int k = 0; k < 8; ++k) d = mfma(AF[k], B[k], d);
      if ((lane0 & 15) < 4) stx2(rsW, pack4(d), ymsg, myX3 + lay * XL_STRIDE);
      STAMPW(0, 20 + lay * 16);
    }
    lptr xb0, xb1; int st4 = 0;
    {
      FRESH();
      xb0 = L + L_XB + ((col < 4) ? col : 5) * XBS + hq * 16; xb1 = L + L_XB + ((col >= 4 && col < 8) ? col - 4 : 5) * XBS + hq * 16;
      PINI(xb0); PINI(xb1);
      if (w < 6) {
        if (hasF) {
          if (lay == 0) { b1v = *(const unsigned*)(p.b1 + 192 * f5 + HFO); drain(); if (hasP3) dma_w2(lane); }
          const int s0 = (hq & 1) * 32, base = L_W1 + w * 4096 + hq * 512 + q4 * 64 + pp * 8;
          AF[0] = trfrag(L, base + s0, 256); AF[1] = trfrag(L, base + s0 + 2048, 256); AF[2] = trfrag(L, base + 32 - s0, 256); AF[3] = trfrag(L, base + 32 - s0 + 2048, 256);
          st4 = w * 512 + (16 * q4 + 4 * hq + pp) * 16;
          PINI(st4); PIN4(AF);
          if (lay == 0) {
            const unsigned o0 = col == 0 ? 0x3F803F80u : 0u, o1 = col == 4 ? 0x3F803F80u : 0u;
            f4 e = mfma(AF[0], v4u{o0, o0, o0, o0}, Z4); e = mfma(AF[1], v4u{o0, o0, o0, o0}, e);
            e = mfma(AF[2], v4u{o1, o1, o1, o1}, e); e = mfma(AF[3], v4u{o1, o1, o1, o1}, e);
            if ((col & 11) == 0) stx(rsW, split4(e), 3072 + w * 128 + (col + hq) * 16, myX4);
          }
        }
      } else {
        SEL8();
        v4u s8 = sel8;
        const int so = xo + X3_OFF + sl * 16 * 512;
        int v0 = hq * 512 + (w - 6) * 256 + col * 16, v1 = v0 + 4096;
        lptr xw = L + L_XB + pp * XBS + TKO * 2;
        f4 xr = unp(xp);
        PINI(s8); PINI(v0); PINI(v1); PINI(xw); PINI(xr);
        __builtin_amdgcn_s_sleep(D_X3);
        f4 y;
        int spin = 0;
        for (;; ++spin) {
          OPAQUE();
          const v4u d0 = ldx(rsW, v0, so), d1 = ldx(rsW, v0 + 2048, so), d2 = ldx(rsW, v1, so), d3 = ldx(rsW, v1 + 2048, so);
          SB();
          y = mfma(s8, d0, Z4); y = mfma(s8, d1, y); y = mfma(s8, d2, y); y = mfma(s8, d3, y);
          if (!any(y.x != y.x)) break;
          if (!any(max(max(mx4(d0), mx4(d1)), max(mx4(d2), mx4(d3))) == SENT) || spin > SPIN_CAP) break;
        }
        OPAQUE();
        if (w == 6) COUNT(43 + lay * 8, spin);
        STAMPW(6, 12 + lay * 16);
        xp = pk4(xr + unp(pk4(y)));
        if (lane0 < 32) *(LAS v2u*)xw = xp;
      }
    }
    lptr sra, srb;
    { FRESH(); sra = L + L_XB + (pp == 0 ? 4 : (pp == 1 ? q4 : 5)) * XBS + hq * 16; srb = L + L_XB + pp * XBS + hq * 16; PINI(sra); PINI(srb); }
    auto stats_lds = [&](int off) {
      const v4u a0 = lds16(sra, 0), a1 = lds16(sra, 64), b0 = lds16(srb, 0), b1 = lds16(srb, 64);
      SB();
      f4 d = mfma(a0, b0, Z4); d = mfma(a1, b1, d);
      if ((lane0 & 15) == (lane0 >> 4)) stx2(rsW, v2u{cl(asu(d.x)), cl(asu(d.y))}, (lane0 & 15) * 8, off);
    };
    auto two_tile = [&]() {
      const v4u b00 = lds16(xb0, 0), b01 = lds16(xb0, 64), b10 = lds16(xb1, 0), b11 = lds16(xb1, 64);
      SB();
      f4 d = mfma(AF[0], b00, Z4); d = mfma(AF[1], b01, d); d = mfma(AF[2], b10, d); d = mfma(AF[3], b11, d);
      return split4(d);
    };
    bar();
    if (lay == 0 && c == 0 && w == 7 && lane0 == 0) stx2(rsW, *(LAS v2u*)(L + L_TAG), 0, WS_EPOCH);
    STAMPW(0, 13 + lay * 16);
    lptr p5a = L, p5b = L;
    if (w < 6) {
      const int so = xo + X4_OFF + f5 * 9 * X4_MSG;
      int spin = 0;
      if (hasF) {
        { const v4u r = two_tile(); if ((lane0 & 15) < 8) stx(rsW, r, st4, myX4 + lay * XL_STRIDE); }
        STAMPW(0, 14 + lay * 16);
        FRESH(); A128();
        const int pn = lane >> 2;
        const int vv = 2 * (q4 & 1) + (col & 1);
        const unsigned on = ((col >> 3) == (hq >> 1) && (col & 2) == 0) ? ((vv & 1) ? 0x3F800000u : 0x3F80u) : 0u;
        v4u selh = {(vv & 2) ? 0u : on, (vv & 2) ? on : 0u, (vv & 2) ? 0u : on, (vv & 2) ? on : 0u};
        v4u selh0 = (hq & 1) ? v4u{0, 0, 0, 0} : selh;
        int vs = (pn < 9 ? pn : 8) * X4_MSG + 3840 + pp * 8;
        float sm = pn < 9 ? (1.f / DM) : 0.f;
        lptr hw = L + L_HB + pp * HBS + HFO * 2;
        int vo[5];
#pragma unroll
        for (int i = 0; i < 5; ++i) vo[i] = (i < 4 ? (hq & 1) + 2 * i : 8) * X4_MSG + w * 512 + (16 * (hq >> 1) + col) * 16;
        p5a = L + L_W2 + a128 + ((w & 3) ^ z128) * 32; p5b = L + L_HB + pp * HBS + hq * 16;
        f2 bb = {blo(b1v), bhi(b1v)};
        PINI(selh); PINI(selh0); PINI(vs); PINI(sm); PINI(hw); PIN4(vo); PINI(vo[4]); PINI(p5a); PINI(p5b); PINI(bb);
        if (lay == 0) {
          const int ve = 3072 + w * 128 + (4 * (hq >> 1) + q4) * 16;
          for (int sp2 = 0;; ++sp2) {
            OPAQUE();
            const v4u e0 = ldx(rsW, (4 * (hq & 1) + pp) * X4_MSG + ve, so), e1 = ldx(rsW, 8 * X4_MSG + ve, so);
            f4 e = mfma(selh, e0, Z4); const f4 e8 = mfma(selh0, e1, Z4);
            e.x += dppf<XOR1>(e.x); e.y += dppf<XOR1>(e.y); e.x += dppf<XOR2>(e.x); e.y += dppf<XOR2>(e.y);
            cs1 = f2{e.x + e8.x, e.y + e8.y};
            if (!any(max(mx4(e0), mx4(e1)) == SENT) || sp2 > SPIN_CAP) break;
          }
        } else {
          if (w < 4) {
#pragma unroll
            for (int k = 0; k < 6; ++k) AF[k] = trfrag(p5a, k * 4096, 512);
          }
          __builtin_amdgcn_s_sleep(D_X4);
        }
        f4 a; float rstd; f2 t;
        for (;; ++spin) {
          OPAQUE();
          const v2u st = ldx2(rsW, vs, so);
          const v4u d0 = ldx(rsW, vo[0], so), d1 = ldx(rsW, vo[1], so), d2 = ldx(rsW, vo[2], so), d3 = ldx(rsW, vo[3], so), d4 = ldx(rsW, vo[4], so);
          SB();
          float s1 = asf(st.x) * sm, s2 = asf(st.y) * sm;
          s1 += dppf<ROR4>(s1); s2 += dppf<ROR4>(s2); s1 += dppf<ROR8>(s1); s2 += dppf<ROR8>(s2);
          s1 = add32(add16(s1)); s2 = add32(add16(s2));
          rstd = __builtin_amdgcn_rsqf(fmaxf(s2 - s1 * s1, 0.f) + 1e-5f);
          t = bb - (s1 * rstd) * cs1;
          SB();
          a = mfma(selh, d0, Z4); a = mfma(selh, d1, a); a = mfma(selh, d2, a); a = mfma(selh, d3, a); a = mfma(selh0, d4, a);
          if (!any(a.x != a.x || t.x != t.x)) break;
          if (!any(max(max(max(mx4(d0), mx4(d1)), max(mx4(d2), mx4(d3))), max(mx4(d4), max(st.x, st.y))) == SENT) || spin > SPIN_CAP) break;
        }
        OPAQUE();
        STAMPW(0, 15 + lay * 16);
        const f2 v = f2{a.x, a.y} * rstd + t;
        const f2 e = v * (v * v * (-2.f * 0.7978845608028654f * 1.4426950408889634f * 0.044715f) + (-2.f * 0.7978845608028654f * 1.4426950408889634f));
        const float h0 = v.x * __builtin_amdgcn_rcpf(1.f + __builtin_amdgcn_exp2f(e.x)), h1 = v.y * __builtin_amdgcn_rcpf(1.f + __builtin_amdgcn_exp2f(e.y));
        *(LAS unsigned*)hw = pk(h0, h1);
      } else if (w == 0) {
        FRESH();
        const int pn = lane >> 2, vs = (pn < 9 ? pn : 8) * X4_MSG + 3840 + pp * 8;
        __builtin_amdgcn_s_sleep(D_X4 + 8);
        for (;; ++spin) {
          OPAQUE();
          const v2u st = ldx2(rsW, vs, so);
          if (!any(max(st.x, st.y) == SENT) || spin > SPIN_CAP) break;
        }
      }
      if (w == 0) COUNT(44 + lay * 8, spin);
    } else if (w == 6 && hasF) stats_lds(myX4 + lay * XL_STRIDE + 3840);
    bar();
    STAMPW(0, 16 + lay * 16);
    if (w < 4 && hasF) {
      v4u B[6];
#pragma unroll
      for (int k = 0; k < 6; ++k) B[k] = lds16(p5b, k * 64);
      if (lay == 0) {
#pragma unroll
        for (int k = 0; k < 6; ++k) AF[k] = trfrag(p5a, k * 4096, 512);
      }
      SB();
      f4 d = Z4;
#pragma unroll
      for (int k = 0; k < 6; ++k) d = mfma(AF[k], B[k], d);
      if ((lane0 & 15) < 4) stx2(rsW, pack4(d), ymsg, myX5 + lay * XL_STRIDE);
      STAMPW(0, 21 + lay * 16);
    }
    if (lay == 1 && (!isOut || w < 6)) return;
    int st1;
    {
      FRESH(); A128();
      if (lay == 0) {
        if (w >= 4) {
          const int ve = hq * X1_MSG + (16 * (w - 4) + col) * 16 + 4096;
          const v4u e0 = ldx(rsW, ve, x1so), e1 = ldx(rsW, ve + 4 * X1_MSG, x1so), e2 = ldx(rsW, ve + (8 - hq) * X1_MSG, x1so);
          const unsigned one = (lane & 1) ? 0x3F800000u : 0x00003F80u;
          const v4u sel4 = {(col & 2) ? 0u : one, (col & 2) ? one : 0u, (col & 2) ? 0u : one, (col & 2) ? one : 0u};
          csq = mfma(sel4, e0, Z4); csq = mfma(sel4, e1, csq); csq = mfma(hq == 0 ? sel4 : v4u{0, 0, 0, 0}, e2, csq);
        }
        if (hasP1) {
          const int base = L_WQ + (w >> 1) * 8192 + a128, t0 = 2 * (w & 1);
          AF[0] = trfrag(L, base + (t0 ^ z128) * 32, 512); AF[1] = trfrag(L, base + (t0 ^ z128) * 32 + 4096, 512);
          AF[2] = trfrag(L, base + ((t0 + 1) ^ z128) * 32, 512); AF[3] = trfrag(L, base + ((t0 + 1) ^ z128) * 32 + 4096, 512);
          PIN4(AF);
        }
      }
      st1 = pp * 1024 + (8 * w + 4 * q4 + hq) * 16;
      PINI(st1);
      if (w >= 6) {
        SEL8();
        v4u s8 = sel8;
        const int so = xo + X5_OFF + sl * 24 * 512;
        int v0 = hq * 512 + (w - 6) * 256 + col * 16, v1 = v0 + 4096, v2 = v0 + 8192;
        lptr xw = L + L_XB + pp * XBS + TKO * 2;
        f4 xr = unp(xp), bb = unp(b2v);
        PINI(s8); PINI(v0); PINI(v1); PINI(v2); PINI(xw); PINI(xr); PINI(bb);
        if (lay == 1) __builtin_amdgcn_s_sleep(D_X5);
        f4 y;
        int spin = 0;
        for (;; ++spin) {
          OPAQUE();
          const v4u d0 = ldx(rsW, v0, so), d1 = ldx(rsW, v0 + 2048, so), d2 = ldx(rsW, v1, so), d3 = ldx(rsW, v1 + 2048, so), d4 = ldx(rsW, v2, so), d5 = ldx(rsW, v2 + 2048, so);
          SB();
          y = mfma(s8, d0, Z4); y = mfma(s8, d1, y); y = mfma(s8, d2, y); y = mfma(s8, d3, y); y = mfma(s8, d4, y); y = mfma(s8, d5, y);
          if (!any(y.x != y.x)) break;
          if (!any(max(max(max(mx4(d0), mx4(d1)), max(mx4(d2), mx4(d3))), max(mx4(d4), mx4(d5))) == SENT) || spin > SPIN_CAP) break;
        }
        OPAQUE();
        if (w == 6) COUNT(45 + lay * 8, spin);
        STAMPW(6, 17 + lay * 16);
        xp = pk4(xr + unp(pk4(unp(pk4(y)) + bb)));
        if (lay == 1) {
          if (hq < 2) *(v2u*)(p.out + pp * DM + 64 * sl + TKO) = xp;
          asm volatile(".Lfwd_end:");
          STAMPW(6, 34);
          if (w == 6) COUNT(61, __builtin_amdgcn_s_memtime());
          return;
        }
        if (lane0 < 32) *(LAS v2u*)xw = xp;
      }
    }
    bar();
    STAMPW(0, 18);
    if (hasP1) {
      const v4u r = two_tile();
      if ((lane0 & 15) < 8) stx(rsW, r, st1, myX1 + XL_STRIDE);
      if (w == 3) stats_lds(myX1 + XL_STRIDE + 5120);
    }
    STAMPW(0, 19);
  }
}

extern "C" void kernel_launch(void* const* d_in, const int* in_sizes, int n_in, void* d_out, int out_size, void* d_ws, size_t ws_size, hipStream_t stream) {
  Params p;
  p.x = (const u16*)d_in[0]; p.kc = (const u16*)d_in[1]; p.vc = (const u16*)d_in[2]; p.wq = (const u16*)d_in[3]; p.wo = (const u16*)d_in[4];
  p.w1 = (const u16*)d_in[6]; p.b1 = (const u16*)d_in[7]; p.w2 = (const u16*)d_in[8]; p.b2 = (const u16*)d_in[9];
  p.out = (u16*)d_out; p.ws = (unsigned char*)d_ws;
  (void)hipFuncSetAttribute((const void*)fused_fwd, hipFuncAttributeMaxDynamicSharedMemorySize, L_BYTES);
  hipLaunchKernelGGL(fused_fwd, dim3(256), dim3(512), L_BYTES, stream, p);
}
```

```cpp
#include <hip/hip_runtime.h>
#include <stdint.h>

#define STAMPS 0

typedef unsigned short u16;
typedef unsigned long long u64;
typedef unsigned v4u __attribute__((ext_vector_type(4)));
typedef unsigned v8u __attribute__((ext_vector_type(8)));
typedef unsigned v2u __attribute__((ext_vector_type(2)));
typedef float f4 __attribute__((ext_vector_type(4)));
typedef float f2 __attribute__((ext_vector_type(2)));
typedef __bf16 bf2 __attribute__((ext_vector_type(2)));
typedef __bf16 bf8 __attribute__((ext_vector_type(8)));
typedef __bf16 bf16v __attribute__((ext_vector_type(16)));
typedef short s4v __attribute__((ext_vector_type(4)));
#define LAS __attribute__((address_space(3)))
#define DI __device__ __forceinline__
typedef __amdgpu_buffer_rsrc_t rsrc_t;

constexpr int DM = 576, SK = 8192, WIN = 2048, DH = 128, FF = 4608, QN = 4096;
constexpr unsigned SENT = 0xFFFFFFFFu;
constexpr int AUX_NT = 2, AUX_SC1 = 16;

constexpr int PG = 2 << 20, BLK = 16384, HALF = 8192;
constexpr int WS_EPOCH = 0, WS_FLAGS = 4096, WS_MARK = 8192, WS_HER = 40960, X1A_OFF = 65536, X1_MSG = 5376;
constexpr int X2_MSG = 640, X2_OFF = PG, X3_OFF = PG + 32 * BLK, X5_OFF = PG + 41 * BLK, SM_STRIDE = 60 * BLK;
constexpr int X4_OFF = 2 * PG;
constexpr int X1_OFF = 4 * PG;
constexpr int WS_USED = 5 * PG;
static_assert(X1A_OFF + 144 * X1_MSG <= PG && 2 * SM_STRIDE <= PG && 120 * BLK <= PG, "pages");
constexpr int WS_STAMP = 16 << 20;

constexpr int L_WQ = 0;
constexpr int L_WO = 32768;
constexpr int L_W2 = 65536;
constexpr int L_W1 = 90112;
constexpr int L_PA = 114688;
constexpr int L_AO = L_PA + 8 * 2176;
constexpr int L_OB = L_AO + 4096;
constexpr int L_HB = L_OB + 4 * 528;
constexpr int L_XB = L_HB + 4 * 400;
constexpr int L_QP = L_XB + 6 * 144;
constexpr int L_ML = L_QP + 512;
constexpr int L_LN0 = L_ML + 128;
constexpr int L_RDY = L_LN0 + 32;
constexpr int L_TAG = L_LN0 + 32;
constexpr int L_TM = L_TAG + 16;
constexpr int L_BYTES = L_TM + 16;
constexpr int XBS = 144, OBS = 528, HBS = 400;
static_assert(L_BYTES <= 160 * 1024 && L_OB % 16 == 0 && L_HB % 16 == 0 && L_XB % 16 == 0 && L_QP % 16 == 0 && L_ML % 16 == 0, "lds");

#define D_X1 12
#define D_X2 18
#define D_X3 20
#define D_X4 10
#define D_X5 18
#define I1 170
#define I2 165
#define I3 100
#define I4 100
#define I5 110
#define D_X5A 22
#define D_X4A 12
#define D_M 8
#define N45 130
constexpr unsigned T1B = I1, T2B = T1B + I2, T3B = T2B + I3, T4B = T3B + I4, T5B = T4B + I5;
#define SCHED 1
#define NUMA 1
#define FPOS 19
#define XPOS 3
#define SPOS 14
#define WND 10
#define TP0 2
#define TP1 5
#define TP2 8
#define TP3 11
#define D_W 11
#define PREFI 1
#define OUT_AUX 0
#define SNT 2
constexpr int SPIN_CAP = 1 << 18;

struct Params {
  const u16 *x, *kc, *vc, *wq, *wo, *w1, *b1, *w2, *b2;
  u16* out;
  unsigned char* ws;
};

DI f4 mfma(v4u a, v4u b, f4 c) { return __builtin_amdgcn_mfma_f32_16x16x32_bf16(__builtin_bit_cast(bf8, a), __builtin_bit_cast(bf8, b), c, 0, 0, 0); }
DI f4 smf(v4u a, v8u b, f4 c, int idx) { return __builtin_amdgcn_smfmac_f32_16x16x64_bf16(__builtin_bit_cast(bf8, a), __builtin_bit_cast(bf16v, b), c, idx, 0, 0); }
DI v8u cat(v4u a, v4u b) { return __builtin_shufflevector(a, b, 0, 1, 2, 3, 4, 5, 6, 7); }
DI float asf(unsigned u) { return __builtin_bit_cast(float, u); }
DI unsigned asu(float f) { return __builtin_bit_cast(unsigned, f); }
template <int C> DI unsigned dppu(unsigned x) { return (unsigned)__builtin_amdgcn_mov_dpp((int)x, C, 0xf, 0xf, true); }
template <int C> DI float dppf(float x) { return asf(dppu<C>(asu(x))); }
constexpr int XOR1 = 0xB1, XOR2 = 0x4E, ROR4 = 0x124, ROR8 = 0x128;
DI float add16(float x) { auto s = __builtin_amdgcn_permlane16_swap(asu(x), asu(x), false, false); return asf(s[0]) + asf(s[1]); }
DI float add32(float x) { auto s = __builtin_amdgcn_permlane32_swap(asu(x), asu(x), false, false); return asf(s[0]) + asf(s[1]); }
DI float vmax(float a, float b) { float r; asm("v_max_f32 %0, %1, %2" : "=v"(r) : "v"(a), "v"(b)); return r; }
DI float max16(float x) { auto s = __builtin_amdgcn_permlane16_swap(asu(x), asu(x), false, false); return vmax(asf(s[0]), asf(s[1])); }
DI float max32(float x) { auto s = __builtin_amdgcn_permlane32_swap(asu(x), asu(x), false, false); return vmax(asf(s[0]), asf(s[1])); }
DI unsigned pk(float lo, float hi) { bf2 v = {(__bf16)lo, (__bf16)hi}; return __builtin_bit_cast(unsigned, v); }
DI float rbf(float x) { return (float)(__bf16)x; }
DI float blo(unsigned u) { return asf(u << 16); }
DI float bhi(unsigned u) { return asf(u & 0xffff0000u); }
DI unsigned cl(unsigned u) { return min(u, SENT - 1); }
DI v4u split4(f4 d) {
  const unsigned h0 = pk(d.x, d.y), h1 = pk(d.z, d.w);
  return v4u{cl(h0), cl(h1), cl(pk(d.x - blo(h0), d.y - bhi(h0))), cl(pk(d.z - blo(h1), d.w - bhi(h1)))};
}
DI v2u pack4(f4 d) { return v2u{cl(pk(d.x, d.y)), cl(pk(d.z, d.w))}; }
DI unsigned mx4(v4u v) { return max(max(v.x, v.y), max(v.z, v.w)); }
DI v4u ldx(rsrc_t r, int voff, int soff) { return __builtin_amdgcn_raw_buffer_load_b128(r, voff, soff, AUX_SC1); }
DI v2u ldx2(rsrc_t r, int voff, int soff) { return __builtin_amdgcn_raw_buffer_load_b64(r, voff, soff, AUX_SC1); }
DI void stx(rsrc_t r, v4u v, int voff, int soff) { __builtin_amdgcn_raw_buffer_store_b128(v, r, voff, soff, AUX_SC1); }
DI void stx2(rsrc_t r, v2u v, int voff, int soff) { __builtin_amdgcn_raw_buffer_store_b64(v, r, voff, soff, AUX_SC1); }
DI v4u ldn(rsrc_t r, int voff, int soff) { return __builtin_amdgcn_raw_buffer_load_b128(r, voff, soff, AUX_SC1 | SNT); }
DI void stn(rsrc_t r, v4u v, int voff, int soff) { __builtin_amdgcn_raw_buffer_store_b128(v, r, voff, soff, AUX_SC1 | SNT); }
DI void bar() { asm volatile("s_waitcnt lgkmcnt(0)\n\ts_barrier" ::: "memory"); }
DI void drain() { asm volatile("s_waitcnt vmcnt(0)" ::: "memory"); }
DI rsrc_t mkrs(const void* p, unsigned bytes) { return __builtin_amdgcn_make_buffer_rsrc((void*)p, (short)0, (int)bytes, 0x00020000); }
DI void dma(rsrc_t r, int voff, int soff, unsigned ldsaddr) {
  unsigned keep;
  asm volatile("s_nop 4\n\ts_mov_b32 %0, m0\n\ts_mov_b32 m0, %1\n\ts_nop 0\n\tbuffer_load_dwordx4 %2, %3, %4 offen nt lds\n\ts_mov_b32 m0, %0"
               : "=&s"(keep) : "s"(ldsaddr), "v"(voff), "s"(r), "s"(soff) : "memory");
}
typedef LAS unsigned char* lptr;
DI v4u lds16(lptr L, int off) { return *(LAS v4u*)(L + off); }
#define SB() __builtin_amdgcn_sched_barrier(0)
DI float dot2(unsigned a, unsigned b, float c) { return __builtin_amdgcn_fdot2_f32_bf16(__builtin_bit_cast(bf2, a), __builtin_bit_cast(bf2, b), c, false); }
DI v4u trfrag(LAS unsigned char* L, int off, int step) {
  s4v a = __builtin_amdgcn_ds_read_tr16_b64_v4i16((LAS s4v*)(L + off));
  s4v b = __builtin_amdgcn_ds_read_tr16_b64_v4i16((LAS s4v*)(L + off + step));
  v2u x = __builtin_bit_cast(v2u, a), y = __builtin_bit_cast(v2u, b);
  return v4u{x.x, x.y, y.x, y.y};
}
DI bool any(bool b) { return __builtin_amdgcn_ballot_w64(b) != 0; }
#define OPAQUE() asm volatile("" ::: "memory")
#define Z4 f4{0.f, 0.f, 0.f, 0.f}
#define FRESH() int lane = lane0; asm volatile("" : "+v"(lane)); const int col = lane & 15, hq = lane >> 4, q4 = col >> 2, pp = lane & 3; (void)col; (void)hq; (void)q4; (void)pp
#define SEL8() const unsigned one = (lane & 1) ? 0x3F800000u : 0x00003F80u; \
  const v4u sel8 = {((col & 7) >> 1) == 0 ? one : 0u, ((col & 7) >> 1) == 1 ? one : 0u, ((col & 7) >> 1) == 2 ? one : 0u, ((col & 7) >> 1) == 3 ? one : 0u}
#define A128() const int z128 = ((q4 >> 1) & 1) | ((hq & 1) << 1), a128 = hq * 1024 + q4 * 128 + pp * 8
#define STAMP(i) do {} while (0)
#define STAMPW(ww, i) do {} while (0)
#define COUNT(i, v) do {} while (0)

#define PIN4(a) asm volatile("" : "+v"(a[0]), "+v"(a[1]), "+v"(a[2]), "+v"(a[3]))
#define PINI(x) asm volatile("" : "+v"(x))
#define PINS(x) asm volatile("" : "+s"(x))
#define TKO (32 * (w & 1) + 8 * q4 + 4 * (hq & 1))
#define HFO (32 * w + 16 * (hq >> 1) + 4 * q4 + 2 * (hq & 1))
#define KLOAD(i) K[i] = __builtin_amdgcn_raw_buffer_load_b128(rsK, kvo, __builtin_amdgcn_readfirstlane((((i) + rot) & 15) * 1024), AUX_NT)
#define VLOAD(i) V[i] = __builtin_amdgcn_raw_buffer_load_b128(rsV, vvo, __builtin_amdgcn_readfirstlane((((i) + rot) & 15) * 1024), AUX_NT)
#define QPW(ww) (L_QP + (q4 & 1) * 256 + (((ww) >> 1) * 8 + 2 * ((col >> 1) & 1) + ((ww) & 1)) * 16 + (2 * (col >> 3) + (col & 1)) * 4)
#define TT(i) do { if (STAMPS == 1 || (STAMPS && (i) == 0)) { SB(); tt[i] = __builtin_amdgcn_s_memrealtime(); asm volatile("s_waitcnt lgkmcnt(0)" : "+s"(tt[i])); SB(); } } while (0)
DI unsigned now() { return (unsigned)__builtin_amdgcn_s_memrealtime() & 0x7FFFFFFFu; }
DI void until(unsigned t) { for (int i = 0; i < 4096 && ((t - now()) & 0x7FFFFFFFu) - 1u < 0x3FFFFFFFu; ++i) {} }
DI unsigned tmax16(unsigned t) {
  const unsigned ref = __builtin_amdgcn_readfirstlane(t);
  int d = (int)((t - ref) << 1);
  d = max(d, (int)dppu<XOR1>(d)); d = max(d, (int)dppu<XOR2>(d)); d = max(d, (int)dppu<ROR4>(d)); d = max(d, (int)dppu<ROR8>(d));
  return (ref + (unsigned)(__builtin_amdgcn_readfirstlane(d) >> 1)) & 0x7FFFFFFFu;
}
#define RTTN 50
#define LATE(T) do { if (SCHED && lay_ == 1 && spin > 0 && lane0 == 0) __hip_atomic_fetch_max((LAS int*)(L + L_TM + 12), (int)(((now() - (t0 + (T) + RTTN)) << 1)) >> 1, __ATOMIC_RELAXED, __HIP_MEMORY_SCOPE_WORKGROUP); } while (0)
#define LOOKAT(T) until(t0 + (T) + (unsigned)__builtin_amdgcn_readfirstlane(*(volatile LAS int*)(L + L_TM + 12)))
DI u64 next_tag(v2u e) { return (((u64)e.y << 32) | e.x) * 6364136223846793005ull + 1442695040888963407ull; }
DI void dma128(int ln, rsrc_t r, int rowbytes, int soff, unsigned lds, int i) {
  dma(r, (ln >> 3) * rowbytes + ((((ln & 7) >> 1) ^ (((ln >> 4) & 1) | ((i & 1) << 1))) * 32) + (ln & 1) * 16, soff + 8 * i * rowbytes, lds + i * 1024);
}
DI f4 unp(v2u v) { return f4{blo(v.x), bhi(v.x), blo(v.y), bhi(v.y)}; }
DI v2u pk4(f4 v) { return v2u{pk(v.x, v.y), pk(v.z, v.w)}; }

__global__ void __launch_bounds__(512, 2) fused_fwd(Params p) {
  extern __shared__ __attribute__((aligned(16))) unsigned char smem[];
  const lptr L = (lptr)smem;
  const int tid = threadIdx.x, lane0 = tid & 63, w = __builtin_amdgcn_readfirstlane(tid >> 6);
  const int c = blockIdx.x, xc = c & 7, j = c >> 3, grp = j >> 4, u = j & 15, g = 2 * xc + grp;
  u64 tt[8] = {};
  TT(0);
  const int ab = u >> 2, as = u & 3;
  const bool hasP1 = u <= 8, hasP3 = u >= 7;
  const unsigned ldsb = (unsigned)(size_t)L;
  const rsrc_t rsW = mkrs((const void*)(((size_t)p.ws + PG - 1) & ~(size_t)(PG - 1)), WS_USED);
  constexpr float CS = 0.08838834764831845f * 1.4426950408889634f;
  v8u K8[8], V8[8];
  int pub1, pub2, pub3, pub4, pub5, gat1, gat2, gat3, gat4, gat5, gatc, mko, cls, slo, wofs, w1ofs, w2ofs, b1ofs;
  bool hasF, isOut;
  lptr qa, pw, pr, aow, mlw, mbr, mmr; int qidx, pidx, mst; v4u pm;
  {
    const int lane = lane0, col = lane & 15, hq = lane >> 4, q4 = col >> 2, pp = lane & 3;
    const size_t kvbase = ((size_t)(ab * 16 + g) * SK + (SK - WIN) + as * 512 + w * 64) * DH;
    const rsrc_t rsK = mkrs(p.kc + kvbase, 16384), rsV = mkrs(p.vc + kvbase, 16384);
    const int rot = __builtin_amdgcn_readfirstlane((w >> 1) + 4 * (j & 3) + 2 * xc);
    const int kvo = q4 * 256 + (4 * hq + pp) * 16, vvo = lane * 16;
    const int r1 = u < 8 ? xc : 8, gq1 = u < 8 ? 8 * grp + u : g, n3 = (u + 9) & 15;
    v4u K[16], V[16], csw = {0, 0, 0, 0};
    u64 tag = 0;
    if (w >= 4) {
      if (w == 4 && hasP1) {
        const rsrc_t rx = mkrs(p.x, 4 * DM * 2);
        const int xo_ = (hq * DM + col * 8) * 2;
        v4u xv[5];
#pragma unroll
        for (int k = 0; k < 4; ++k) xv[k] = __builtin_amdgcn_raw_buffer_load_b128(rx, xo_ + 256 * k, 0, 0);
        xv[4] = __builtin_amdgcn_raw_buffer_load_b128(rx, col < 8 ? xo_ + 1024 : 0x40000000, 0, 0);
        if (lane == 0) *(volatile LAS unsigned*)(L + L_RDY) = 0u;
        bar();
        SB();
        const int ks = r1 >> 1;
        const v4u xs = ks == 0 ? xv[0] : (ks == 1 ? xv[1] : (ks == 2 ? xv[2] : (ks == 3 ? xv[3] : xv[4])));
        if ((col >> 3) == (r1 & 1)) *(LAS v4u*)(L + L_XB + hq * XBS + (col & 7) * 16) = xs;
        float s1 = 0.f, s2 = 0.f, s3 = 0.f, s4 = 0.f;
#pragma unroll
        for (int k = 0; k < 5; ++k) {
          s1 = dot2(xv[k][0], 0x3F803F80u, s1); s2 = dot2(xv[k][0], xv[k][0], s2); s3 = dot2(xv[k][1], 0x3F803F80u, s3); s4 = dot2(xv[k][1], xv[k][1], s4);
          s1 = dot2(xv[k][2], 0x3F803F80u, s1); s2 = dot2(xv[k][2], xv[k][2], s2); s3 = dot2(xv[k][3], 0x3F803F80u, s3); s4 = dot2(xv[k][3], xv[k][3], s4);
        }
        s1 += s3; s2 += s4;
        s1 += dppf<XOR1>(s1); s2 += dppf<XOR1>(s2); s1 += dppf<XOR2>(s1); s2 += dppf<XOR2>(s2);
        s1 += dppf<ROR4>(s1); s2 += dppf<ROR4>(s2); s1 += dppf<ROR8>(s1); s2 += dppf<ROR8>(s2);
        const float mu = s1 * (1.f / DM), rs = __builtin_amdgcn_rsqf(fmaxf(s2 * (1.f / DM) - mu * mu, 0.f) + 1e-5f) * CS;
        if (col == 0) *(LAS f2*)(L + L_LN0 + hq * 8) = f2{mu, rs};
        OPAQUE();
        if (lane == 0) *(volatile LAS unsigned*)(L + L_RDY) = 1u;
        OPAQUE(); SB();
#pragma unroll
        for (int i = 0; i < 4; ++i) KLOAD(i);
      } else {
        KLOAD(0); KLOAD(1); KLOAD(2);
        if (hasP1) bar();
        KLOAD(3);
      }
    } else {
      unsigned nearb = 0;
      if (w == 3) {
        const v4u SENT4 = {SENT, SENT, SENT, SENT};
        const int a2 = lane < 40 ? X2_OFF + (2 * g + (ab >> 1)) * BLK + ((ab & 1) * 4 + as) * X2_MSG + lane * 16 : 0x40000000;
        const int a3 = (hasP3 && lane < 32) ? X3_OFF + n3 * BLK + g * 512 + lane * 16 : 0x40000000;
        u64 ta, tb;
        asm volatile("buffer_store_dwordx4 %2, %3, %5, 0 offen sc1\n\tbuffer_store_dwordx4 %2, %3, %5, %6 offen sc1\n\t"
                     "buffer_store_dwordx4 %2, %4, %5, 0 offen sc1\n\tbuffer_store_dwordx4 %2, %4, %5, %6 offen sc1\n\t"
                     "s_waitcnt vmcnt(3)\n\ts_memtime %0\n\ts_waitcnt vmcnt(2)\n\ts_memtime %1\n\ts_waitcnt lgkmcnt(0)"
                     : "=&s"(ta), "=&s"(tb) : "v"(SENT4), "v"(a2), "v"(a3), "s"(rsW), "s"(HALF) : "memory");
        COUNT(56, tb - ta);
        nearb = ((unsigned)(tb - ta) > 140u ? 1u : 0u) | (((unsigned)__builtin_amdgcn_s_getreg(20 | (3 << 11)) >> 1) & 2u);
      }
      v2u ep;
      asm volatile("buffer_load_dwordx2 %0, off, %1, 0 sc1" : "=v"(ep) : "s"(rsW) : "memory");
      {
        const rsrc_t rq = mkrs(hasP1 ? p.wq : p.wo, DM * QN * 2);
        const int rb = hasP1 ? QN * 2 : DM * 2, so = hasP1 ? 64 * r1 * (QN * 2) + gq1 * 512 + w * 128 : (256 * g + 64 * w) * (DM * 2) + n3 * 128;
#pragma unroll
        for (int i = 0; i < 8; ++i) dma128(lane, rq, rb, so, ldsb + (hasP1 ? L_WQ : L_WO) + w * 8192, i);
        if (hasP1) bar();
      }
      OPAQUE(); SB();
      v4u pf = {0, 0, 0, 0};
      if (PREFI) {
        u64 pc; unsigned len;
        asm volatile("s_getpc_b64 %0\n.Lfwd_pc:\n\ts_mov_b32 %1, .Lfwd_end-.Lfwd_pc" : "=s"(pc), "=s"(len));
        pf = __builtin_amdgcn_raw_buffer_load_b128(mkrs((const void*)(pc & ~15ull), len & ~15u), w == 2 ? j * 1024 + lane * 16 : 0x40000000, 0, 0);
      }
      OPAQUE(); SB();
      TT(1);
#pragma unroll
      for (int i = 0; i < 4; ++i) KLOAD(i);
      OPAQUE(); SB();
      asm volatile("s_waitcnt vmcnt(%1)" : "+v"(ep) : "i"(12 + PREFI) : "memory");
      tag = next_tag(ep);
      if (w == 3 && lane == 0) { const u64 f = tag + (u64)c * 0x9E3779B97F4A7C15ull; stx2(rsW, v2u{((unsigned)f & ~3u) | nearb, (unsigned)(f >> 32)}, 0, WS_FLAGS + c * 8); }
      if (hasP1) {
        A128();
        asm volatile("s_waitcnt vmcnt(%0)" ::"i"(4 + PREFI) : "memory");
        TT(2);
        v4u A[8];
#pragma unroll
        for (int t = 0; t < 4; ++t) { const int base = L_WQ + w * 8192 + a128 + (t ^ z128) * 32; A[2 * t] = trfrag(L, base, 512); A[2 * t + 1] = trfrag(L, base + 4096, 512); }
        for (int spin = 0; *(volatile LAS unsigned*)(L + L_RDY) == 0u && spin < (SPIN_CAP << 4); ++spin) __builtin_amdgcn_s_sleep(1);
        OPAQUE();
        TT(3);
        const v4u xf0 = lds16(L, L_XB + pp * XBS + hq * 16), xf1 = lds16(L, L_XB + pp * XBS + hq * 16 + 64);
        const f2 ln = *(LAS f2*)(L + L_LN0 + pp * 8);
        SB();
        f4 d = Z4, e = Z4;
#pragma unroll
        for (int t = 0; t < 4; ++t) {
          const unsigned m = q4 == t ? ~0u : 0u, o = m & 0x3F803F80u;
          d = mfma(A[2 * t], xf0 & m, d); e = mfma(A[2 * t], v4u{o, o, o, o}, e);
          d = mfma(A[2 * t + 1], xf1 & m, d); e = mfma(A[2 * t + 1], v4u{o, o, o, o}, e);
        }
        d = (d - ln.x * e) * ln.y;
        stn(rsW, v4u{pk(d.x, d.y), (unsigned)tag & 0xBFFFBFFFu, pk(d.z, d.w), (unsigned)(tag >> 32) & 0xBFFFBFFFu},
            pp * 1024 + (16 * w + 4 * q4 + hq) * 16, X1A_OFF + (gq1 * 9 + r1) * X1_MSG);
        csw = split4(e);
      }
      TT(4);
      asm volatile("" ::"v"(pf));
    }
    OPAQUE(); SB();
    v4u fa;
#define WIN() do { if (WND) asm volatile("s_waitcnt vmcnt(%0)" ::"i"(WND - 1) : "memory"); } while (0)
#pragma unroll
    for (int i = 4; i < 16; ++i) {
      if (i >= WND) WIN();
      KLOAD(i); SB();
      if (i == FPOS) { if (w < 4) fa = ldn(rsW, lane * 16 + (w & 1) * 1024, WS_FLAGS); OPAQUE(); SB(); }
    }
    {
      const int fi0 = 14 * grp + (u <= 6 ? u : u - 2);
      hasF = (u != 7) && (u != 8) && (fi0 < 27);
      const int fi = fi0 < 27 ? fi0 : 0;
      const int n4 = fi < 24 ? xc : 8, f4i = fi < 24 ? fi : 3 * xc + fi - 24, f5 = 3 * xc + fi / 9, n5 = fi % 9;
      const int sl = u == 8 ? 8 : (hasF ? n4 : xc);
      isOut = grp == 0 && (u == 0 || (u == 8 && xc == 0));
      pub1 = X1_OFF + 5 * gq1 * BLK + (r1 < 8 ? r1 * 1024 : 4 * BLK); pub2 = X2_OFF + (2 * g + (ab >> 1)) * BLK + ((ab & 1) * 4 + as) * X2_MSG;
      pub3 = X3_OFF + n3 * BLK + g * 512; pub4 = X4_OFF + (5 * f4i + (n4 >> 1)) * BLK + (n4 & 1) * 4096; pub5 = X5_OFF + (2 * n5 + (f5 >> 4)) * BLK + (f5 & 15) * 512;
      gat1 = X1_OFF + 5 * g * BLK; gat2 = X2_OFF + (2 * g + ((w & 3) >> 1)) * BLK + (w & 1) * 4 * X2_MSG; gat3 = X3_OFF + sl * BLK;
      gat4 = X4_OFF + 5 * f5 * BLK; gat5 = X5_OFF + 2 * sl * BLK; gatc = X1A_OFF + g * 9 * X1_MSG; mko = WS_MARK + (n5 * 24 + f5) * 128;
      cls = (gq1 >> 1) | (n3 << 4) | ((f4i / 3) << 8) | (n5 << 12) | (r1 << 16) | (u << 20) | (g << 24);
      slo = 64 * sl; wofs = 256 * g * (DM * 2) + n3 * 128; w1ofs = 64 * n4 * (FF * 2) + f4i * 384; w2ofs = 192 * f5 * (DM * 2) + n5 * 128; b1ofs = 192 * f5;
      PINS(pub1); PINS(pub2); PINS(pub3); PINS(pub4); PINS(pub5); PINS(gat1); PINS(gat2); PINS(gat3); PINS(gat4); PINS(gat5); PINS(gatc); PINS(mko); PINS(cls);
      PINS(slo); PINS(wofs); PINS(w1ofs); PINS(w2ofs); PINS(b1ofs);
      qa = L + L_QP + (pp * 4 + hq) * 16;
      qidx = (lane & 1) ? 0xEEEE : 0x4444; pidx = pp * 0x1111;
      pw = L + L_PA + w * 2176 + (2 * hq + (pp >> 1)) * 272 + (2 * (pp & 1) + (q4 >> 1)) * 64 + (q4 & 1) * 8;
      pr = L + L_PA + w * 2176 + (hq * 4 + (col >> 3) * 2 + (q4 & 1)) * 16;
      aow = L + L_AO + w * 512 + ((lane >> 5) * 128 + 8 * col + 4 * (hq & 1)) * 2;
      mlw = L + L_ML + (w * 2 + (lane >> 5)) * 8;
      mbr = L + L_AO + (w & 1) * 256 + hq * 512 + col * 16; mmr = L + L_ML + (w & 1) * 8 + hq * 16;
      mst = hq < 2 ? (w & 1) * 256 + col * 16 + hq * 8 : 0x40000000;
      const unsigned hm = (lane & 1) ? 0xFFFF0000u : 0xFFFFu;
#pragma unroll
      for (int k = 0; k < 4; ++k) pm[k] = ((col & 7) >> 1) == k ? hm : 0u;
      PINI(qa); PINI(pw); PINI(pr); PINI(aow); PINI(mlw); PINI(mbr); PINI(mmr); PINI(qidx); PINI(pidx); PINI(mst); PINI(pm);
      const v4u z = {0, 0, 0, 0};
      const lptr pa = L + L_PA + w * 2176 + lane * 16;
      *(LAS v4u*)pa = z; *(LAS v4u*)(pa + 1024) = z; if (lane < 8) *(LAS v4u*)(pa + 2048) = z;
      if (w == 7 && lane == 0) { *(LAS unsigned*)(L + L_TM + 4) = 0u; *(LAS unsigned*)(L + L_TM + 12) = 0u; }
      if (w == 7 && lane < 36) { *(LAS unsigned*)(L + L_XB + 4 * XBS + lane * 4) = 0x3F803F80u; *(LAS unsigned*)(L + L_XB + 5 * XBS + lane * 4) = 0u; }
    }
    SB();
    auto transp = [&](int T) {
      const bool o1 = lane & 1, o2 = lane & 2;
#pragma unroll
      for (int d = 0; d < 4; ++d) {
        const unsigned m0 = K[4 * T][d], m1 = K[4 * T + 1][d], m2 = K[4 * T + 2][d], m3 = K[4 * T + 3][d];
        const unsigned s0 = dppu<XOR1>(m0), s1 = dppu<XOR1>(m1), s2 = dppu<XOR1>(m2), s3 = dppu<XOR1>(m3);
        const unsigned n0 = o1 ? s1 : m0, n1 = o1 ? m1 : s0, n2 = o1 ? s3 : m2, n3_ = o1 ? m3 : s2;
        const unsigned t0 = dppu<XOR2>(n0), t1 = dppu<XOR2>(n1), t2 = dppu<XOR2>(n2), t3 = dppu<XOR2>(n3_);
        K8[2 * T][d] = o2 ? t2 : n0; K8[2 * T + 1][d] = o2 ? n2 : t0;
        K8[2 * T][4 + d] = o2 ? t3 : n1; K8[2 * T + 1][4 + d] = o2 ? n3_ : t1;
      }
      asm volatile("" : "+v"(K8[2 * T]), "+v"(K8[2 * T + 1]));
    };
    int x1a = hq * X1_MSG + (16 * (w & 3) + col) * 16 + ab * 1024, x1b = x1a + 4 * X1_MSG, x1c = x1a + (8 - hq) * X1_MSG;
    int ta = (w < 4 && hasP1 && pp == 0) ? X1A_OFF + (gq1 * 9 + r1) * X1_MSG + 4096 + (16 * w + 4 * q4 + hq) * 16 : 0x40000000;
    const unsigned one = (lane & 1) ? 0x3F800000u : 0x00003F80u;
    v4u selT = {(col & 2) ? 0u : one, 0u, (col & 2) ? one : 0u, 0u}, selT0 = hq == 0 ? selT : v4u{0, 0, 0, 0};
    unsigned tgx = (unsigned)tag & 0xBFFFBFFFu, tgy = (unsigned)(tag >> 32) & 0xBFFFBFFFu;
    lptr qw = L + QPW(w & 3);
    PINI(x1a); PINI(x1b); PINI(x1c); PINI(ta); PINI(csw); PINI(selT); PINI(selT0); PINI(tgx); PINI(tgy); PINI(qw);
    v4u g0, g1, g2;
#pragma unroll
    for (int i = 0; i < 16; ++i) {
      WIN(); VLOAD(i); SB();
      if (i == FPOS - 16) { if (w < 4) fa = ldn(rsW, lane * 16 + (w & 1) * 1024, WS_FLAGS); OPAQUE(); SB(); }
      if (i == XPOS) {
        if (w < 4) { g0 = ldn(rsW, x1a, gatc); g1 = ldn(rsW, x1b, gatc); g2 = ldn(rsW, x1c, gatc); }
        OPAQUE(); SB();
      }
      if (i == SPOS) {
        PINI(fa);
        if (w < 4) {
          const u64 e0 = tag + (u64)(lane * 2 + (w & 1) * 128) * 0x9E3779B97F4A7C15ull, e1 = e0 + 0x9E3779B97F4A7C15ull;
          int spin = 0;
#pragma nounroll
          for (; spin < SPIN_CAP; ++spin) {
            if (!any(((fa.x ^ (unsigned)e0) > 3u) || fa.y != (unsigned)(e0 >> 32) || ((fa.z ^ (unsigned)e1) > 3u) || fa.w != (unsigned)(e1 >> 32))) break;
            OPAQUE();
            fa = ldn(rsW, lane * 16 + (w & 1) * 1024, WS_FLAGS);
            PINI(fa);
          }
          if (w == 0) COUNT(40, spin);
          stn(rsW, csw, ta, 0);
          if (!(w & 1)) {
            const int g0v = __builtin_popcountll(__builtin_amdgcn_ballot_w64((fa.x & 3u) == 1u)) + __builtin_popcountll(__builtin_amdgcn_ballot_w64((fa.z & 3u) == 1u));
            const int g1v = __builtin_popcountll(__builtin_amdgcn_ballot_w64((fa.x & 3u) == 3u)) + __builtin_popcountll(__builtin_amdgcn_ballot_w64((fa.z & 3u) == 3u));
            const unsigned se = (unsigned)__builtin_amdgcn_ballot_w64(fa.x & 2u), so_ = (unsigned)__builtin_amdgcn_ballot_w64(fa.z & 2u);
            unsigned hm = 0;
#pragma unroll
            for (int k = 0; k < 4; ++k) hm |= ((se >> k) & 1u) << (2 * k) | ((so_ >> k) & 1u) << (2 * k + 1);
            if (g0v > g1v) hm ^= 0xFFu;
            if (!NUMA) hm = 0;
            if (w == 0 && lane == 0) { *(LAS unsigned*)(L + L_TM + 8) = hm; *(LAS u64*)(L + L_TAG + 8) = tag; }
            if (hasF) {
              unsigned sn = SENT; PINI(sn);
              const v4u SENT4 = {sn, sn, sn, sn};
              const int a4 = pub4 + (((hm >> ((cls >> 8) & 15)) & 1) << 13) + w * 1024, n5 = (cls >> 12) & 15;
              stn(rsW, SENT4, lane * 16, a4);
              if (w == 0 || lane < 56) stn(rsW, SENT4, lane * 16, a4 + 1024);
              if (lane < 32 && (n5 == 8 || (((hm >> n5) ^ 1) & 1) == (unsigned)(w >> 1))) stn(rsW, SENT4, lane * 16, pub5 + (w >> 1) * HALF);
            }
          }
        }
        OPAQUE(); SB();
      }
      if (i == TP0) { transp(0); SB(); }
      if (i == TP1) { transp(1); SB(); }
      if (i == TP2) { transp(2); SB(); }
      if (i == TP3) { transp(3); SB(); }
    }
#pragma unroll
    for (int m = 0; m < 8; ++m) V8[m] = cat(V[2 * m], V[2 * m + 1]);
    OPAQUE(); SB();
    STAMPW(0, 3); STAMPW(7, 4);
    if (w == 0) { COUNT(0, tt[0]); COUNT(60, __builtin_amdgcn_s_memtime()); COUNT(59, __builtin_amdgcn_s_memrealtime()); if (hasP1) { COUNT(1, tt[1]); COUNT(2, tt[2]); COUNT(5, tt[3]); COUNT(35, tt[4]); } }
    asm volatile("" : "+v"(g0), "+v"(g1), "+v"(g2));
    if (w < 4) {
      int spin = 0;
#pragma nounroll
      for (; spin < SPIN_CAP; ++spin) {
        if (!any(g0.y != tgx || g0.w != tgy || g1.y != tgx || g1.w != tgy || g2.y != tgx || g2.w != tgy)) break;
        OPAQUE();
        g0 = ldn(rsW, x1a, gatc); g1 = ldn(rsW, x1b, gatc); g2 = ldn(rsW, x1c, gatc);
        asm volatile("" : "+v"(g0), "+v"(g1), "+v"(g2));
      }
      f4 q = mfma(selT, g0, Z4); q = mfma(selT, g1, q); q = mfma(selT0, g2, q);
      if (lane < 16) { LAS unsigned* qp = (LAS unsigned*)qw; qp[0] = pk(q.x, q.y); qp[16] = pk(q.z, q.w); }
      if (w == 0) COUNT(41, spin);
      STAMPW(0, 6);
    }
  }
  f4 S[4] = {Z4, Z4, Z4, Z4}, O = Z4;
  PIN4(S); PINI(O);
  bar();
  STAMPW(0, 7);
  {
    const unsigned hm = __builtin_amdgcn_readfirstlane(*(LAS unsigned*)(L + L_TM + 8));
    if (w == 0) COUNT(55, hm);
    const int own = ((hm >> xc) & 1) << 13;
    pub1 += ((hm >> (cls & 15)) & 1) << 13; pub2 += own ^ HALF; pub3 += (((hm >> ((cls >> 4) & 15)) & 1) << 13) ^ HALF;
    pub4 += ((hm >> ((cls >> 8) & 15)) & 1) << 13; pub5 += (((hm >> ((cls >> 12) & 15)) & 1) << 13) ^ HALF;
    gat1 += own; gat2 += own ^ HALF; gat3 += own ^ HALF; gat4 += own; gat5 += own ^ HALF;
  }

  v2u xp = {0, 0}, b2v = {0, 0}; unsigned b1v = 0;
  f4 csq = Z4; f2 cs1 = {0.f, 0.f};
  unsigned t0 = 0;
  const int w_ = w, ab_ = ab, pub1_ = pub1, pub2_ = pub2, pub3_ = pub3, pub4_ = pub4, pub5_ = pub5, gat1_ = gat1, gat2_ = gat2, gat3_ = gat3, gat4_ = gat4, gat5_ = gat5, gatc_ = gatc, mko_ = mko, cls_ = cls, slo_ = slo, wofs_ = wofs, w1ofs_ = w1ofs, w2ofs_ = w2ofs, b1ofs_ = b1ofs;
#pragma nounroll
  for (int lay = 0; lay < 2; ++lay) {
    const int xs = __builtin_amdgcn_readfirstlane(lay * SM_STRIDE), x4 = __builtin_amdgcn_readfirstlane(lay * (PG + HALF));
    int w = w_, ab = ab_, pub1 = pub1_, pub2 = pub2_, pub3 = pub3_, pub4 = pub4_, pub5 = pub5_, gat1 = gat1_, gat2 = gat2_, gat3 = gat3_, gat4 = gat4_, gat5 = gat5_, gatc = gatc_, mko = mko_, cls = cls_, slo = slo_, wofs = wofs_, w1ofs = w1ofs_, w2ofs = w2ofs_, b1ofs = b1ofs_;
    asm volatile("" : "+s"(w), "+s"(ab), "+s"(pub1), "+s"(pub2), "+s"(pub3), "+s"(pub4), "+s"(pub5), "+s"(gat1), "+s"(gat2), "+s"(gat3), "+s"(gat4), "+s"(gat5), "+s"(gatc), "+s"(mko), "+s"(cls), "+s"(slo), "+s"(wofs), "+s"(w1ofs), "+s"(w2ofs), "+s"(b1ofs));
    const int lay_ = lay;
    {
      v4u AF[8];
      const v4u Q0 = lds16(qa, 0), Q1 = lds16(qa, 256);
      SB();
#pragma unroll
      for (int T = 0; T < 4; ++T) S[T] = smf(Q0, K8[2 * T], S[T], qidx);
#pragma unroll
      for (int T = 0; T < 4; ++T) S[T] = smf(Q1, K8[2 * T + 1], S[T], qidx);
      float s0, s1;
      asm("s_nop 7\n\t"
          "v_add_f32_dpp %0, %2, %3 quad_perm:[0,1,2,3] row_mask:0x1 bank_mask:0xf\n\tv_add_f32_dpp %1, %4, %5 quad_perm:[0,1,2,3] row_mask:0x1 bank_mask:0xf\n\t"
          "v_add_f32_dpp %0, %6, %7 quad_perm:[0,1,2,3] row_mask:0x2 bank_mask:0xf\n\tv_add_f32_dpp %1, %8, %9 quad_perm:[0,1,2,3] row_mask:0x2 bank_mask:0xf\n\t"
          "v_add_f32_dpp %0, %10, %11 quad_perm:[0,1,2,3] row_mask:0x4 bank_mask:0xf\n\tv_add_f32_dpp %1, %12, %13 quad_perm:[0,1,2,3] row_mask:0x4 bank_mask:0xf\n\t"
          "v_add_f32_dpp %0, %14, %15 quad_perm:[0,1,2,3] row_mask:0x8 bank_mask:0xf\n\tv_add_f32_dpp %1, %16, %17 quad_perm:[0,1,2,3] row_mask:0x8 bank_mask:0xf"
          : "=&v"(s0), "=&v"(s1)
          : "v"(S[0].x), "v"(S[0].y), "v"(S[0].z), "v"(S[0].w), "v"(S[1].x), "v"(S[1].y), "v"(S[1].z), "v"(S[1].w),
            "v"(S[2].x), "v"(S[2].y), "v"(S[2].z), "v"(S[2].w), "v"(S[3].x), "v"(S[3].y), "v"(S[3].z), "v"(S[3].w));
      auto sw = __builtin_amdgcn_permlane32_swap(asu(s0), asu(s1), false, false);
      float mx;
      asm("v_max_f32 %0, %1, %2\n\ts_nop 1\n\tv_max_f32_dpp %0, %0, %0 quad_perm:[1,0,3,2] row_mask:0xf bank_mask:0xf\n\ts_nop 1\n\t"
          "v_max_f32_dpp %0, %0, %0 quad_perm:[2,3,0,1] row_mask:0xf bank_mask:0xf\n\ts_nop 1\n\tv_max_f32_dpp %0, %0, %0 row_ror:4 row_mask:0xf bank_mask:0xf\n\ts_nop 1\n\t"
          "v_max_f32_dpp %0, %0, %0 row_ror:8 row_mask:0xf bank_mask:0xf\n\ts_nop 1\n\tv_max_f32_dpp %0, %0, %0 row_bcast:15 row_mask:0xa bank_mask:0xf\n\ts_nop 0"
          : "=&v"(mx) : "v"(asf(sw[0])), "v"(asf(sw[1])));
      const float m0 = asf(__builtin_amdgcn_readlane(asu(mx), 31)), m1 = asf(__builtin_amdgcn_readlane(asu(mx), 63));
      const float p0 = __builtin_amdgcn_exp2f(s0 - m0), p1 = __builtin_amdgcn_exp2f(s1 - m1);
      const unsigned pb = pk(p0, p1);
      *(LAS u16*)(pw) = (u16)pb; *(LAS u16*)(pw + 20) = (u16)pb; *(LAS u16*)(pw + 32) = (u16)(pb >> 16); *(LAS u16*)(pw + 52) = (u16)(pb >> 16);
#pragma unroll
      for (int m = 0; m < 8; ++m) AF[m] = lds16(pr, m * 272);
      SB();
      auto sl2 = __builtin_amdgcn_permlane32_swap(asu(p0), asu(p1), false, false);
      float ls = asf(sl2[0]) + asf(sl2[1]);
      ls += dppf<XOR1>(ls); ls += dppf<XOR2>(ls); ls += dppf<ROR4>(ls); ls += dppf<ROR8>(ls);
      ls += asf((unsigned)__builtin_amdgcn_update_dpp(0, (int)asu(ls), 0x142, 0xa, 0xf, false));
      if ((lane0 & 31) == 31) *(LAS f2*)mlw = f2{mx, ls};
      SB();
#pragma unroll
      for (int m = 0; m < 8; ++m) O = smf(AF[m], V8[m], O, pidx);
      *(LAS v2u*)aow = pk4(O);
    }
    bar();
    STAMPW(0, 8 + lay * 16);
#define XBPTR() lptr xb0 = L + L_XB + (col < 4 ? col : (col == 8 ? 4 : 5)) * XBS + hq * 16, xb1 = L + L_XB + ((col >= 4 && col < 8) ? col - 4 : (col == 12 ? 4 : 5)) * XBS + hq * 16; PINI(xb0); PINI(xb1)
#define STPTR() lptr sra = L + L_XB + (pp == 0 ? 4 : (pp == 1 ? q4 : 5)) * XBS + hq * 16, srb = L + L_XB + pp * XBS + hq * 16; int sso = col == hq ? col * 16 : 0x40000000; PINI(sra); PINI(srb); PINI(sso)
#define STATS(off) do { const unsigned tm_ = now(); const v4u a0 = lds16(sra, 0), a1 = lds16(sra, 64), b0 = lds16(srb, 0), b1 = lds16(srb, 64); SB(); \
      f4 d_ = mfma(a0, b0, Z4); d_ = mfma(a1, b1, d_); stx(rsW, v4u{cl(asu(d_.x)), cl(asu(d_.y)), tm_, 0u}, sso, off); } while (0)
#define STAT1 (pub1 + (((cls >> 16) & 15) < 8 ? 4 * BLK + 4096 - ((cls >> 16) & 15) * 896 : 4096 + 1024))
#define P1PREP() v4u A1[4]; int st1 = col < 8 ? pp * (((cls >> 16) & 15) < 8 ? BLK : 1024) + (8 * w + 4 * q4 + hq) * 16 : 0x40000000, sp1 = pub1; XBPTR(); \
      { const int base = L_WQ + (w >> 1) * 8192 + a128, t0 = 2 * (w & 1); \
        A1[0] = trfrag(L, base + (t0 ^ z128) * 32, 512); A1[1] = trfrag(L, base + (t0 ^ z128) * 32 + 4096, 512); \
        A1[2] = trfrag(L, base + ((t0 + 1) ^ z128) * 32, 512); A1[3] = trfrag(L, base + ((t0 + 1) ^ z128) * 32 + 4096, 512); } \
      PIN4(A1); PINI(st1); PINS(sp1)
#define P1RUN() do { if (hasP1) { const v4u b00 = lds16(xb0, 0), b01 = lds16(xb0, 64), b10 = lds16(xb1, 0), b11 = lds16(xb1, 64); SB(); \
        f4 d_ = mfma(A1[0], b00, Z4); d_ = mfma(A1[1], b01, d_); d_ = mfma(A1[2], b10, d_); d_ = mfma(A1[3], b11, d_); stx(rsW, split4(d_), st1, sp1); } } while (0)
#define CSQLOAD() const int ve = hq * X1_MSG + (16 * (w & 3) + col) * 16 + 4096;     \
      const v4u c0 = ldx(rsW, ve, gatc), c1 = ldx(rsW, ve + 4 * X1_MSG, gatc), c2 = ldx(rsW, ve + (8 - hq) * X1_MSG, gatc)
#define CSQSUM() do { const unsigned one_ = (lane & 1) ? 0x3F800000u : 0x00003F80u; \
      const v4u sel4 = {(col & 2) ? 0u : one_, (col & 2) ? one_ : 0u, (col & 2) ? 0u : one_, (col & 2) ? one_ : 0u}; \
      csq = mfma(sel4, c0, Z4); csq = mfma(sel4, c1, csq); csq = mfma(hq == 0 ? sel4 : v4u{0, 0, 0, 0}, c2, csq); } while (0)
    if (w < 6) {
      int ymsg;
      if (lay == 0) { FRESH(); b1v = *(const unsigned*)(p.b1 + b1ofs + HFO); PINI(b1v); }
      if (w < 4) {
        FRESH(); A128();
        int so = gat2 + xs, vm = hq * X2_MSG + 512 + (col >> 3) * 16;
        ymsg = col < 4 ? (4 * (2 * w + (hq >> 1)) + pp) * 16 + (hq & 1) * 8 : 0x40000000;
        PINS(so); PINI(vm); PINI(ymsg);
        int spin = 0;
        if (hasP3) {
          int vd = hq * X2_MSG + col * 16, sp = pub3 + xs;
          lptr p3a = L + L_WO + a128 + (w ^ z128) * 32, p3b = L + L_OB + pp * OBS + hq * 16;
          v4u pm0 = col < 8 ? pm : v4u{0, 0, 0, 0}, pm1 = col < 8 ? v4u{0, 0, 0, 0} : pm;
          lptr ow = L + L_OB + w * OBS + ((hq >> 1) * 128 + 8 * col + 4 * (hq & 1)) * 2;
          PINI(vd); PINS(sp); PINI(p3a); PINI(p3b); PINI(pm0); PINI(pm1); PINI(ow);
          v4u A[8];
#pragma unroll
          for (int k = 0; k < 8; ++k) A[k] = trfrag(p3a, k * 4096, 512);
          if (lay == 0 || !SCHED) __builtin_amdgcn_s_sleep(D_X2); else LOOKAT(T2B);
          f4 o;
          for (;; ++spin) {
            OPAQUE();
            const v2u ml = ldx2(rsW, vm, so);
            SB();
            const v4u d0 = ldx(rsW, vd, so), d1 = ldx(rsW, vd + 256, so);
            SB();
            const float m = asf(ml.x), M = max32(max16(m));
            const float f = __builtin_amdgcn_exp2f(m - M);
            const float Ls = add32(add16(f * asf(ml.y)));
            const float fs = f * __builtin_amdgcn_rcpf(Ls);
            const unsigned gg = pk(fs, fs);
            o = mfma(pm0 & gg, d0, Z4); o = mfma(pm1 & gg, d1, o);
            if (!any(o.x != o.x)) break;
            if (!any(max(max(mx4(d0), mx4(d1)), max(ml.x, ml.y)) == SENT) || spin > SPIN_CAP) break;
          }
          OPAQUE();
          *(LAS v2u*)ow = pk4(o);
          LATE(T2B);
          STAMPW(0, 10 + lay * 16);
          bar();
          STAMPW(0, 11 + lay * 16);
          v4u B[8];
#pragma unroll
          for (int k = 0; k < 8; ++k) B[k] = lds16(p3b, k * 64);
          if (lay == 0) {
#pragma unroll
            for (int k = 0; k < 8; ++k) A[k] = trfrag(p3a, k * 4096, 512);
          }
          SB();
          f4 d = Z4;
#pragma unroll
          for (int k = 0; k < 8; ++k) d = mfma(A[k], B[k], d);
          const v2u yv = pack4(d);
          stx2(rsW, yv, ymsg, sp);
          if (((cls >> 4) & 15) == 8) stx2(rsW, yv, ymsg, sp ^ HALF);
          STAMPW(0, 20 + lay * 16);
        } else bar();
        if (w == 0) COUNT(42 + lay * 8, spin);
      } else {
        ymsg = 0;
        if (lay == 0) {
          FRESH();
          unsigned sn = SENT; PINI(sn);
          const v4u SENT4 = {sn, sn, sn, sn};
          const int lo = lane * 16, r1 = (cls >> 16) & 15;
          __builtin_amdgcn_s_sleep(D_W);
          if (hasP3 && !hasP1) __builtin_amdgcn_s_sleep(D_X2 + 2 - D_W);
          if (hasP3 && hasP1) {
            const rsrc_t ro = mkrs(p.wo, QN * DM * 2);
#pragma unroll
            for (int i = 0; i < 16; ++i) dma128(lane, ro, DM * 2, wofs + 128 * (w - 4) * (DM * 2), ldsb + L_WO + (w - 4) * 16384, i);
          }
          if (w == 4) {
            if (hasF) {
              const int a0 = (pub4 + PG) ^ HALF;
              stx(rsW, SENT4, lo, a0); stx(rsW, SENT4, lo, a0 + 1024); stx(rsW, SENT4, lo, a0 + 2048);
              if (lane < 8) stx(rsW, SENT4, lo, a0 + 3840);
              if (lane < 32) { stx(rsW, SENT4, lo, pub5 + SM_STRIDE); if (((cls >> 12) & 15) == 8) stx(rsW, SENT4, lo, (pub5 + SM_STRIDE) ^ HALF); }
            }
            if (lane < 40) stx(rsW, SENT4, lo, pub2 + SM_STRIDE);
          } else {
            if (hasP1) {
              const int rs = r1 < 8 ? BLK : 1024;
              stx(rsW, SENT4, lo, pub1); stx(rsW, SENT4, lo, pub1 + rs); stx(rsW, SENT4, lo, pub1 + 2 * rs); stx(rsW, SENT4, lo, pub1 + 3 * rs);
              if (lane < 8) stx(rsW, SENT4, lo, STAT1);
            }
            if (hasP3 && lane < 32) { stx(rsW, SENT4, lo, pub3 + SM_STRIDE); if (((cls >> 4) & 15) == 8) stx(rsW, SENT4, lo, (pub3 + SM_STRIDE) ^ HALF); }
          }
          if (hasF) {
            const rsrc_t r = mkrs(p.w1, DM * FF * 2), r2 = mkrs(p.w2, FF * DM * 2);
            const int vo = (lane >> 2) * (FF * 2) + ((lane & 3) ^ (2 * (lane >> 5))) * 16;
#pragma unroll
            for (int i = 0; i < 12; ++i) { const int t = 3 * (w - 4) + (i >> 2); dma(r, vo, w1ofs + 16 * (i & 3) * (FF * 2) + t * 64, ldsb + L_W1 + t * 4096 + (i & 3) * 1024); }
            asm volatile("s_waitcnt vmcnt(12)" ::: "memory");
            bar();
            for (int sp2 = 0; *(volatile LAS unsigned*)(L + L_TM + 4) == 0u && sp2 < (SPIN_CAP << 4); ++sp2) __builtin_amdgcn_s_sleep(1);
#pragma unroll
            for (int i = 0; i < 12; ++i) dma128(lane, r2, DM * 2, w2ofs + 96 * (w - 4) * (DM * 2), ldsb + L_W2 + (w - 4) * 12288, i);
            asm volatile("s_waitcnt vmcnt(12)" ::: "memory");
          } else { drain(); bar(); }
        } else bar();
      }
      int spin = 0;
      if (hasF) {
        v4u A[6];
        lptr p5a, p5b;
        {
          FRESH();
          XBPTR();
          const int s0 = (hq & 1) * 32;
          lptr p4a0 = L + L_W1 + w * 4096 + hq * 512 + q4 * 64 + pp * 8 + s0, p4a1 = p4a0 + 32 - 2 * s0;
          int st4 = col < 8 ? w * 512 + (16 * q4 + 4 * hq + pp) * 16 : ((lay == 0 && pp == 0) ? 3072 + w * 128 + (col - 8 + hq) * 16 : 0x40000000), sp = (pub4 + (x4 & ~HALF)) ^ (x4 & HALF);
          v4u A4[4] = {trfrag(p4a0, 0, 256), trfrag(p4a0, 2048, 256), trfrag(p4a1, 0, 256), trfrag(p4a1, 2048, 256)};
          PINI(st4); PINS(sp); PINI(p4a0); PINI(p4a1); PIN4(A4);
          bar();
          STAMPW(0, 13 + lay * 16);
          const v4u b00 = lds16(xb0, 0), b01 = lds16(xb0, 64), b10 = lds16(xb1, 0), b11 = lds16(xb1, 64);
          if (lay == 0) { A4[0] = trfrag(p4a0, 0, 256); A4[1] = trfrag(p4a0, 2048, 256); A4[2] = trfrag(p4a1, 0, 256); A4[3] = trfrag(p4a1, 2048, 256); }
          SB();
          f4 d = mfma(A4[0], b00, Z4); d = mfma(A4[1], b01, d); d = mfma(A4[2], b10, d); d = mfma(A4[3], b11, d);
          stx(rsW, split4(d), st4, sp);
          STAMPW(0, 14 + lay * 16);
        }
        FRESH(); A128();
        const int pn = lane >> 2;
        const int vv = 2 * (q4 & 1) + (col & 1);
        const unsigned on = ((col >> 3) == (hq >> 1) && (col & 2) == 0) ? ((vv & 1) ? 0x3F800000u : 0x3F80u) : 0u;
        v4u selh = {(vv & 2) ? 0u : on, (vv & 2) ? on : 0u, (vv & 2) ? 0u : on, (vv & 2) ? on : 0u};
        v4u selh0 = (hq & 1) ? v4u{0, 0, 0, 0} : selh;
        const int pc = pn < 9 ? pn : 8;
        int so = (gat4 + (x4 & ~HALF)) ^ (x4 & HALF), vs = (pc >> 1) * BLK + (pc & 1) * 4096 + 3840 + pp * 16, sp5 = pub5 + xs;
        float sm = pn < 9 ? (1.f / DM) : 0.f;
        lptr hw = L + L_HB + pp * HBS + HFO * 2;
        int vo4 = 4 * BLK + w * 512 + (16 * (hq >> 1) + col) * 16, vo = vo4 - 4 * BLK + (hq & 1) * 4096;
        int sa = so + BLK, sb = so + 2 * BLK, sc = so + 3 * BLK;
        int ve1 = 4 * BLK + 3072 + w * 128 + (4 * (hq >> 1) + q4) * 16, ve0 = ve1 + (2 * (hq & 1) + (pp >> 1) - 4) * BLK + (pp & 1) * 4096;
        p5a = L + L_W2 + a128 + ((w & 3) ^ z128) * 32; p5b = L + L_HB + pp * HBS + hq * 16;
        f2 bb = {blo(b1v), bhi(b1v)};
        PINS(so); PINS(sp5); PINI(selh); PINI(selh0); PINI(vs); PINI(sm); PINI(hw); PINI(vo); PINI(vo4); PINS(sa); PINS(sb); PINS(sc); PINI(ve0); PINI(ve1); PINI(p5a); PINI(p5b); PINI(bb);
#pragma unroll
        for (int k = 0; k < 6; ++k) A[k] = trfrag(p5a, k * 4096, 512);
        if (lay == 0) __builtin_amdgcn_s_sleep(D_X4A); else if (SCHED) LOOKAT(T4B); else __builtin_amdgcn_s_sleep(D_X4);
        f4 a; float rstd; f2 t;
        for (;; ++spin) {
          OPAQUE();
          const v2u st = ldx2(rsW, vs, so);
          v4u e0 = {0, 0, 0, 0}, e1 = e0;
          if (lay == 0) { e0 = ldx(rsW, ve0, so); e1 = ldx(rsW, ve1, so); }
          const v4u d0 = ldx(rsW, vo, so), d1 = ldx(rsW, vo, sa), d2 = ldx(rsW, vo, sb), d3 = ldx(rsW, vo, sc), d4 = ldx(rsW, vo4, so);
          SB();
          float s1 = asf(st.x) * sm, s2 = asf(st.y) * sm;
          s1 += dppf<ROR4>(s1); s2 += dppf<ROR4>(s2); s1 += dppf<ROR8>(s1); s2 += dppf<ROR8>(s2);
          s1 = add32(add16(s1)); s2 = add32(add16(s2));
          rstd = __builtin_amdgcn_rsqf(fmaxf(s2 - s1 * s1, 0.f) + 1e-5f);
          if (lay == 0) {
            f4 e = mfma(selh, e0, Z4); const f4 e8 = mfma(selh0, e1, Z4);
            e.x += dppf<XOR1>(e.x); e.y += dppf<XOR1>(e.y); e.x += dppf<XOR2>(e.x); e.y += dppf<XOR2>(e.y);
            cs1 = f2{e.x + e8.x, e.y + e8.y};
          }
          t = bb - (s1 * rstd) * cs1;
          SB();
          a = mfma(selh, d0, Z4); a = mfma(selh, d1, a); a = mfma(selh, d2, a); a = mfma(selh, d3, a); a = mfma(selh0, d4, a);
          if (!any(a.x != a.x || t.x != t.x)) break;
          if (!any(max(max(max(max(mx4(d0), mx4(d1)), max(mx4(d2), mx4(d3))), max(mx4(d4), max(st.x, st.y))), max(mx4(e0), mx4(e1))) == SENT) || spin > SPIN_CAP) break;
        }
        OPAQUE();
        STAMPW(0, 15 + lay * 16);
        const f2 v = f2{a.x, a.y} * rstd + t;
        const f2 e = v * (v * v * (-2.f * 0.7978845608028654f * 1.4426950408889634f * 0.044715f) + (-2.f * 0.7978845608028654f * 1.4426950408889634f));
        const float h0 = v.x * __builtin_amdgcn_rcpf(1.f + __builtin_amdgcn_exp2f(e.x)), h1 = v.y * __builtin_amdgcn_rcpf(1.f + __builtin_amdgcn_exp2f(e.y));
        *(LAS unsigned*)hw = pk(h0, h1);
        LATE(T4B);
        bar();
        STAMPW(0, 16 + lay * 16);
        if (w < 4) {
          v4u B[6];
#pragma unroll
          for (int k = 0; k < 6; ++k) B[k] = lds16(p5b, k * 64);
          if (lay == 0) {
#pragma unroll
            for (int k = 0; k < 6; ++k) A[k] = trfrag(p5a, k * 4096, 512);
          }
          SB();
          f4 d = Z4;
#pragma unroll
          for (int k = 0; k < 6; ++k) d = mfma(A[k], B[k], d);
          const v2u zv = pack4(d);
          stx2(rsW, zv, ymsg, sp5);
          if (((cls >> 12) & 15) == 8) stx2(rsW, zv, ymsg, sp5 ^ HALF);
          STAMPW(0, 21 + lay * 16);
        }
      } else {
        bar();
        bar();
      }
      if (w == 0) COUNT(44 + lay * 8, spin);
      if (lay == 1) return;
      FRESH(); A128();
      P1PREP(); STPTR();
      if (w >= 4) {
        const unsigned tn = now();
        CSQLOAD(); CSQSUM(); PINI(csq);
        if (w == 5) {
          __builtin_amdgcn_s_sleep(D_M);
          int dm;
          for (int sp2 = 0;; ++sp2) {
            OPAQUE();
            const unsigned m = __builtin_amdgcn_raw_buffer_load_b32(rsW, ((slo >> 6) * 24 + (lane < 24 ? lane : 0)) * 128, WS_MARK, AUX_SC1);
            dm = (int)((m - tn) << 1);
            const bool bad = dm > 1600 || dm < -1600;
            if (bad) dm = -1600;
            if (!any(bad) || sp2 > 8) break;
          }
          dm = max(dm, (int)dppu<XOR1>(dm)); dm = max(dm, (int)dppu<XOR2>(dm)); dm = max(dm, (int)dppu<ROR4>(dm)); dm = max(dm, (int)dppu<ROR8>(dm));
          dm = max(__builtin_amdgcn_readlane(dm, 0), __builtin_amdgcn_readlane(dm, 16));
          if (lane == 0) *(LAS unsigned*)(L + L_TM) = (tn + (unsigned)(dm >> 1)) & 0x7FFFFFFFu;
        }
      }
      bar();
      STAMPW(0, 18);
      P1RUN();
      t0 = __builtin_amdgcn_readfirstlane(*(LAS unsigned*)(L + L_TM));
      if (w == 3 && hasP1) STATS(STAT1);
      asm volatile(".Lfwd_end:");
      STAMPW(0, 19);
    } else {
      {
        const unsigned tm = now();
        const f2 ml0 = *(LAS f2*)mmr, ml1 = *(LAS f2*)(mmr + 64);
        const v4u B0 = lds16(mbr, 0), B1 = lds16(mbr, 2048);
        const int so = pub2 + xs;
        SB();
        const float M = max32(max16(vmax(ml0.x, ml1.x)));
        const float f0 = __builtin_amdgcn_exp2f(ml0.x - M), f1 = __builtin_amdgcn_exp2f(ml1.x - M);
        const unsigned h0 = pk(f0, f0), h1 = pk(f1, f1);
        f4 o = mfma(pm & h0, B0, Z4); o = mfma(pm & h1, B1, o);
        const float Ls = add32(add16(blo(h0) * ml0.y + blo(h1) * ml1.y));
        if (lane0 == 0) stx(rsW, v4u{cl(asu(M)), cl(asu(Ls)), tm, 0u}, 512 + (w & 1) * 16, so);
        stx2(rsW, pack4(o), mst, so);
        STAMPW(6, 9 + lay * 16);
      }
      FRESH(); A128(); SEL8(); STPTR();
      if (lay == 0) { xp = *(const v2u*)(p.x + pp * DM + slo + TKO); b2v = *(const v2u*)(p.b2 + slo + TKO); PINI(xp); PINI(b2v); }
      v4u s8 = sel8;
      int v0 = hq * 512 + (w - 6) * 256 + col * 16;
      lptr xw = L + L_XB + pp * XBS + TKO * 2;
      {
        int so = gat3 + xs, so1 = so + 4096, sp = ((pub4 + (x4 & ~HALF)) ^ (x4 & HALF)) + 3840;
        f4 xr = unp(xp);
        PINS(so); PINS(so1); PINS(sp); PINI(s8); PINI(v0); PINI(xw); PINI(xr);
        if (lay == 0) {
          const unsigned tb = now();
          if (w == 7 && ((cls >> 20) & 15) == 15) {
            __builtin_amdgcn_s_sleep(D_X2 - 10);
            for (int sp2 = 0;; ++sp2) {
              OPAQUE();
              const unsigned t = __builtin_amdgcn_raw_buffer_load_b32(rsW, (col >> 3) * BLK + (col & 7) * X2_MSG + 512 + 16 + 8, gat2 - BLK - 4 * X2_MSG, AUX_SC1);
              if (!any(t == SENT) || sp2 > SPIN_CAP) break;
            }
            if (lane0 == 0) __builtin_amdgcn_raw_buffer_store_b32(now(), rsW, 0, WS_HER + (cls >> 24) * 128, AUX_SC1);
          }
          bar();
          for (int sp2 = 0; sp2 < 64; ++sp2) {
            OPAQUE();
            const unsigned t = __builtin_amdgcn_raw_buffer_load_b32(rsW, col * 128, WS_HER, AUX_SC1);
            if (!any(((t - tb + 800u) & 0x7FFFFFFFu) > 4000u || t > 0x7FFFFFFFu)) break;
          }
          STAMPW(6, 38);
        } else bar();
        if (lay == 0) {} else if (SCHED) LOOKAT(T3B); else { __builtin_amdgcn_s_sleep(D_X3); if (!hasP3) __builtin_amdgcn_s_sleep(36); }
        f4 y;
        int spin = 0;
        for (;; ++spin) {
          OPAQUE();
          const v4u d0 = ldx(rsW, v0, so), d1 = ldx(rsW, v0 + 2048, so), d2 = ldx(rsW, v0, so1), d3 = ldx(rsW, v0 + 2048, so1);
          if (lane0 == 0) *(volatile LAS unsigned*)(L + L_TM + 4) = 1u;
          SB();
          y = mfma(s8, d0, Z4); y = mfma(s8, d1, y); y = mfma(s8, d2, y); y = mfma(s8, d3, y);
          if (!any(y.x != y.x)) break;
          if (!any(max(max(mx4(d0), mx4(d1)), max(mx4(d2), mx4(d3))) == SENT) || spin > SPIN_CAP) break;
        }
        OPAQUE();
        if (w == 6) COUNT(43 + lay * 8, spin);
        STAMPW(6, 12 + lay * 16);
        xp = pk4(xr + unp(pk4(y)));
        if (lane0 < 32) *(LAS v2u*)xw = xp;
        LATE(T3B);
        bar();
        if (w == 6) { if (hasF) STATS(sp); }
        else if (lay == 0 && c == 0 && lane0 == 0) stx2(rsW, *(LAS v2u*)(L + L_TAG + 8), 0, WS_EPOCH);
      }
      if (lay == 1 && !isOut) { bar(); return; }
      int so = gat5 + xs, so1 = so + 4096, so2 = so + BLK;
      int oo = hq < 2 ? (pp * DM + slo + TKO) * 2 : 0x40000000;
      f4 xr = unp(xp), bb = unp(b2v);
      P1PREP();
      PINS(so); PINS(so1); PINS(so2); PINI(xr); PINI(bb); PINI(oo);
      unsigned a4 = 0;
      if (lay == 0) {
        CSQLOAD(); CSQSUM(); PINI(csq);
        if (w == 7) {
          unsigned t;
          for (int sp2 = 0;; ++sp2) { OPAQUE(); t = __builtin_amdgcn_raw_buffer_load_b32(rsW, ((col < 9 ? col : 8) >> 1) * BLK + (col < 9 ? col & 1 : 0) * 4096 + 3840 + 8, gat4, AUX_SC1); if (!any(t == SENT) || sp2 > SPIN_CAP) break; }
          a4 = tmax16(t) + N45;
          if (!hasF) until(a4);
        }
      }
      bar();
      if (lay == 0 && w == 7 && hasF) {
        const unsigned tn = now();
        if (lane0 == 0) __builtin_amdgcn_raw_buffer_store_b32((int)((tn - a4) << 1) > 0 ? tn : a4 & 0x7FFFFFFFu, rsW, 0, mko, AUX_SC1);
      }
      if (lay == 0) __builtin_amdgcn_s_sleep(D_X5A); else if (SCHED) LOOKAT(T5B); else { __builtin_amdgcn_s_sleep(D_X5); if (!hasF) __builtin_amdgcn_s_sleep(44); }
      f4 y;
      int spin = 0;
      for (;; ++spin) {
        OPAQUE();
        const v4u d0 = ldx(rsW, v0, so), d1 = ldx(rsW, v0 + 2048, so), d2 = ldx(rsW, v0, so1), d3 = ldx(rsW, v0 + 2048, so1), d4 = ldx(rsW, v0, so2), d5 = ldx(rsW, v0 + 2048, so2);
        SB();
        y = mfma(s8, d0, Z4); y = mfma(s8, d1, y); y = mfma(s8, d2, y); y = mfma(s8, d3, y); y = mfma(s8, d4, y); y = mfma(s8, d5, y);
        if (!any(y.x != y.x)) break;
        if (!any(max(max(max(mx4(d0), mx4(d1)), max(mx4(d2), mx4(d3))), max(mx4(d4), mx4(d5))) == SENT) || spin > SPIN_CAP) break;
      }
      OPAQUE();
      if (w == 6) COUNT(45 + lay * 8, spin);
      STAMPW(6, 17 + lay * 16);
      xp = pk4(xr + unp(pk4(unp(pk4(y)) + bb)));
      if (lay == 1) {
        __builtin_amdgcn_raw_buffer_store_b64(xp, mkrs(p.out, 4 * DM * 2), oo, 0, OUT_AUX);
        STAMPW(6, 34);
        if (w == 6) { COUNT(61, __builtin_amdgcn_s_memtime()); }
        return;
      }
      if (lane0 < 32) *(LAS v2u*)xw = xp;
      bar();
      P1RUN();
      t0 = __builtin_amdgcn_readfirstlane(*(LAS unsigned*)(L + L_TM));
    }
#pragma unroll
    for (int T = 0; T < 4; ++T) S[T] = Z4;
    O = Z4;
    PIN4(S); PINI(O);
    if (w >= 4) {
      FRESH();
      const unsigned one = (lane & 1) ? 0x3F800000u : 0x00003F80u;
      v4u sel4 = {(col & 2) ? 0u : one, (col & 2) ? one : 0u, (col & 2) ? 0u : one, (col & 2) ? one : 0u};
      v4u sel40 = hq == 0 ? sel4 : v4u{0, 0, 0, 0};
      int so = gat1 + ab * BLK, so1 = so + 4096, so2 = gat1 + 4 * BLK + ab * 1024, so3 = gat1 + 4 * BLK + 4096 + ab * 16;
      int v2 = (16 * (w - 4) + col) * 16, v0 = v2 + hq * 1024, vs = (col < 9 ? col : 8) * 128;
      float sm = col < 9 ? (1.f / DM) : 0.f;
      lptr qw = L + QPW(w - 4);
      PINS(so); PINS(so1); PINS(so2); PINS(so3); PINI(sel4); PINI(sel40); PINI(v0); PINI(v2); PINI(vs); PINI(qw); PINI(sm);
      const int lay_ = 1;
      if (SCHED) until(t0 + T1B); else __builtin_amdgcn_s_sleep(D_X1);
      f4 d, cc; float rs;
      int spin = 0;
      for (;; ++spin) {
        OPAQUE();
        const v2u st = ldx2(rsW, vs, so3);
        SB();
        const v4u d0 = ldx(rsW, v0, so), d1 = ldx(rsW, v0, so1), d2 = ldx(rsW, v2, so2);
        SB();
        float s1 = asf(st.x) * sm, s2 = asf(st.y) * sm;
        s1 += dppf<XOR1>(s1); s2 += dppf<XOR1>(s2); s1 += dppf<XOR2>(s1); s2 += dppf<XOR2>(s2);
        SB();
        d = mfma(sel4, d0, Z4); d = mfma(sel4, d1, d); d = mfma(sel40, d2, d);
        SB();
        s1 += dppf<ROR4>(s1); s2 += dppf<ROR4>(s2); s1 += dppf<ROR8>(s1); s2 += dppf<ROR8>(s2);
        rs = __builtin_amdgcn_rsqf(fmaxf(s2 - s1 * s1, 0.f) + 1e-5f) * CS;
        cc = (-s1 * rs) * csq;
        d = d * rs + cc;
        if (!any(d.x != d.x)) break;
        if (!any(max(max(max(mx4(d0), mx4(d1)), mx4(d2)), max(st.x, st.y)) == SENT) || spin > SPIN_CAP) break;
      }
      OPAQUE();
      LATE(T1B);
      if (w == 4) COUNT(49, spin);
      STAMPW(4, 22);
      if (lane0 < 16) { LAS unsigned* qp = (LAS unsigned*)qw; qp[0] = pk(d.x, d.y); qp[16] = pk(d.z, d.w); }
    }
    bar();
    STAMPW(0, 23);
  }
}

extern "C" void kernel_launch(void* const* d_in, const int* in_sizes, int n_in, void* d_out, int out_size, void* d_ws, size_t ws_size, hipStream_t stream) {
  Params p;
  p.x = (const u16*)d_in[0]; p.kc = (const u16*)d_in[1]; p.vc = (const u16*)d_in[2]; p.wq = (const u16*)d_in[3]; p.wo = (const u16*)d_in[4];
  p.w1 = (const u16*)d_in[6]; p.b1 = (const u16*)d_in[7]; p.w2 = (const u16*)d_in[8]; p.b2 = (const u16*)d_in[9];
  p.out = (u16*)d_out; p.ws = (unsigned char*)d_ws;
  (void)hipFuncSetAttribute((const void*)fused_fwd, hipFuncAttributeMaxDynamicSharedMemorySize, L_BYTES);
  hipLaunchKernelGGL(fused_fwd, dim3(256), dim3(512), L_BYTES, stream, p);
}
```

```cpp
#include <hip/hip_runtime.h>
#include <stdint.h>

#define EVT 1
#define STAMPS 0

typedef unsigned short u16;
typedef unsigned long long u64;
typedef unsigned v4u __attribute__((ext_vector_type(4)));
typedef unsigned v8u __attribute__((ext_vector_type(8)));
typedef unsigned v2u __attribute__((ext_vector_type(2)));
typedef float f4 __attribute__((ext_vector_type(4)));
typedef float f2 __attribute__((ext_vector_type(2)));
typedef __bf16 bf2 __attribute__((ext_vector_type(2)));
typedef __bf16 bf8 __attribute__((ext_vector_type(8)));
typedef __bf16 bf16v __attribute__((ext_vector_type(16)));
typedef short s4v __attribute__((ext_vector_type(4)));
#define LAS __attribute__((address_space(3)))
#define DI __device__ __forceinline__
typedef __amdgpu_buffer_rsrc_t rsrc_t;

constexpr int DM = 576, SK = 8192, WIN = 2048, DH = 128, FF = 4608, QN = 4096;
constexpr unsigned SENT = 0xFFFFFFFFu;
constexpr int AUX_NT = 2, AUX_SC1 = 16;

constexpr int PG = 2 << 20, BLK = 16384, HALF = 8192;
constexpr int WS_EPOCH = 0, WS_FLAGS = 4096, WS_MARK = 8192, WS_HER = 40960, X1A_OFF = 65536, X1_MSG = 5376;
constexpr int X2_MSG = 640, X2_OFF = PG, X3_OFF = PG + 32 * BLK, X5_OFF = PG + 41 * BLK, SM_STRIDE = 60 * BLK;
constexpr int X4_OFF = 2 * PG;
constexpr int X1_OFF = 4 * PG;
constexpr int WS_USED = 5 * PG;
static_assert(X1A_OFF + 144 * X1_MSG <= PG && 2 * SM_STRIDE <= PG && 120 * BLK <= PG, "pages");
constexpr int WS_STAMP = 16 << 20;

constexpr int L_WQ = 0;
constexpr int L_WO = 32768;
constexpr int L_W2 = 65536;
constexpr int L_W1 = 90112;
constexpr int L_PA = 114688;
constexpr int L_AO = L_PA + 8 * 2176;
constexpr int L_OB = L_AO + 4096;
constexpr int L_HB = L_OB + 4 * 528;
constexpr int L_XB = L_HB + 4 * 400;
constexpr int L_QP = L_XB + 6 * 144;
constexpr int L_ML = L_QP + 512;
constexpr int L_LN0 = L_ML + 128;
constexpr int L_RDY = L_LN0 + 32;
constexpr int L_TAG = L_LN0 + 32;
constexpr int L_TM = L_TAG + 16;
constexpr int L_BYTES = L_TM + 16;
constexpr int XBS = 144, OBS = 528, HBS = 400;
static_assert(L_BYTES <= 160 * 1024 && L_OB % 16 == 0 && L_HB % 16 == 0 && L_XB % 16 == 0 && L_QP % 16 == 0 && L_ML % 16 == 0, "lds");

#define D_X1 12
#define D_X2 18
#define D_X3 20
#define D_X4 10
#define D_X5 18
#define I1 170
#define I2 165
#define I3 100
#define I4 100
#define I5 110
#define D_X5A 22
#define D_X4A 12
#define D_M 8
#define N45 130
constexpr unsigned T1B = I1, T2B = T1B + I2, T3B = T2B + I3, T4B = T3B + I4, T5B = T4B + I5;
#define SCHED 1
#define NUMA 1
#define FPOS 19
#define XPOS 3
#define SPOS 14
#define WND 10
#define TP0 2
#define TP1 5
#define TP2 8
#define TP3 11
#define D_W 11
#define PREFI 1
#define OUT_AUX 0
#define SNT 2
constexpr int SPIN_CAP = 1 << 18;

struct Params {
  const u16 *x, *kc, *vc, *wq, *wo, *w1, *b1, *w2, *b2;
  u16* out;
  unsigned char* ws;
};

DI f4 mfma(v4u a, v4u b, f4 c) { return __builtin_amdgcn_mfma_f32_16x16x32_bf16(__builtin_bit_cast(bf8, a), __builtin_bit_cast(bf8, b), c, 0, 0, 0); }
DI f4 smf(v4u a, v8u b, f4 c, int idx) { return __builtin_amdgcn_smfmac_f32_16x16x64_bf16(__builtin_bit_cast(bf8, a), __builtin_bit_cast(bf16v, b), c, idx, 0, 0); }
DI v8u cat(v4u a, v4u b) { return __builtin_shufflevector(a, b, 0, 1, 2, 3, 4, 5, 6, 7); }
DI float asf(unsigned u) { return __builtin_bit_cast(float, u); }
DI unsigned asu(float f) { return __builtin_bit_cast(unsigned, f); }
template <int C> DI unsigned dppu(unsigned x) { return (unsigned)__builtin_amdgcn_mov_dpp((int)x, C, 0xf, 0xf, true); }
template <int C> DI float dppf(float x) { return asf(dppu<C>(asu(x))); }
constexpr int XOR1 = 0xB1, XOR2 = 0x4E, ROR4 = 0x124, ROR8 = 0x128;
DI float add16(float x) { auto s = __builtin_amdgcn_permlane16_swap(asu(x), asu(x), false, false); return asf(s[0]) + asf(s[1]); }
DI float add32(float x) { auto s = __builtin_amdgcn_permlane32_swap(asu(x), asu(x), false, false); return asf(s[0]) + asf(s[1]); }
DI float vmax(float a, float b) { float r; asm("v_max_f32 %0, %1, %2" : "=v"(r) : "v"(a), "v"(b)); return r; }
DI float max16(float x) { auto s = __builtin_amdgcn_permlane16_swap(asu(x), asu(x), false, false); return vmax(asf(s[0]), asf(s[1])); }
DI float max32(float x) { auto s = __builtin_amdgcn_permlane32_swap(asu(x), asu(x), false, false); return vmax(asf(s[0]), asf(s[1])); }
DI unsigned pk(float lo, float hi) { bf2 v = {(__bf16)lo, (__bf16)hi}; return __builtin_bit_cast(unsigned, v); }
DI float rbf(float x) { return (float)(__bf16)x; }
DI float blo(unsigned u) { return asf(u << 16); }
DI float bhi(unsigned u) { return asf(u & 0xffff0000u); }
DI unsigned cl(unsigned u) { return min(u, SENT - 1); }
DI v4u split4(f4 d) {
  const unsigned h0 = pk(d.x, d.y), h1 = pk(d.z, d.w);
  return v4u{cl(h0), cl(h1), cl(pk(d.x - blo(h0), d.y - bhi(h0))), cl(pk(d.z - blo(h1), d.w - bhi(h1)))};
}
DI v2u pack4(f4 d) { return v2u{cl(pk(d.x, d.y)), cl(pk(d.z, d.w))}; }
DI unsigned mx4(v4u v) { return max(max(v.x, v.y), max(v.z, v.w)); }
DI v4u ldx(rsrc_t r, int voff, int soff) { return __builtin_amdgcn_raw_buffer_load_b128(r, voff, soff, AUX_SC1); }
DI v2u ldx2(rsrc_t r, int voff, int soff) { return __builtin_amdgcn_raw_buffer_load_b64(r, voff, soff, AUX_SC1); }
DI void stx(rsrc_t r, v4u v, int voff, int soff) { __builtin_amdgcn_raw_buffer_store_b128(v, r, voff, soff, AUX_SC1); }
DI void stx2(rsrc_t r, v2u v, int voff, int soff) { __builtin_amdgcn_raw_buffer_store_b64(v, r, voff, soff, AUX_SC1); }
DI v4u ldn(rsrc_t r, int voff, int soff) { return __builtin_amdgcn_raw_buffer_load_b128(r, voff, soff, AUX_SC1 | SNT); }
DI void stn(rsrc_t r, v4u v, int voff, int soff) { __builtin_amdgcn_raw_buffer_store_b128(v, r, voff, soff, AUX_SC1 | SNT); }
DI void bar() { asm volatile("s_waitcnt lgkmcnt(0)\n\ts_barrier" ::: "memory"); }
DI void drain() { asm volatile("s_waitcnt vmcnt(0)" ::: "memory"); }
DI rsrc_t mkrs(const void* p, unsigned bytes) { return __builtin_amdgcn_make_buffer_rsrc((void*)p, (short)0, (int)bytes, 0x00020000); }
DI void dma(rsrc_t r, int voff, int soff, unsigned ldsaddr) {
  unsigned keep;
  asm volatile("s_nop 4\n\ts_mov_b32 %0, m0\n\ts_mov_b32 m0, %1\n\ts_nop 0\n\tbuffer_load_dwordx4 %2, %3, %4 offen nt lds\n\ts_mov_b32 m0, %0"
               : "=&s"(keep) : "s"(ldsaddr), "v"(voff), "s"(r), "s"(soff) : "memory");
}
typedef LAS unsigned char* lptr;
DI v4u lds16(lptr L, int off) { return *(LAS v4u*)(L + off); }
#define SB() __builtin_amdgcn_sched_barrier(0)
DI float dot2(unsigned a, unsigned b, float c) { return __builtin_amdgcn_fdot2_f32_bf16(__builtin_bit_cast(bf2, a), __builtin_bit_cast(bf2, b), c, false); }
DI v4u trfrag(LAS unsigned char* L, int off, int step) {
  s4v a = __builtin_amdgcn_ds_read_tr16_b64_v4i16((LAS s4v*)(L + off));
  s4v b = __builtin_amdgcn_ds_read_tr16_b64_v4i16((LAS s4v*)(L + off + step));
  v2u x = __builtin_bit_cast(v2u, a), y = __builtin_bit_cast(v2u, b);
  return v4u{x.x, x.y, y.x, y.y};
}
DI bool any(bool b) { return __builtin_amdgcn_ballot_w64(b) != 0; }
#define OPAQUE() asm volatile("" ::: "memory")
#define Z4 f4{0.f, 0.f, 0.f, 0.f}
#define FRESH() int lane = lane0; asm volatile("" : "+v"(lane)); const int col = lane & 15, hq = lane >> 4, q4 = col >> 2, pp = lane & 3; (void)col; (void)hq; (void)q4; (void)pp
#define SEL8() const unsigned one = (lane & 1) ? 0x3F800000u : 0x00003F80u; \
  const v4u sel8 = {((col & 7) >> 1) == 0 ? one : 0u, ((col & 7) >> 1) == 1 ? one : 0u, ((col & 7) >> 1) == 2 ? one : 0u, ((col & 7) >> 1) == 3 ? one : 0u}
#define A128() const int z128 = ((q4 >> 1) & 1) | ((hq & 1) << 1), a128 = hq * 1024 + q4 * 128 + pp * 8
#define STAMP(i) do {} while (0)
#define STAMPW(ww, i) do {} while (0)
#define COUNT(i, v) do {} while (0)

#define PIN4(a) asm volatile("" : "+v"(a[0]), "+v"(a[1]), "+v"(a[2]), "+v"(a[3]))
#define PINI(x) asm volatile("" : "+v"(x))
#define PINS(x) asm volatile("" : "+s"(x))
#define TKO (32 * (w & 1) + 8 * q4 + 4 * (hq & 1))
#define HFO (32 * w + 16 * (hq >> 1) + 4 * q4 + 2 * (hq & 1))
#define KLOAD(i) K[i] = __builtin_amdgcn_raw_buffer_load_b128(rsK, kvo, __builtin_amdgcn_readfirstlane((((i) + rot) & 15) * 1024), AUX_NT)
#define VLOAD(i) V[i] = __builtin_amdgcn_raw_buffer_load_b128(rsV, vvo, __builtin_amdgcn_readfirstlane((((i) + rot) & 15) * 1024), AUX_NT)
#define QPW(ww) (L_QP + (q4 & 1) * 256 + (((ww) >> 1) * 8 + 2 * ((col >> 1) & 1) + ((ww) & 1)) * 16 + (2 * (col >> 3) + (col & 1)) * 4)
#define TT(i) do { if (STAMPS == 1 || (STAMPS && (i) == 0)) { SB(); tt[i] = __builtin_amdgcn_s_memrealtime(); asm volatile("s_waitcnt lgkmcnt(0)" : "+s"(tt[i])); SB(); } } while (0)
DI unsigned now() { return (unsigned)__builtin_amdgcn_s_memrealtime() & 0x7FFFFFFFu; }
DI void until(unsigned t) { for (int i = 0; i < 4096 && ((t - now()) & 0x7FFFFFFFu) - 1u < 0x3FFFFFFFu; ++i) {} }
DI unsigned tmax16(unsigned t) {
  const unsigned ref = __builtin_amdgcn_readfirstlane(t);
  int d = (int)((t - ref) << 1);
  d = max(d, (int)dppu<XOR1>(d)); d = max(d, (int)dppu<XOR2>(d)); d = max(d, (int)dppu<ROR4>(d)); d = max(d, (int)dppu<ROR8>(d));
  return (ref + (unsigned)(__builtin_amdgcn_readfirstlane(d) >> 1)) & 0x7FFFFFFFu;
}
#define RTTN 50
#define LATE(T) do { if (SCHED && lay_ == 1 && spin > 0 && lane0 == 0) __hip_atomic_fetch_max((LAS int*)(L + L_TM + 12), (int)(((now() - (t0 + (T) + RTTN)) << 1)) >> 1, __ATOMIC_RELAXED, __HIP_MEMORY_SCOPE_WORKGROUP); } while (0)
#define LOOKAT(T) until(t0 + (T) + (unsigned)__builtin_amdgcn_readfirstlane(*(volatile LAS int*)(L + L_TM + 12)))
DI u64 next_tag(v2u e) { return (((u64)e.y << 32) | e.x) * 6364136223846793005ull + 1442695040888963407ull; }
DI void dma128(int ln, rsrc_t r, int rowbytes, int soff, unsigned lds, int i) {
  dma(r, (ln >> 3) * rowbytes + ((((ln & 7) >> 1) ^ (((ln >> 4) & 1) | ((i & 1) << 1))) * 32) + (ln & 1) * 16, soff + 8 * i * rowbytes, lds + i * 1024);
}
DI f4 unp(v2u v) { return f4{blo(v.x), bhi(v.x), blo(v.y), bhi(v.y)}; }
DI v2u pk4(f4 v) { return v2u{pk(v.x, v.y), pk(v.z, v.w)}; }

__global__ void __launch_bounds__(512, 2) fused_fwd(Params p) {
  extern __shared__ __attribute__((aligned(16))) unsigned char smem[];
  const lptr L = (lptr)smem;
  const int tid = threadIdx.x, lane0 = tid & 63, w = __builtin_amdgcn_readfirstlane(tid >> 6);
  const int c = blockIdx.x, xc = c & 7, j = c >> 3, grp = j >> 4, u = j & 15, g = 2 * xc + grp;
  u64 tt[8] = {};
  const unsigned tst_ = now();
#define EV(k) do { if (EVT == (k) && lane0 == 0) *(volatile LAS unsigned*)(L + L_BYTES) = now() - tst_; } while (0)

  const int ab = u >> 2, as = u & 3;
  const bool hasP1 = u <= 8, hasP3 = u >= 7;
  const unsigned ldsb = (unsigned)(size_t)L;
  const rsrc_t rsW = mkrs((const void*)(((size_t)p.ws + PG - 1) & ~(size_t)(PG - 1)), WS_USED);
  constexpr float CS = 0.08838834764831845f * 1.4426950408889634f;
  v8u K8[8], V8[8];
  int pub1, pub2, pub3, pub4, pub5, gat1, gat2, gat3, gat4, gat5, gatc, mko, cls, slo, wofs, w1ofs, w2ofs, b1ofs;
  bool hasF, isOut;
  lptr qa, pw, pr, aow, mlw, mbr, mmr; int qidx, pidx, mst; v4u pm;
  {
    const int lane = lane0, col = lane & 15, hq = lane >> 4, q4 = col >> 2, pp = lane & 3;
    const size_t kvbase = ((size_t)(ab * 16 + g) * SK + (SK - WIN) + as * 512 + w * 64) * DH;
    const rsrc_t rsK = mkrs(p.kc + kvbase, 16384), rsV = mkrs(p.vc + kvbase, 16384);
    const int rot = __builtin_amdgcn_readfirstlane((w >> 1) + 4 * (j & 3) + 2 * xc);
    const int kvo = q4 * 256 + (4 * hq + pp) * 16, vvo = lane * 16;
    const int r1 = u < 8 ? xc : 8, gq1 = u < 8 ? 8 * grp + u : g, n3 = (u + 9) & 15;
    v4u K[16], V[16], csw = {0, 0, 0, 0};
    u64 tag = 0;
    if (w >= 4) {
      if (w == 4 && hasP1) {
        const rsrc_t rx = mkrs(p.x, 4 * DM * 2);
        const int xo_ = (hq * DM + col * 8) * 2;
        v4u xv[5];
#pragma unroll
        for (int k = 0; k < 4; ++k) xv[k] = __builtin_amdgcn_raw_buffer_load_b128(rx, xo_ + 256 * k, 0, 0);
        xv[4] = __builtin_amdgcn_raw_buffer_load_b128(rx, col < 8 ? xo_ + 1024 : 0x40000000, 0, 0);
        if (lane == 0) *(volatile LAS unsigned*)(L + L_RDY) = 0u;
        bar();
        SB();
        const int ks = r1 >> 1;
        const v4u xs = ks == 0 ? xv[0] : (ks == 1 ? xv[1] : (ks == 2 ? xv[2] : (ks == 3 ? xv[3] : xv[4])));
        if ((col >> 3) == (r1 & 1)) *(LAS v4u*)(L + L_XB + hq * XBS + (col & 7) * 16) = xs;
        float s1 = 0.f, s2 = 0.f, s3 = 0.f, s4 = 0.f;
#pragma unroll
        for (int k = 0; k < 5; ++k) {
          s1 = dot2(xv[k][0], 0x3F803F80u, s1); s2 = dot2(xv[k][0], xv[k][0], s2); s3 = dot2(xv[k][1], 0x3F803F80u, s3); s4 = dot2(xv[k][1], xv[k][1], s4);
          s1 = dot2(xv[k][2], 0x3F803F80u, s1); s2 = dot2(xv[k][2], xv[k][2], s2); s3 = dot2(xv[k][3], 0x3F803F80u, s3); s4 = dot2(xv[k][3], xv[k][3], s4);
        }
        s1 += s3; s2 += s4;
        s1 += dppf<XOR1>(s1); s2 += dppf<XOR1>(s2); s1 += dppf<XOR2>(s1); s2 += dppf<XOR2>(s2);
        s1 += dppf<ROR4>(s1); s2 += dppf<ROR4>(s2); s1 += dppf<ROR8>(s1); s2 += dppf<ROR8>(s2);
        const float mu = s1 * (1.f / DM), rs = __builtin_amdgcn_rsqf(fmaxf(s2 * (1.f / DM) - mu * mu, 0.f) + 1e-5f) * CS;
        if (col == 0) *(LAS f2*)(L + L_LN0 + hq * 8) = f2{mu, rs};
        OPAQUE();
        if (lane == 0) *(volatile LAS unsigned*)(L + L_RDY) = 1u;
        OPAQUE(); SB();
#pragma unroll
        for (int i = 0; i < 4; ++i) KLOAD(i);
      } else {
        KLOAD(0); KLOAD(1); KLOAD(2);
        if (hasP1) bar();
        KLOAD(3);
      }
    } else {
      unsigned nearb = 0;
      if (w == 3) {
        const v4u SENT4 = {SENT, SENT, SENT, SENT};
        const int a2 = lane < 40 ? X2_OFF + (2 * g + (ab >> 1)) * BLK + ((ab & 1) * 4 + as) * X2_MSG + lane * 16 : 0x40000000;
        const int a3 = (hasP3 && lane < 32) ? X3_OFF + n3 * BLK + g * 512 + lane * 16 : 0x40000000;
        u64 ta, tb;
        asm volatile("buffer_store_dwordx4 %2, %3, %5, 0 offen sc1\n\tbuffer_store_dwordx4 %2, %3, %5, %6 offen sc1\n\t"
                     "buffer_store_dwordx4 %2, %4, %5, 0 offen sc1\n\tbuffer_store_dwordx4 %2, %4, %5, %6 offen sc1\n\t"
                     "s_waitcnt vmcnt(3)\n\ts_memtime %0\n\ts_waitcnt vmcnt(2)\n\ts_memtime %1\n\ts_waitcnt lgkmcnt(0)"
                     : "=&s"(ta), "=&s"(tb) : "v"(SENT4), "v"(a2), "v"(a3), "s"(rsW), "s"(HALF) : "memory");
        COUNT(56, tb - ta);
        nearb = ((unsigned)(tb - ta) > 140u ? 1u : 0u) | (((unsigned)__builtin_amdgcn_s_getreg(20 | (3 << 11)) >> 1) & 2u);
      }
      v2u ep;
      asm volatile("buffer_load_dwordx2 %0, off, %1, 0 sc1" : "=v"(ep) : "s"(rsW) : "memory");
      {
        const rsrc_t rq = mkrs(hasP1 ? p.wq : p.wo, DM * QN * 2);
        const int rb = hasP1 ? QN * 2 : DM * 2, so = hasP1 ? 64 * r1 * (QN * 2) + gq1 * 512 + w * 128 : (256 * g + 64 * w) * (DM * 2) + n3 * 128;
#pragma unroll
        for (int i = 0; i < 8; ++i) dma128(lane, rq, rb, so, ldsb + (hasP1 ? L_WQ : L_WO) + w * 8192, i);
        if (hasP1) bar();
      }
      OPAQUE(); SB();
      v4u pf = {0, 0, 0, 0};
      if (PREFI) {
        u64 pc; unsigned len;
        asm volatile("s_getpc_b64 %0\n.Lfwd_pc:\n\ts_mov_b32 %1, .Lfwd_end-.Lfwd_pc" : "=s"(pc), "=s"(len));
        pf = __builtin_amdgcn_raw_buffer_load_b128(mkrs((const void*)(pc & ~15ull), len & ~15u), w == 2 ? j * 1024 + lane * 16 : 0x40000000, 0, 0);
      }
      OPAQUE(); SB();
      TT(1);
#pragma unroll
      for (int i = 0; i < 4; ++i) KLOAD(i);
      OPAQUE(); SB();
      asm volatile("s_waitcnt vmcnt(%1)" : "+v"(ep) : "i"(12 + PREFI) : "memory");
      if (w == 0) EV(1);
      tag = next_tag(ep);
      if (w == 3 && lane == 0) { const u64 f = tag + (u64)c * 0x9E3779B97F4A7C15ull; stx2(rsW, v2u{((unsigned)f & ~3u) | nearb, (unsigned)(f >> 32)}, 0, WS_FLAGS + c * 8); }
      if (hasP1) {
        A128();
        asm volatile("s_waitcnt vmcnt(%0)" ::"i"(4 + PREFI) : "memory");
        if (w == 0) EV(2);
        v4u A[8];
#pragma unroll
        for (int t = 0; t < 4; ++t) { const int base = L_WQ + w * 8192 + a128 + (t ^ z128) * 32; A[2 * t] = trfrag(L, base, 512); A[2 * t + 1] = trfrag(L, base + 4096, 512); }
        for (int spin = 0; *(volatile LAS unsigned*)(L + L_RDY) == 0u && spin < (SPIN_CAP << 4); ++spin) __builtin_amdgcn_s_sleep(1);
        OPAQUE();
        TT(3);
        const v4u xf0 = lds16(L, L_XB + pp * XBS + hq * 16), xf1 = lds16(L, L_XB + pp * XBS + hq * 16 + 64);
        const f2 ln = *(LAS f2*)(L + L_LN0 + pp * 8);
        SB();
        f4 d = Z4, e = Z4;
#pragma unroll
        for (int t = 0; t < 4; ++t) {
          const unsigned m = q4 == t ? ~0u : 0u, o = m & 0x3F803F80u;
          d = mfma(A[2 * t], xf0 & m, d); e = mfma(A[2 * t], v4u{o, o, o, o}, e);
          d = mfma(A[2 * t + 1], xf1 & m, d); e = mfma(A[2 * t + 1], v4u{o, o, o, o}, e);
        }
        d = (d - ln.x * e) * ln.y;
        stn(rsW, v4u{pk(d.x, d.y), (unsigned)tag & 0xBFFFBFFFu, pk(d.z, d.w), (unsigned)(tag >> 32) & 0xBFFFBFFFu},
            pp * 1024 + (16 * w + 4 * q4 + hq) * 16, X1A_OFF + (gq1 * 9 + r1) * X1_MSG);
        csw = split4(e);
      }
      TT(4);
      asm volatile("" ::"v"(pf));
    }
    OPAQUE(); SB();
    v4u fa;
#define WIN() do { if (WND) asm volatile("s_waitcnt vmcnt(%0)" ::"i"(WND - 1) : "memory"); } while (0)
#pragma unroll
    for (int i = 4; i < 16; ++i) {
      if (i >= WND) WIN();
      KLOAD(i); SB();
      if (i == FPOS) { if (w < 4) fa = ldn(rsW, lane * 16 + (w & 1) * 1024, WS_FLAGS); OPAQUE(); SB(); }
    }
    {
      const int fi0 = 14 * grp + (u <= 6 ? u : u - 2);
      hasF = (u != 7) && (u != 8) && (fi0 < 27);
      const int fi = fi0 < 27 ? fi0 : 0;
      const int n4 = fi < 24 ? xc : 8, f4i = fi < 24 ? fi : 3 * xc + fi - 24, f5 = 3 * xc + fi / 9, n5 = fi % 9;
      const int sl = u == 8 ? 8 : (hasF ? n4 : xc);
      isOut = grp == 0 && (u == 0 || (u == 8 && xc == 0));
      pub1 = X1_OFF + 5 * gq1 * BLK + (r1 < 8 ? r1 * 1024 : 4 * BLK); pub2 = X2_OFF + (2 * g + (ab >> 1)) * BLK + ((ab & 1) * 4 + as) * X2_MSG;
      pub3 = X3_OFF + n3 * BLK + g * 512; pub4 = X4_OFF + (5 * f4i + (n4 >> 1)) * BLK + (n4 & 1) * 4096; pub5 = X5_OFF + (2 * n5 + (f5 >> 4)) * BLK + (f5 & 15) * 512;
      gat1 = X1_OFF + 5 * g * BLK; gat2 = X2_OFF + (2 * g + ((w & 3) >> 1)) * BLK + (w & 1) * 4 * X2_MSG; gat3 = X3_OFF + sl * BLK;
      gat4 = X4_OFF + 5 * f5 * BLK; gat5 = X5_OFF + 2 * sl * BLK; gatc = X1A_OFF + g * 9 * X1_MSG; mko = WS_MARK + (n5 * 24 + f5) * 128;
      cls = (gq1 >> 1) | (n3 << 4) | ((f4i / 3) << 8) | (n5 << 12) | (r1 << 16) | (u << 20) | (g << 24);
      slo = 64 * sl; wofs = 256 * g * (DM * 2) + n3 * 128; w1ofs = 64 * n4 * (FF * 2) + f4i * 384; w2ofs = 192 * f5 * (DM * 2) + n5 * 128; b1ofs = 192 * f5;
      PINS(pub1); PINS(pub2); PINS(pub3); PINS(pub4); PINS(pub5); PINS(gat1); PINS(gat2); PINS(gat3); PINS(gat4); PINS(gat5); PINS(gatc); PINS(mko); PINS(cls);
      PINS(slo); PINS(wofs); PINS(w1ofs); PINS(w2ofs); PINS(b1ofs);
      qa = L + L_QP + (pp * 4 + hq) * 16;
      qidx = (lane & 1) ? 0xEEEE : 0x4444; pidx = pp * 0x1111;
      pw = L + L_PA + w * 2176 + (2 * hq + (pp >> 1)) * 272 + (2 * (pp & 1) + (q4 >> 1)) * 64 + (q4 & 1) * 8;
      pr = L + L_PA + w * 2176 + (hq * 4 + (col >> 3) * 2 + (q4 & 1)) * 16;
      aow = L + L_AO + w * 512 + ((lane >> 5) * 128 + 8 * col + 4 * (hq & 1)) * 2;
      mlw = L + L_ML + (w * 2 + (lane >> 5)) * 8;
      mbr = L + L_AO + (w & 1) * 256 + hq * 512 + col * 16; mmr = L + L_ML + (w & 1) * 8 + hq * 16;
      mst = hq < 2 ? (w & 1) * 256 + col * 16 + hq * 8 : 0x40000000;
      const unsigned hm = (lane & 1) ? 0xFFFF0000u : 0xFFFFu;
#pragma unroll
      for (int k = 0; k < 4; ++k) pm[k] = ((col & 7) >> 1) == k ? hm : 0u;
      PINI(qa); PINI(pw); PINI(pr); PINI(aow); PINI(mlw); PINI(mbr); PINI(mmr); PINI(qidx); PINI(pidx); PINI(mst); PINI(pm);
      const v4u z = {0, 0, 0, 0};
      const lptr pa = L + L_PA + w * 2176 + lane * 16;
      *(LAS v4u*)pa = z; *(LAS v4u*)(pa + 1024) = z; if (lane < 8) *(LAS v4u*)(pa + 2048) = z;
      if (w == 7 && lane == 0) { *(LAS unsigned*)(L + L_TM + 4) = 0u; *(LAS unsigned*)(L + L_TM + 12) = 0u; }
      if (w == 7 && lane < 36) { *(LAS unsigned*)(L + L_XB + 4 * XBS + lane * 4) = 0x3F803F80u; *(LAS unsigned*)(L + L_XB + 5 * XBS + lane * 4) = 0u; }
    }
    SB();
    auto transp = [&](int T) {
      const bool o1 = lane & 1, o2 = lane & 2;
#pragma unroll
      for (int d = 0; d < 4; ++d) {
        const unsigned m0 = K[4 * T][d], m1 = K[4 * T + 1][d], m2 = K[4 * T + 2][d], m3 = K[4 * T + 3][d];
        const unsigned s0 = dppu<XOR1>(m0), s1 = dppu<XOR1>(m1), s2 = dppu<XOR1>(m2), s3 = dppu<XOR1>(m3);
        const unsigned n0 = o1 ? s1 : m0, n1 = o1 ? m1 : s0, n2 = o1 ? s3 : m2, n3_ = o1 ? m3 : s2;
        const unsigned t0 = dppu<XOR2>(n0), t1 = dppu<XOR2>(n1), t2 = dppu<XOR2>(n2), t3 = dppu<XOR2>(n3_);
        K8[2 * T][d] = o2 ? t2 : n0; K8[2 * T + 1][d] = o2 ? n2 : t0;
        K8[2 * T][4 + d] = o2 ? t3 : n1; K8[2 * T + 1][4 + d] = o2 ? n3_ : t1;
      }
      asm volatile("" : "+v"(K8[2 * T]), "+v"(K8[2 * T + 1]));
    };
    int x1a = hq * X1_MSG + (16 * (w & 3) + col) * 16 + ab * 1024, x1b = x1a + 4 * X1_MSG, x1c = x1a + (8 - hq) * X1_MSG;
    int ta = (w < 4 && hasP1 && pp == 0) ? X1A_OFF + (gq1 * 9 + r1) * X1_MSG + 4096 + (16 * w + 4 * q4 + hq) * 16 : 0x40000000;
    const unsigned one = (lane & 1) ? 0x3F800000u : 0x00003F80u;
    v4u selT = {(col & 2) ? 0u : one, 0u, (col & 2) ? one : 0u, 0u}, selT0 = hq == 0 ? selT : v4u{0, 0, 0, 0};
    unsigned tgx = (unsigned)tag & 0xBFFFBFFFu, tgy = (unsigned)(tag >> 32) & 0xBFFFBFFFu;
    lptr qw = L + QPW(w & 3);
    PINI(x1a); PINI(x1b); PINI(x1c); PINI(ta); PINI(csw); PINI(selT); PINI(selT0); PINI(tgx); PINI(tgy); PINI(qw);
    v4u g0, g1, g2;
#pragma unroll
    for (int i = 0; i < 16; ++i) {
      WIN(); VLOAD(i); SB();
      if (i == FPOS - 16) { if (w < 4) fa = ldn(rsW, lane * 16 + (w & 1) * 1024, WS_FLAGS); OPAQUE(); SB(); }
      if (i == XPOS) {
        if (w < 4) { g0 = ldn(rsW, x1a, gatc); g1 = ldn(rsW, x1b, gatc); g2 = ldn(rsW, x1c, gatc); }
        OPAQUE(); SB();
      }
      if (i == SPOS) {
        PINI(fa);
        if (w < 4) {
          const u64 e0 = tag + (u64)(lane * 2 + (w & 1) * 128) * 0x9E3779B97F4A7C15ull, e1 = e0 + 0x9E3779B97F4A7C15ull;
          int spin = 0;
#pragma nounroll
          for (; spin < SPIN_CAP; ++spin) {
            if (!any(((fa.x ^ (unsigned)e0) > 3u) || fa.y != (unsigned)(e0 >> 32) || ((fa.z ^ (unsigned)e1) > 3u) || fa.w != (unsigned)(e1 >> 32))) break;
            OPAQUE();
            fa = ldn(rsW, lane * 16 + (w & 1) * 1024, WS_FLAGS);
            PINI(fa);
          }
          if (w == 0) COUNT(40, spin);
          stn(rsW, csw, ta, 0);
          if (!(w & 1)) {
            const int g0v = __builtin_popcountll(__builtin_amdgcn_ballot_w64((fa.x & 3u) == 1u)) + __builtin_popcountll(__builtin_amdgcn_ballot_w64((fa.z & 3u) == 1u));
            const int g1v = __builtin_popcountll(__builtin_amdgcn_ballot_w64((fa.x & 3u) == 3u)) + __builtin_popcountll(__builtin_amdgcn_ballot_w64((fa.z & 3u) == 3u));
            const unsigned se = (unsigned)__builtin_amdgcn_ballot_w64(fa.x & 2u), so_ = (unsigned)__builtin_amdgcn_ballot_w64(fa.z & 2u);
            unsigned hm = 0;
#pragma unroll
            for (int k = 0; k < 4; ++k) hm |= ((se >> k) & 1u) << (2 * k) | ((so_ >> k) & 1u) << (2 * k + 1);
            if (g0v > g1v) hm ^= 0xFFu;
            if (!NUMA) hm = 0;
            if (w == 0 && lane == 0) { *(LAS unsigned*)(L + L_TM + 8) = hm; *(LAS u64*)(L + L_TAG + 8) = tag; }
            if (hasF) {
              unsigned sn = SENT; PINI(sn);
              const v4u SENT4 = {sn, sn, sn, sn};
              const int a4 = pub4 + (((hm >> ((cls >> 8) & 15)) & 1) << 13) + w * 1024, n5 = (cls >> 12) & 15;
              stn(rsW, SENT4, lane * 16, a4);
              if (w == 0 || lane < 56) stn(rsW, SENT4, lane * 16, a4 + 1024);
              if (lane < 32 && (n5 == 8 || (((hm >> n5) ^ 1) & 1) == (unsigned)(w >> 1))) stn(rsW, SENT4, lane * 16, pub5 + (w >> 1) * HALF);
            }
          }
        }
        OPAQUE(); SB();
      }
      if (i == TP0) { transp(0); SB(); }
      if (i == TP1) { transp(1); SB(); }
      if (i == TP2) { transp(2); SB(); }
      if (i == TP3) { transp(3); SB(); }
    }
#pragma unroll
    for (int m = 0; m < 8; ++m) V8[m] = cat(V[2 * m], V[2 * m + 1]);
    OPAQUE(); SB();
    STAMPW(0, 3); STAMPW(7, 4);
    if (w == 0) { COUNT(0, tt[0]); COUNT(60, __builtin_amdgcn_s_memtime()); COUNT(59, __builtin_amdgcn_s_memrealtime()); if (hasP1) { COUNT(1, tt[1]); COUNT(2, tt[2]); COUNT(5, tt[3]); COUNT(35, tt[4]); } }
    asm volatile("" : "+v"(g0), "+v"(g1), "+v"(g2));
    if (w < 4) {
      int spin = 0;
#pragma nounroll
      for (; spin < SPIN_CAP; ++spin) {
        if (!any(g0.y != tgx || g0.w != tgy || g1.y != tgx || g1.w != tgy || g2.y != tgx || g2.w != tgy)) break;
        OPAQUE();
        g0 = ldn(rsW, x1a, gatc); g1 = ldn(rsW, x1b, gatc); g2 = ldn(rsW, x1c, gatc);
        asm volatile("" : "+v"(g0), "+v"(g1), "+v"(g2));
      }
      f4 q = mfma(selT, g0, Z4); q = mfma(selT, g1, q); q = mfma(selT0, g2, q);
      if (lane < 16) { LAS unsigned* qp = (LAS unsigned*)qw; qp[0] = pk(q.x, q.y); qp[16] = pk(q.z, q.w); }
      if (w == 0) COUNT(41, spin);
      STAMPW(0, 6);
    }
  }
  f4 S[4] = {Z4, Z4, Z4, Z4}, O = Z4;
  PIN4(S); PINI(O);
  bar();
  if (w == 0) EV(3);
  {
    const unsigned hm = __builtin_amdgcn_readfirstlane(*(LAS unsigned*)(L + L_TM + 8));
    if (w == 0) COUNT(55, hm);
    const int own = ((hm >> xc) & 1) << 13;
    pub1 += ((hm >> (cls & 15)) & 1) << 13; pub2 += own ^ HALF; pub3 += (((hm >> ((cls >> 4) & 15)) & 1) << 13) ^ HALF;
    pub4 += ((hm >> ((cls >> 8) & 15)) & 1) << 13; pub5 += (((hm >> ((cls >> 12) & 15)) & 1) << 13) ^ HALF;
    gat1 += own; gat2 += own ^ HALF; gat3 += own ^ HALF; gat4 += own; gat5 += own ^ HALF;
  }

  v2u xp = {0, 0}, b2v = {0, 0}; unsigned b1v = 0;
  f4 csq = Z4; f2 cs1 = {0.f, 0.f};
  unsigned t0 = 0;
  const int w_ = w, ab_ = ab, pub1_ = pub1, pub2_ = pub2, pub3_ = pub3, pub4_ = pub4, pub5_ = pub5, gat1_ = gat1, gat2_ = gat2, gat3_ = gat3, gat4_ = gat4, gat5_ = gat5, gatc_ = gatc, mko_ = mko, cls_ = cls, slo_ = slo, wofs_ = wofs, w1ofs_ = w1ofs, w2ofs_ = w2ofs, b1ofs_ = b1ofs;
#pragma nounroll
  for (int lay = 0; lay < 2; ++lay) {
    const int xs = __builtin_amdgcn_readfirstlane(lay * SM_STRIDE), x4 = __builtin_amdgcn_readfirstlane(lay * (PG + HALF));
    int w = w_, ab = ab_, pub1 = pub1_, pub2 = pub2_, pub3 = pub3_, pub4 = pub4_, pub5 = pub5_, gat1 = gat1_, gat2 = gat2_, gat3 = gat3_, gat4 = gat4_, gat5 = gat5_, gatc = gatc_, mko = mko_, cls = cls_, slo = slo_, wofs = wofs_, w1ofs = w1ofs_, w2ofs = w2ofs_, b1ofs = b1ofs_;
    asm volatile("" : "+s"(w), "+s"(ab), "+s"(pub1), "+s"(pub2), "+s"(pub3), "+s"(pub4), "+s"(pub5), "+s"(gat1), "+s"(gat2), "+s"(gat3), "+s"(gat4), "+s"(gat5), "+s"(gatc), "+s"(mko), "+s"(cls), "+s"(slo), "+s"(wofs), "+s"(w1ofs), "+s"(w2ofs), "+s"(b1ofs));
    const int lay_ = lay;
    {
      v4u AF[8];
      const v4u Q0 = lds16(qa, 0), Q1 = lds16(qa, 256);
      SB();
#pragma unroll
      for (int T = 0; T < 4; ++T) S[T] = smf(Q0, K8[2 * T], S[T], qidx);
#pragma unroll
      for (int T = 0; T < 4; ++T) S[T] = smf(Q1, K8[2 * T + 1], S[T], qidx);
      float s0, s1;
      asm("s_nop 7\n\t"
          "v_add_f32_dpp %0, %2, %3 quad_perm:[0,1,2,3] row_mask:0x1 bank_mask:0xf\n\tv_add_f32_dpp %1, %4, %5 quad_perm:[0,1,2,3] row_mask:0x1 bank_mask:0xf\n\t"
          "v_add_f32_dpp %0, %6, %7 quad_perm:[0,1,2,3] row_mask:0x2 bank_mask:0xf\n\tv_add_f32_dpp %1, %8, %9 quad_perm:[0,1,2,3] row_mask:0x2 bank_mask:0xf\n\t"
          "v_add_f32_dpp %0, %10, %11 quad_perm:[0,1,2,3] row_mask:0x4 bank_mask:0xf\n\tv_add_f32_dpp %1, %12, %13 quad_perm:[0,1,2,3] row_mask:0x4 bank_mask:0xf\n\t"
          "v_add_f32_dpp %0, %14, %15 quad_perm:[0,1,2,3] row_mask:0x8 bank_mask:0xf\n\tv_add_f32_dpp %1, %16, %17 quad_perm:[0,1,2,3] row_mask:0x8 bank_mask:0xf"
          : "=&v"(s0), "=&v"(s1)
          : "v"(S[0].x), "v"(S[0].y), "v"(S[0].z), "v"(S[0].w), "v"(S[1].x), "v"(S[1].y), "v"(S[1].z), "v"(S[1].w),
            "v"(S[2].x), "v"(S[2].y), "v"(S[2].z), "v"(S[2].w), "v"(S[3].x), "v"(S[3].y), "v"(S[3].z), "v"(S[3].w));
      auto sw = __builtin_amdgcn_permlane32_swap(asu(s0), asu(s1), false, false);
      float mx;
      asm("v_max_f32 %0, %1, %2\n\ts_nop 1\n\tv_max_f32_dpp %0, %0, %0 quad_perm:[1,0,3,2] row_mask:0xf bank_mask:0xf\n\ts_nop 1\n\t"
          "v_max_f32_dpp %0, %0, %0 quad_perm:[2,3,0,1] row_mask:0xf bank_mask:0xf\n\ts_nop 1\n\tv_max_f32_dpp %0, %0, %0 row_ror:4 row_mask:0xf bank_mask:0xf\n\ts_nop 1\n\t"
          "v_max_f32_dpp %0, %0, %0 row_ror:8 row_mask:0xf bank_mask:0xf\n\ts_nop 1\n\tv_max_f32_dpp %0, %0, %0 row_bcast:15 row_mask:0xa bank_mask:0xf\n\ts_nop 0"
          : "=&v"(mx) : "v"(asf(sw[0])), "v"(asf(sw[1])));
      const float m0 = asf(__builtin_amdgcn_readlane(asu(mx), 31)), m1 = asf(__builtin_amdgcn_readlane(asu(mx), 63));
      const float p0 = __builtin_amdgcn_exp2f(s0 - m0), p1 = __builtin_amdgcn_exp2f(s1 - m1);
      const unsigned pb = pk(p0, p1);
      *(LAS u16*)(pw) = (u16)pb; *(LAS u16*)(pw + 20) = (u16)pb; *(LAS u16*)(pw + 32) = (u16)(pb >> 16); *(LAS u16*)(pw + 52) = (u16)(pb >> 16);
#pragma unroll
      for (int m = 0; m < 8; ++m) AF[m] = lds16(pr, m * 272);
      SB();
      auto sl2 = __builtin_amdgcn_permlane32_swap(asu(p0), asu(p1), false, false);
      float ls = asf(sl2[0]) + asf(sl2[1]);
      ls += dppf<XOR1>(ls); ls += dppf<XOR2>(ls); ls += dppf<ROR4>(ls); ls += dppf<ROR8>(ls);
      ls += asf((unsigned)__builtin_amdgcn_update_dpp(0, (int)asu(ls), 0x142, 0xa, 0xf, false));
      if ((lane0 & 31) == 31) *(LAS f2*)mlw = f2{mx, ls};
      SB();
#pragma unroll
      for (int m = 0; m < 8; ++m) O = smf(AF[m], V8[m], O, pidx);
      *(LAS v2u*)aow = pk4(O);
    }
    bar();
    if (w == 0 && lay == 0) EV(4);
#define XBPTR() lptr xb0 = L + L_XB + (col < 4 ? col : (col == 8 ? 4 : 5)) * XBS + hq * 16, xb1 = L + L_XB + ((col >= 4 && col < 8) ? col - 4 : (col == 12 ? 4 : 5)) * XBS + hq * 16; PINI(xb0); PINI(xb1)
#define STPTR() lptr sra = L + L_XB + (pp == 0 ? 4 : (pp == 1 ? q4 : 5)) * XBS + hq * 16, srb = L + L_XB + pp * XBS + hq * 16; int sso = col == hq ? col * 16 : 0x40000000; PINI(sra); PINI(srb); PINI(sso)
#define STATS(off) do { const unsigned tm_ = now(); const v4u a0 = lds16(sra, 0), a1 = lds16(sra, 64), b0 = lds16(srb, 0), b1 = lds16(srb, 64); SB(); \
      f4 d_ = mfma(a0, b0, Z4); d_ = mfma(a1, b1, d_); stx(rsW, v4u{cl(asu(d_.x)), cl(asu(d_.y)), tm_, 0u}, sso, off); } while (0)
#define STAT1 (pub1 + (((cls >> 16) & 15) < 8 ? 4 * BLK + 4096 - ((cls >> 16) & 15) * 896 : 4096 + 1024))
#define P1PREP() v4u A1[4]; int st1 = col < 8 ? pp * (((cls >> 16) & 15) < 8 ? BLK : 1024) + (8 * w + 4 * q4 + hq) * 16 : 0x40000000, sp1 = pub1; XBPTR(); \
      { const int base = L_WQ + (w >> 1) * 8192 + a128, t0 = 2 * (w & 1); \
        A1[0] = trfrag(L, base + (t0 ^ z128) * 32, 512); A1[1] = trfrag(L, base + (t0 ^ z128) * 32 + 4096, 512); \
        A1[2] = trfrag(L, base + ((t0 + 1) ^ z128) * 32, 512); A1[3] = trfrag(L, base + ((t0 + 1) ^ z128) * 32 + 4096, 512); } \
      PIN4(A1); PINI(st1); PINS(sp1)
#define P1RUN() do { if (hasP1) { const v4u b00 = lds16(xb0, 0), b01 = lds16(xb0, 64), b10 = lds16(xb1, 0), b11 = lds16(xb1, 64); SB(); \
        f4 d_ = mfma(A1[0], b00, Z4); d_ = mfma(A1[1], b01, d_); d_ = mfma(A1[2], b10, d_); d_ = mfma(A1[3], b11, d_); stx(rsW, split4(d_), st1, sp1); } } while (0)
#define CSQLOAD() const int ve = hq * X1_MSG + (16 * (w & 3) + col) * 16 + 4096;     \
      const v4u c0 = ldx(rsW, ve, gatc), c1 = ldx(rsW, ve + 4 * X1_MSG, gatc), c2 = ldx(rsW, ve + (8 - hq) * X1_MSG, gatc)
#define CSQSUM() do { const unsigned one_ = (lane & 1) ? 0x3F800000u : 0x00003F80u; \
      const v4u sel4 = {(col & 2) ? 0u : one_, (col & 2) ? one_ : 0u, (col & 2) ? 0u : one_, (col & 2) ? one_ : 0u}; \
      csq = mfma(sel4, c0, Z4); csq = mfma(sel4, c1, csq); csq = mfma(hq == 0 ? sel4 : v4u{0, 0, 0, 0}, c2, csq); } while (0)
    if (w < 6) {
      int ymsg;
      if (lay == 0) { FRESH(); b1v = *(const unsigned*)(p.b1 + b1ofs + HFO); PINI(b1v); }
      if (w < 4) {
        FRESH(); A128();
        int so = gat2 + xs, vm = hq * X2_MSG + 512 + (col >> 3) * 16;
        ymsg = col < 4 ? (4 * (2 * w + (hq >> 1)) + pp) * 16 + (hq & 1) * 8 : 0x40000000;
        PINS(so); PINI(vm); PINI(ymsg);
        int spin = 0;
        if (hasP3) {
          int vd = hq * X2_MSG + col * 16, sp = pub3 + xs;
          lptr p3a = L + L_WO + a128 + (w ^ z128) * 32, p3b = L + L_OB + pp * OBS + hq * 16;
          v4u pm0 = col < 8 ? pm : v4u{0, 0, 0, 0}, pm1 = col < 8 ? v4u{0, 0, 0, 0} : pm;
          lptr ow = L + L_OB + w * OBS + ((hq >> 1) * 128 + 8 * col + 4 * (hq & 1)) * 2;
          PINI(vd); PINS(sp); PINI(p3a); PINI(p3b); PINI(pm0); PINI(pm1); PINI(ow);
          v4u A[8];
#pragma unroll
          for (int k = 0; k < 8; ++k) A[k] = trfrag(p3a, k * 4096, 512);
          if (lay == 0 || !SCHED) __builtin_amdgcn_s_sleep(D_X2); else LOOKAT(T2B);
          f4 o;
          for (;; ++spin) {
            OPAQUE();
            const v2u ml = ldx2(rsW, vm, so);
            SB();
            const v4u d0 = ldx(rsW, vd, so), d1 = ldx(rsW, vd + 256, so);
            SB();
            const float m = asf(ml.x), M = max32(max16(m));
            const float f = __builtin_amdgcn_exp2f(m - M);
            const float Ls = add32(add16(f * asf(ml.y)));
            const float fs = f * __builtin_amdgcn_rcpf(Ls);
            const unsigned gg = pk(fs, fs);
            o = mfma(pm0 & gg, d0, Z4); o = mfma(pm1 & gg, d1, o);
            if (!any(o.x != o.x)) break;
            if (!any(max(max(mx4(d0), mx4(d1)), max(ml.x, ml.y)) == SENT) || spin > SPIN_CAP) break;
          }
          OPAQUE();
          *(LAS v2u*)ow = pk4(o);
          LATE(T2B);
          STAMPW(0, 10 + lay * 16);
          bar();
          STAMPW(0, 11 + lay * 16);
          v4u B[8];
#pragma unroll
          for (int k = 0; k < 8; ++k) B[k] = lds16(p3b, k * 64);
          if (lay == 0) {
#pragma unroll
            for (int k = 0; k < 8; ++k) A[k] = trfrag(p3a, k * 4096, 512);
          }
          SB();
          f4 d = Z4;
#pragma unroll
          for (int k = 0; k < 8; ++k) d = mfma(A[k], B[k], d);
          const v2u yv = pack4(d);
          stx2(rsW, yv, ymsg, sp);
          if (((cls >> 4) & 15) == 8) stx2(rsW, yv, ymsg, sp ^ HALF);
          STAMPW(0, 20 + lay * 16);
        } else bar();
        if (w == 0) COUNT(42 + lay * 8, spin);
      } else {
        ymsg = 0;
        if (lay == 0) {
          FRESH();
          unsigned sn = SENT; PINI(sn);
          const v4u SENT4 = {sn, sn, sn, sn};
          const int lo = lane * 16, r1 = (cls >> 16) & 15;
          __builtin_amdgcn_s_sleep(D_W);
          if (hasP3 && !hasP1) __builtin_amdgcn_s_sleep(D_X2 + 2 - D_W);
          if (hasP3 && hasP1) {
            const rsrc_t ro = mkrs(p.wo, QN * DM * 2);
#pragma unroll
            for (int i = 0; i < 16; ++i) dma128(lane, ro, DM * 2, wofs + 128 * (w - 4) * (DM * 2), ldsb + L_WO + (w - 4) * 16384, i);
          }
          if (w == 4) {
            if (hasF) {
              const int a0 = (pub4 + PG) ^ HALF;
              stx(rsW, SENT4, lo, a0); stx(rsW, SENT4, lo, a0 + 1024); stx(rsW, SENT4, lo, a0 + 2048);
              if (lane < 8) stx(rsW, SENT4, lo, a0 + 3840);
              if (lane < 32) { stx(rsW, SENT4, lo, pub5 + SM_STRIDE); if (((cls >> 12) & 15) == 8) stx(rsW, SENT4, lo, (pub5 + SM_STRIDE) ^ HALF); }
            }
            if (lane < 40) stx(rsW, SENT4, lo, pub2 + SM_STRIDE);
          } else {
            if (hasP1) {
              const int rs = r1 < 8 ? BLK : 1024;
              stx(rsW, SENT4, lo, pub1); stx(rsW, SENT4, lo, pub1 + rs); stx(rsW, SENT4, lo, pub1 + 2 * rs); stx(rsW, SENT4, lo, pub1 + 3 * rs);
              if (lane < 8) stx(rsW, SENT4, lo, STAT1);
            }
            if (hasP3 && lane < 32) { stx(rsW, SENT4, lo, pub3 + SM_STRIDE); if (((cls >> 4) & 15) == 8) stx(rsW, SENT4, lo, (pub3 + SM_STRIDE) ^ HALF); }
          }
          if (hasF) {
            const rsrc_t r = mkrs(p.w1, DM * FF * 2), r2 = mkrs(p.w2, FF * DM * 2);
            const int vo = (lane >> 2) * (FF * 2) + ((lane & 3) ^ (2 * (lane >> 5))) * 16;
#pragma unroll
            for (int i = 0; i < 12; ++i) { const int t = 3 * (w - 4) + (i >> 2); dma(r, vo, w1ofs + 16 * (i & 3) * (FF * 2) + t * 64, ldsb + L_W1 + t * 4096 + (i & 3) * 1024); }
            asm volatile("s_waitcnt vmcnt(12)" ::: "memory");
            bar();
            for (int sp2 = 0; *(volatile LAS unsigned*)(L + L_TM + 4) == 0u && sp2 < (SPIN_CAP << 4); ++sp2) __builtin_amdgcn_s_sleep(1);
#pragma unroll
            for (int i = 0; i < 12; ++i) dma128(lane, r2, DM * 2, w2ofs + 96 * (w - 4) * (DM * 2), ldsb + L_W2 + (w - 4) * 12288, i);
            asm volatile("s_waitcnt vmcnt(12)" ::: "memory");
          } else { drain(); bar(); }
        } else bar();
      }
      int spin = 0;
      if (hasF) {
        v4u A[6];
        lptr p5a, p5b;
        {
          FRESH();
          XBPTR();
          const int s0 = (hq & 1) * 32;
          lptr p4a0 = L + L_W1 + w * 4096 + hq * 512 + q4 * 64 + pp * 8 + s0, p4a1 = p4a0 + 32 - 2 * s0;
          int st4 = col < 8 ? w * 512 + (16 * q4 + 4 * hq + pp) * 16 : ((lay == 0 && pp == 0) ? 3072 + w * 128 + (col - 8 + hq) * 16 : 0x40000000), sp = (pub4 + (x4 & ~HALF)) ^ (x4 & HALF);
          v4u A4[4] = {trfrag(p4a0, 0, 256), trfrag(p4a0, 2048, 256), trfrag(p4a1, 0, 256), trfrag(p4a1, 2048, 256)};
          PINI(st4); PINS(sp); PINI(p4a0); PINI(p4a1); PIN4(A4);
          bar();
          if (w == 0 && lay == 0) EV(7);
          const v4u b00 = lds16(xb0, 0), b01 = lds16(xb0, 64), b10 = lds16(xb1, 0), b11 = lds16(xb1, 64);
          if (lay == 0) { A4[0] = trfrag(p4a0, 0, 256); A4[1] = trfrag(p4a0, 2048, 256); A4[2] = trfrag(p4a1, 0, 256); A4[3] = trfrag(p4a1, 2048, 256); }
          SB();
          f4 d = mfma(A4[0], b00, Z4); d = mfma(A4[1], b01, d); d = mfma(A4[2], b10, d); d = mfma(A4[3], b11, d);
          stx(rsW, split4(d), st4, sp);
          STAMPW(0, 14 + lay * 16);
        }
        FRESH(); A128();
        const int pn = lane >> 2;
        const int vv = 2 * (q4 & 1) + (col & 1);
        const unsigned on = ((col >> 3) == (hq >> 1) && (col & 2) == 0) ? ((vv & 1) ? 0x3F800000u : 0x3F80u) : 0u;
        v4u selh = {(vv & 2) ? 0u : on, (vv & 2) ? on : 0u, (vv & 2) ? 0u : on, (vv & 2) ? on : 0u};
        v4u selh0 = (hq & 1) ? v4u{0, 0, 0, 0} : selh;
        const int pc = pn < 9 ? pn : 8;
        int so = (gat4 + (x4 & ~HALF)) ^ (x4 & HALF), vs = (pc >> 1) * BLK + (pc & 1) * 4096 + 3840 + pp * 16, sp5 = pub5 + xs;
        float sm = pn < 9 ? (1.f / DM) : 0.f;
        lptr hw = L + L_HB + pp * HBS + HFO * 2;
        int vo4 = 4 * BLK + w * 512 + (16 * (hq >> 1) + col) * 16, vo = vo4 - 4 * BLK + (hq & 1) * 4096;
        int sa = so + BLK, sb = so + 2 * BLK, sc = so + 3 * BLK;
        int ve1 = 4 * BLK + 3072 + w * 128 + (4 * (hq >> 1) + q4) * 16, ve0 = ve1 + (2 * (hq & 1) + (pp >> 1) - 4) * BLK + (pp & 1) * 4096;
        p5a = L + L_W2 + a128 + ((w & 3) ^ z128) * 32; p5b = L + L_HB + pp * HBS + hq * 16;
        f2 bb = {blo(b1v), bhi(b1v)};
        PINS(so); PINS(sp5); PINI(selh); PINI(selh0); PINI(vs); PINI(sm); PINI(hw); PINI(vo); PINI(vo4); PINS(sa); PINS(sb); PINS(sc); PINI(ve0); PINI(ve1); PINI(p5a); PINI(p5b); PINI(bb);
#pragma unroll
        for (int k = 0; k < 6; ++k) A[k] = trfrag(p5a, k * 4096, 512);
        if (lay == 0) __builtin_amdgcn_s_sleep(D_X4A); else if (SCHED) LOOKAT(T4B); else __builtin_amdgcn_s_sleep(D_X4);
        f4 a; float rstd; f2 t;
        for (;; ++spin) {
          OPAQUE();
          const v2u st = ldx2(rsW, vs, so);
          v4u e0 = {0, 0, 0, 0}, e1 = e0;
          if (lay == 0) { e0 = ldx(rsW, ve0, so); e1 = ldx(rsW, ve1, so); }
          const v4u d0 = ldx(rsW, vo, so), d1 = ldx(rsW, vo, sa), d2 = ldx(rsW, vo, sb), d3 = ldx(rsW, vo, sc), d4 = ldx(rsW, vo4, so);
          SB();
          float s1 = asf(st.x) * sm, s2 = asf(st.y) * sm;
          s1 += dppf<ROR4>(s1); s2 += dppf<ROR4>(s2); s1 += dppf<ROR8>(s1); s2 += dppf<ROR8>(s2);
          s1 = add32(add16(s1)); s2 = add32(add16(s2));
          rstd = __builtin_amdgcn_rsqf(fmaxf(s2 - s1 * s1, 0.f) + 1e-5f);
          if (lay == 0) {
            f4 e = mfma(selh, e0, Z4); const f4 e8 = mfma(selh0, e1, Z4);
            e.x += dppf<XOR1>(e.x); e.y += dppf<XOR1>(e.y); e.x += dppf<XOR2>(e.x); e.y += dppf<XOR2>(e.y);
            cs1 = f2{e.x + e8.x, e.y + e8.y};
          }
          t = bb - (s1 * rstd) * cs1;
          SB();
          a = mfma(selh, d0, Z4); a = mfma(selh, d1, a); a = mfma(selh, d2, a); a = mfma(selh, d3, a); a = mfma(selh0, d4, a);
          if (!any(a.x != a.x || t.x != t.x)) break;
          if (!any(max(max(max(max(mx4(d0), mx4(d1)), max(mx4(d2), mx4(d3))), max(mx4(d4), max(st.x, st.y))), max(mx4(e0), mx4(e1))) == SENT) || spin > SPIN_CAP) break;
        }
        OPAQUE();
        STAMPW(0, 15 + lay * 16);
        const f2 v = f2{a.x, a.y} * rstd + t;
        const f2 e = v * (v * v * (-2.f * 0.7978845608028654f * 1.4426950408889634f * 0.044715f) + (-2.f * 0.7978845608028654f * 1.4426950408889634f));
        const float h0 = v.x * __builtin_amdgcn_rcpf(1.f + __builtin_amdgcn_exp2f(e.x)), h1 = v.y * __builtin_amdgcn_rcpf(1.f + __builtin_amdgcn_exp2f(e.y));
        *(LAS unsigned*)hw = pk(h0, h1);
        LATE(T4B);
        bar();
        STAMPW(0, 16 + lay * 16);
        if (w < 4) {
          v4u B[6];
#pragma unroll
          for (int k = 0; k < 6; ++k) B[k] = lds16(p5b, k * 64);
          if (lay == 0) {
#pragma unroll
            for (int k = 0; k < 6; ++k) A[k] = trfrag(p5a, k * 4096, 512);
          }
          SB();
          f4 d = Z4;
#pragma unroll
          for (int k = 0; k < 6; ++k) d = mfma(A[k], B[k], d);
          const v2u zv = pack4(d);
          stx2(rsW, zv, ymsg, sp5);
          if (((cls >> 12) & 15) == 8) stx2(rsW, zv, ymsg, sp5 ^ HALF);
          STAMPW(0, 21 + lay * 16);
        }
      } else {
        bar();
        bar();
      }
      if (w == 0) COUNT(44 + lay * 8, spin);
      if (lay == 1) return;
      FRESH(); A128();
      P1PREP(); STPTR();
      if (w >= 4) {
        const unsigned tn = now();
        CSQLOAD(); CSQSUM(); PINI(csq);
        if (w == 5) {
          __builtin_amdgcn_s_sleep(D_M);
          int dm;
          for (int sp2 = 0;; ++sp2) {
            OPAQUE();
            const unsigned m = __builtin_amdgcn_raw_buffer_load_b32(rsW, ((slo >> 6) * 24 + (lane < 24 ? lane : 0)) * 128, WS_MARK, AUX_SC1);
            dm = (int)((m - tn) << 1);
            const bool bad = dm > 1600 || dm < -1600;
            if (bad) dm = -1600;
            if (!any(bad) || sp2 > 8) break;
          }
          dm = max(dm, (int)dppu<XOR1>(dm)); dm = max(dm, (int)dppu<XOR2>(dm)); dm = max(dm, (int)dppu<ROR4>(dm)); dm = max(dm, (int)dppu<ROR8>(dm));
          dm = max(__builtin_amdgcn_readlane(dm, 0), __builtin_amdgcn_readlane(dm, 16));
          if (lane == 0) *(LAS unsigned*)(L + L_TM) = (tn + (unsigned)(dm >> 1)) & 0x7FFFFFFFu;
        }
      }
      bar();
      if (w == 0) EV(6);
      P1RUN();
      t0 = __builtin_amdgcn_readfirstlane(*(LAS unsigned*)(L + L_TM));
      if (w == 3 && hasP1) STATS(STAT1);
      asm volatile(".Lfwd_end:");
      STAMPW(0, 19);
    } else {
      {
        const unsigned tm = now();
        const f2 ml0 = *(LAS f2*)mmr, ml1 = *(LAS f2*)(mmr + 64);
        const v4u B0 = lds16(mbr, 0), B1 = lds16(mbr, 2048);
        const int so = pub2 + xs;
        SB();
        const float M = max32(max16(vmax(ml0.x, ml1.x)));
        const float f0 = __builtin_amdgcn_exp2f(ml0.x - M), f1 = __builtin_amdgcn_exp2f(ml1.x - M);
        const unsigned h0 = pk(f0, f0), h1 = pk(f1, f1);
        f4 o = mfma(pm & h0, B0, Z4); o = mfma(pm & h1, B1, o);
        const float Ls = add32(add16(blo(h0) * ml0.y + blo(h1) * ml1.y));
        if (lane0 == 0) stx(rsW, v4u{cl(asu(M)), cl(asu(Ls)), tm, 0u}, 512 + (w & 1) * 16, so);
        stx2(rsW, pack4(o), mst, so);
        STAMPW(6, 9 + lay * 16);
      }
      FRESH(); A128(); SEL8(); STPTR();
      if (lay == 0) { xp = *(const v2u*)(p.x + pp * DM + slo + TKO); b2v = *(const v2u*)(p.b2 + slo + TKO); PINI(xp); PINI(b2v); }
      v4u s8 = sel8;
      int v0 = hq * 512 + (w - 6) * 256 + col * 16;
      lptr xw = L + L_XB + pp * XBS + TKO * 2;
      {
        int so = gat3 + xs, so1 = so + 4096, sp = ((pub4 + (x4 & ~HALF)) ^ (x4 & HALF)) + 3840;
        f4 xr = unp(xp);
        PINS(so); PINS(so1); PINS(sp); PINI(s8); PINI(v0); PINI(xw); PINI(xr);
        if (lay == 0) {
          const unsigned tb = now();
          if (w == 7 && ((cls >> 20) & 15) == 15) {
            __builtin_amdgcn_s_sleep(D_X2 - 10);
            for (int sp2 = 0;; ++sp2) {
              OPAQUE();
              const unsigned t = __builtin_amdgcn_raw_buffer_load_b32(rsW, (col >> 3) * BLK + (col & 7) * X2_MSG + 512 + 16 + 8, gat2 - BLK - 4 * X2_MSG, AUX_SC1);
              if (!any(t == SENT) || sp2 > SPIN_CAP) break;
            }
            if (lane0 == 0) __builtin_amdgcn_raw_buffer_store_b32(now(), rsW, 0, WS_HER + (cls >> 24) * 128, AUX_SC1);
          }
          bar();
          for (int sp2 = 0; sp2 < 64; ++sp2) {
            OPAQUE();
            const unsigned t = __builtin_amdgcn_raw_buffer_load_b32(rsW, col * 128, WS_HER, AUX_SC1);
            if (!any(((t - tb + 800u) & 0x7FFFFFFFu) > 4000u || t > 0x7FFFFFFFu)) break;
          }
          if (w == 6) EV(5);
        } else bar();
        if (lay == 0) {} else if (SCHED) LOOKAT(T3B); else { __builtin_amdgcn_s_sleep(D_X3); if (!hasP3) __builtin_amdgcn_s_sleep(36); }
        f4 y;
        int spin = 0;
        for (;; ++spin) {
          OPAQUE();
          const v4u d0 = ldx(rsW, v0, so), d1 = ldx(rsW, v0 + 2048, so), d2 = ldx(rsW, v0, so1), d3 = ldx(rsW, v0 + 2048, so1);
          if (lane0 == 0) *(volatile LAS unsigned*)(L + L_TM + 4) = 1u;
          SB();
          y = mfma(s8, d0, Z4); y = mfma(s8, d1, y); y = mfma(s8, d2, y); y = mfma(s8, d3, y);
          if (!any(y.x != y.x)) break;
          if (!any(max(max(mx4(d0), mx4(d1)), max(mx4(d2), mx4(d3))) == SENT) || spin > SPIN_CAP) break;
        }
        OPAQUE();
        if (w == 6) COUNT(43 + lay * 8, spin);
        STAMPW(6, 12 + lay * 16);
        xp = pk4(xr + unp(pk4(y)));
        if (lane0 < 32) *(LAS v2u*)xw = xp;
        LATE(T3B);
        bar();
        if (w == 6) { if (hasF) STATS(sp); }
        else if (lay == 0 && c == 0 && lane0 == 0) stx2(rsW, *(LAS v2u*)(L + L_TAG + 8), 0, WS_EPOCH);
      }
      if (lay == 1 && !isOut) { bar(); return; }
      int so = gat5 + xs, so1 = so + 4096, so2 = so + BLK;
      int oo = hq < 2 ? (pp * DM + slo + TKO) * 2 : 0x40000000;
      f4 xr = unp(xp), bb = unp(b2v);
      P1PREP();
      PINS(so); PINS(so1); PINS(so2); PINI(xr); PINI(bb); PINI(oo);
      unsigned a4 = 0;
      if (lay == 0) {
        CSQLOAD(); CSQSUM(); PINI(csq);
        if (w == 7) {
          unsigned t;
          for (int sp2 = 0;; ++sp2) { OPAQUE(); t = __builtin_amdgcn_raw_buffer_load_b32(rsW, ((col < 9 ? col : 8) >> 1) * BLK + (col < 9 ? col & 1 : 0) * 4096 + 3840 + 8, gat4, AUX_SC1); if (!any(t == SENT) || sp2 > SPIN_CAP) break; }
          a4 = tmax16(t) + N45;
          if (!hasF) until(a4);
        }
      }
      bar();
      if (lay == 0 && w == 7 && hasF) {
        const unsigned tn = now();
        if (lane0 == 0) __builtin_amdgcn_raw_buffer_store_b32((int)((tn - a4) << 1) > 0 ? tn : a4 & 0x7FFFFFFFu, rsW, 0, mko, AUX_SC1);
      }
      if (lay == 0) __builtin_amdgcn_s_sleep(D_X5A); else if (SCHED) LOOKAT(T5B); else { __builtin_amdgcn_s_sleep(D_X5); if (!hasF) __builtin_amdgcn_s_sleep(44); }
      f4 y;
      int spin = 0;
      for (;; ++spin) {
        OPAQUE();
        const v4u d0 = ldx(rsW, v0, so), d1 = ldx(rsW, v0 + 2048, so), d2 = ldx(rsW, v0, so1), d3 = ldx(rsW, v0 + 2048, so1), d4 = ldx(rsW, v0, so2), d5 = ldx(rsW, v0 + 2048, so2);
        SB();
        y = mfma(s8, d0, Z4); y = mfma(s8, d1, y); y = mfma(s8, d2, y); y = mfma(s8, d3, y); y = mfma(s8, d4, y); y = mfma(s8, d5, y);
        if (!any(y.x != y.x)) break;
        if (!any(max(max(max(mx4(d0), mx4(d1)), max(mx4(d2), mx4(d3))), max(mx4(d4), mx4(d5))) == SENT) || spin > SPIN_CAP) break;
      }
      OPAQUE();
      if (w == 6) COUNT(45 + lay * 8, spin);
      STAMPW(6, 17 + lay * 16);
      xp = pk4(xr + unp(pk4(unp(pk4(y)) + bb)));
      if (lay == 1) {
        if (w == 6) EV(9);
        if (blockIdx.x == 0 && w == 6) { const unsigned e_ = __builtin_amdgcn_readfirstlane(*(volatile LAS unsigned*)(L + L_BYTES)); while (now() - tst_ < 4000 + e_) {} }
        __builtin_amdgcn_raw_buffer_store_b64(xp, mkrs(p.out, 4 * DM * 2), oo, 0, OUT_AUX);
        STAMPW(6, 34);
        if (w == 6) { COUNT(61, __builtin_amdgcn_s_memtime()); }
        return;
      }
      if (lane0 < 32) *(LAS v2u*)xw = xp;
      bar();
      P1RUN();
      t0 = __builtin_amdgcn_readfirstlane(*(LAS unsigned*)(L + L_TM));
    }
#pragma unroll
    for (int T = 0; T < 4; ++T) S[T] = Z4;
    O = Z4;
    PIN4(S); PINI(O);
    if (w >= 4) {
      FRESH();
      const unsigned one = (lane & 1) ? 0x3F800000u : 0x00003F80u;
      v4u sel4 = {(col & 2) ? 0u : one, (col & 2) ? one : 0u, (col & 2) ? 0u : one, (col & 2) ? one : 0u};
      v4u sel40 = hq == 0 ? sel4 : v4u{0, 0, 0, 0};
      int so = gat1 + ab * BLK, so1 = so + 4096, so2 = gat1 + 4 * BLK + ab * 1024, so3 = gat1 + 4 * BLK + 4096 + ab * 16;
      int v2 = (16 * (w - 4) + col) * 16, v0 = v2 + hq * 1024, vs = (col < 9 ? col : 8) * 128;
      float sm = col < 9 ? (1.f / DM) : 0.f;
      lptr qw = L + QPW(w - 4);
      PINS(so); PINS(so1); PINS(so2); PINS(so3); PINI(sel4); PINI(sel40); PINI(v0); PINI(v2); PINI(vs); PINI(qw); PINI(sm);
      const int lay_ = 1;
      if (SCHED) until(t0 + T1B); else __builtin_amdgcn_s_sleep(D_X1);
      f4 d, cc; float rs;
      int spin = 0;
      for (;; ++spin) {
        OPAQUE();
        const v2u st = ldx2(rsW, vs, so3);
        SB();
        const v4u d0 = ldx(rsW, v0, so), d1 = ldx(rsW, v0, so1), d2 = ldx(rsW, v2, so2);
        SB();
        float s1 = asf(st.x) * sm, s2 = asf(st.y) * sm;
        s1 += dppf<XOR1>(s1); s2 += dppf<XOR1>(s2); s1 += dppf<XOR2>(s1); s2 += dppf<XOR2>(s2);
        SB();
        d = mfma(sel4, d0, Z4); d = mfma(sel4, d1, d); d = mfma(sel40, d2, d);
        SB();
        s1 += dppf<ROR4>(s1); s2 += dppf<ROR4>(s2); s1 += dppf<ROR8>(s1); s2 += dppf<ROR8>(s2);
        rs = __builtin_amdgcn_rsqf(fmaxf(s2 - s1 * s1, 0.f) + 1e-5f) * CS;
        cc = (-s1 * rs) * csq;
        d = d * rs + cc;
        if (!any(d.x != d.x)) break;
        if (!any(max(max(max(mx4(d0), mx4(d1)), mx4(d2)), max(st.x, st.y)) == SENT) || spin > SPIN_CAP) break;
      }
      OPAQUE();
      LATE(T1B);
      if (w == 4) COUNT(49, spin);
      STAMPW(4, 22);
      if (lane0 < 16) { LAS unsigned* qp = (LAS unsigned*)qw; qp[0] = pk(d.x, d.y); qp[16] = pk(d.z, d.w); }
    }
    bar();
    STAMPW(0, 23);
  }
}

extern "C" void kernel_launch(void* const* d_in, const int* in_sizes, int n_in, void* d_out, int out_size, void* d_ws, size_t ws_size, hipStream_t stream) {
  Params p;
  p.x = (const u16*)d_in[0]; p.kc = (const u16*)d_in[1]; p.vc = (const u16*)d_in[2]; p.wq = (const u16*)d_in[3]; p.wo = (const u16*)d_in[4];
  p.w1 = (const u16*)d_in[6]; p.b1 = (const u16*)d_in[7]; p.w2 = (const u16*)d_in[8]; p.b2 = (const u16*)d_in[9];
  p.out = (u16*)d_out; p.ws = (unsigned char*)d_ws;
  (void)hipFuncSetAttribute((const void*)fused_fwd, hipFuncAttributeMaxDynamicSharedMemorySize, L_BYTES + 16);
  hipLaunchKernelGGL(fused_fwd, dim3(256), dim3(512), L_BYTES + 16, stream, p);
}
```

```cpp
#include <hip/hip_runtime.h>
#include <stdint.h>

#define EVT 21
#define STAMPS 0

typedef unsigned short u16;
typedef unsigned long long u64;
typedef unsigned v4u __attribute__((ext_vector_type(4)));
typedef unsigned v8u __attribute__((ext_vector_type(8)));
typedef unsigned v2u __attribute__((ext_vector_type(2)));
typedef float f4 __attribute__((ext_vector_type(4)));
typedef float f2 __attribute__((ext_vector_type(2)));
typedef __bf16 bf2 __attribute__((ext_vector_type(2)));
typedef __bf16 bf8 __attribute__((ext_vector_type(8)));
typedef __bf16 bf16v __attribute__((ext_vector_type(16)));
typedef short s4v __attribute__((ext_vector_type(4)));
#define LAS __attribute__((address_space(3)))
#define DI __device__ __forceinline__
typedef __amdgpu_buffer_rsrc_t rsrc_t;

constexpr int DM = 576, SK = 8192, WIN = 2048, DH = 128, FF = 4608, QN = 4096;
constexpr unsigned SENT = 0xFFFFFFFFu;
constexpr int AUX_NT = 2, AUX_SC1 = 16;

constexpr int PG = 2 << 20, BLK = 16384, HALF = 8192;
constexpr int WS_EPOCH = 0, WS_FLAGS = 4096, WS_MARK = 8192, WS_HER = 40960, X1A_OFF = 65536, X1_MSG = 5376;
constexpr int X2_MSG = 640, X2_OFF = PG, X3_OFF = PG + 32 * BLK, X5_OFF = PG + 41 * BLK, SM_STRIDE = 60 * BLK;
constexpr int X4_OFF = 2 * PG;
constexpr int X1_OFF = 4 * PG;
constexpr int WS_USED = 5 * PG;
static_assert(X1A_OFF + 144 * X1_MSG <= PG && 2 * SM_STRIDE <= PG && 120 * BLK <= PG, "pages");
constexpr int WS_STAMP = 16 << 20;

constexpr int L_WQ = 0;
constexpr int L_WO = 32768;
constexpr int L_W2 = 65536;
constexpr int L_W1 = 90112;
constexpr int L_PA = 114688;
constexpr int L_AO = L_PA + 8 * 2176;
constexpr int L_OB = L_AO + 4096;
constexpr int L_HB = L_OB + 4 * 528;
constexpr int L_XB = L_HB + 4 * 400;
constexpr int L_QP = L_XB + 6 * 144;
constexpr int L_ML = L_QP + 512;
constexpr int L_LN0 = L_ML + 128;
constexpr int L_RDY = L_LN0 + 32;
constexpr int L_TAG = L_LN0 + 32;
constexpr int L_TM = L_TAG + 16;
constexpr int L_BYTES = L_TM + 16;
constexpr int XBS = 144, OBS = 528, HBS = 400;
static_assert(L_BYTES <= 160 * 1024 && L_OB % 16 == 0 && L_HB % 16 == 0 && L_XB % 16 == 0 && L_QP % 16 == 0 && L_ML % 16 == 0, "lds");

#define D_X1 12
#define D_X2 18
#define D_X3 20
#define D_X4 10
#define D_X5 18
#define I1 170
#define I2 165
#define I3 100
#define I4 100
#define I5 110
#define D_X5A 22
#define D_X4A 12
#define D_M 8
#define N45 130
constexpr unsigned T1B = I1, T2B = T1B + I2, T3B = T2B + I3, T4B = T3B + I4, T5B = T4B + I5;
#define SCHED 1
#define NUMA 1
#define FPOS 19
#define XPOS 3
#define SPOS 14
#define WND 10
#define TP0 2
#define TP1 5
#define TP2 8
#define TP3 11
#define D_W 11
#define PREFI 1
#define OUT_AUX 0
#define SNT 2
constexpr int SPIN_CAP = 1 << 18;

struct Params {
  const u16 *x, *kc, *vc, *wq, *wo, *w1, *b1, *w2, *b2;
  u16* out;
  unsigned char* ws;
};

DI f4 mfma(v4u a, v4u b, f4 c) { return __builtin_amdgcn_mfma_f32_16x16x32_bf16(__builtin_bit_cast(bf8, a), __builtin_bit_cast(bf8, b), c, 0, 0, 0); }
DI f4 smf(v4u a, v8u b, f4 c, int idx) { return __builtin_amdgcn_smfmac_f32_16x16x64_bf16(__builtin_bit_cast(bf8, a), __builtin_bit_cast(bf16v, b), c, idx, 0, 0); }
DI v8u cat(v4u a, v4u b) { return __builtin_shufflevector(a, b, 0, 1, 2, 3, 4, 5, 6, 7); }
DI float asf(unsigned u) { return __builtin_bit_cast(float, u); }
DI unsigned asu(float f) { return __builtin_bit_cast(unsigned, f); }
template <int C> DI unsigned dppu(unsigned x) { return (unsigned)__builtin_amdgcn_mov_dpp((int)x, C, 0xf, 0xf, true); }
template <int C> DI float dppf(float x) { return asf(dppu<C>(asu(x))); }
constexpr int XOR1 = 0xB1, XOR2 = 0x4E, ROR4 = 0x124, ROR8 = 0x128;
DI float add16(float x) { auto s = __builtin_amdgcn_permlane16_swap(asu(x), asu(x), false, false); return asf(s[0]) + asf(s[1]); }
DI float add32(float x) { auto s = __builtin_amdgcn_permlane32_swap(asu(x), asu(x), false, false); return asf(s[0]) + asf(s[1]); }
DI float vmax(float a, float b) { float r; asm("v_max_f32 %0, %1, %2" : "=v"(r) : "v"(a), "v"(b)); return r; }
DI float max16(float x) { auto s = __builtin_amdgcn_permlane16_swap(asu(x), asu(x), false, false); return vmax(asf(s[0]), asf(s[1])); }
DI float max32(float x) { auto s = __builtin_amdgcn_permlane32_swap(asu(x), asu(x), false, false); return vmax(asf(s[0]), asf(s[1])); }
DI unsigned pk(float lo, float hi) { bf2 v = {(__bf16)lo, (__bf16)hi}; return __builtin_bit_cast(unsigned, v); }
DI float rbf(float x) { return (float)(__bf16)x; }
DI float blo(unsigned u) { return asf(u << 16); }
DI float bhi(unsigned u) { return asf(u & 0xffff0000u); }
DI unsigned cl(unsigned u) { return min(u, SENT - 1); }
DI v4u split4(f4 d) {
  const unsigned h0 = pk(d.x, d.y), h1 = pk(d.z, d.w);
  return v4u{cl(h0), cl(h1), cl(pk(d.x - blo(h0), d.y - bhi(h0))), cl(pk(d.z - blo(h1), d.w - bhi(h1)))};
}
DI v2u pack4(f4 d) { return v2u{cl(pk(d.x, d.y)), cl(pk(d.z, d.w))}; }
DI unsigned mx4(v4u v) { return max(max(v.x, v.y), max(v.z, v.w)); }
DI v4u ldx(rsrc_t r, int voff, int soff) { return __builtin_amdgcn_raw_buffer_load_b128(r, voff, soff, AUX_SC1); }
DI v2u ldx2(rsrc_t r, int voff, int soff) { return __builtin_amdgcn_raw_buffer_load_b64(r, voff, soff, AUX_SC1); }
DI void stx(rsrc_t r, v4u v, int voff, int soff) { __builtin_amdgcn_raw_buffer_store_b128(v, r, voff, soff, AUX_SC1); }
DI void stx2(rsrc_t r, v2u v, int voff, int soff) { __builtin_amdgcn_raw_buffer_store_b64(v, r, voff, soff, AUX_SC1); }
DI v4u ldn(rsrc_t r, int voff, int soff) { return __builtin_amdgcn_raw_buffer_load_b128(r, voff, soff, AUX_SC1 | SNT); }
DI void stn(rsrc_t r, v4u v, int voff, int soff) { __builtin_amdgcn_raw_buffer_store_b128(v, r, voff, soff, AUX_SC1 | SNT); }
DI void bar() { asm volatile("s_waitcnt lgkmcnt(0)\n\ts_barrier" ::: "memory"); }
DI void drain() { asm volatile("s_waitcnt vmcnt(0)" ::: "memory"); }
DI rsrc_t mkrs(const void* p, unsigned bytes) { return __builtin_amdgcn_make_buffer_rsrc((void*)p, (short)0, (int)bytes, 0x00020000); }
DI void dma(rsrc_t r, int voff, int soff, unsigned ldsaddr) {
  unsigned keep;
  asm volatile("s_nop 4\n\ts_mov_b32 %0, m0\n\ts_mov_b32 m0, %1\n\ts_nop 0\n\tbuffer_load_dwordx4 %2, %3, %4 offen nt lds\n\ts_mov_b32 m0, %0"
               : "=&s"(keep) : "s"(ldsaddr), "v"(voff), "s"(r), "s"(soff) : "memory");
}
typedef LAS unsigned char* lptr;
DI v4u lds16(lptr L, int off) { return *(LAS v4u*)(L + off); }
#define SB() __builtin_amdgcn_sched_barrier(0)
DI float dot2(unsigned a, unsigned b, float c) { return __builtin_amdgcn_fdot2_f32_bf16(__builtin_bit_cast(bf2, a), __builtin_bit_cast(bf2, b), c, false); }
DI v4u trfrag(LAS unsigned char* L, int off, int step) {
  s4v a = __builtin_amdgcn_ds_read_tr16_b64_v4i16((LAS s4v*)(L + off));
  s4v b = __builtin_amdgcn_ds_read_tr16_b64_v4i16((LAS s4v*)(L + off + step));
  v2u x = __builtin_bit_cast(v2u, a), y = __builtin_bit_cast(v2u, b);
  return v4u{x.x, x.y, y.x, y.y};
}
DI bool any(bool b) { return __builtin_amdgcn_ballot_w64(b) != 0; }
#define OPAQUE() asm volatile("" ::: "memory")
#define Z4 f4{0.f, 0.f, 0.f, 0.f}
#define FRESH() int lane = lane0; asm volatile("" : "+v"(lane)); const int col = lane & 15, hq = lane >> 4, q4 = col >> 2, pp = lane & 3; (void)col; (void)hq; (void)q4; (void)pp
#define SEL8() const unsigned one = (lane & 1) ? 0x3F800000u : 0x00003F80u; \
  const v4u sel8 = {((col & 7) >> 1) == 0 ? one : 0u, ((col & 7) >> 1) == 1 ? one : 0u, ((col & 7) >> 1) == 2 ? one : 0u, ((col & 7) >> 1) == 3 ? one : 0u}
#define A128() const int z128 = ((q4 >> 1) & 1) | ((hq & 1) << 1), a128 = hq * 1024 + q4 * 128 + pp * 8
#define STAMP(i) do {} while (0)
#define STAMPW(ww, i) do {} while (0)
#define COUNT(i, v) do {} while (0)

#define PIN4(a) asm volatile("" : "+v"(a[0]), "+v"(a[1]), "+v"(a[2]), "+v"(a[3]))
#define PINI(x) asm volatile("" : "+v"(x))
#define PINS(x) asm volatile("" : "+s"(x))
#define TKO (32 * (w & 1) + 8 * q4 + 4 * (hq & 1))
#define HFO (32 * w + 16 * (hq >> 1) + 4 * q4 + 2 * (hq & 1))
#define KLOAD(i) K[i] = __builtin_amdgcn_raw_buffer_load_b128(rsK, kvo, __builtin_amdgcn_readfirstlane((((i) + rot) & 15) * 1024), AUX_NT)
#define VLOAD(i) V[i] = __builtin_amdgcn_raw_buffer_load_b128(rsV, vvo, __builtin_amdgcn_readfirstlane((((i) + rot) & 15) * 1024), AUX_NT)
#define QPW(ww) (L_QP + (q4 & 1) * 256 + (((ww) >> 1) * 8 + 2 * ((col >> 1) & 1) + ((ww) & 1)) * 16 + (2 * (col >> 3) + (col & 1)) * 4)
#define TT(i) do { if (STAMPS == 1 || (STAMPS && (i) == 0)) { SB(); tt[i] = __builtin_amdgcn_s_memrealtime(); asm volatile("s_waitcnt lgkmcnt(0)" : "+s"(tt[i])); SB(); } } while (0)
DI unsigned now() { return (unsigned)__builtin_amdgcn_s_memrealtime() & 0x7FFFFFFFu; }
DI void until(unsigned t) { for (int i = 0; i < 4096 && ((t - now()) & 0x7FFFFFFFu) - 1u < 0x3FFFFFFFu; ++i) {} }
DI unsigned tmax16(unsigned t) {
  const unsigned ref = __builtin_amdgcn_readfirstlane(t);
  int d = (int)((t - ref) << 1);
  d = max(d, (int)dppu<XOR1>(d)); d = max(d, (int)dppu<XOR2>(d)); d = max(d, (int)dppu<ROR4>(d)); d = max(d, (int)dppu<ROR8>(d));
  return (ref + (unsigned)(__builtin_amdgcn_readfirstlane(d) >> 1)) & 0x7FFFFFFFu;
}
#define RTTN 50
#define LATE(T) do { if (SCHED && lay_ == 1 && spin > 0 && lane0 == 0) __hip_atomic_fetch_max((LAS int*)(L + L_TM + 12), (int)(((now() - (t0 + (T) + RTTN)) << 1)) >> 1, __ATOMIC_RELAXED, __HIP_MEMORY_SCOPE_WORKGROUP); } while (0)
#define LOOKAT(T) until(t0 + (T) + (unsigned)__builtin_amdgcn_readfirstlane(*(volatile LAS int*)(L + L_TM + 12)))
DI u64 next_tag(v2u e) { return (((u64)e.y << 32) | e.x) * 6364136223846793005ull + 1442695040888963407ull; }
DI void dma128(int ln, rsrc_t r, int rowbytes, int soff, unsigned lds, int i) {
  dma(r, (ln >> 3) * rowbytes + ((((ln & 7) >> 1) ^ (((ln >> 4) & 1) | ((i & 1) << 1))) * 32) + (ln & 1) * 16, soff + 8 * i * rowbytes, lds + i * 1024);
}
DI f4 unp(v2u v) { return f4{blo(v.x), bhi(v.x), blo(v.y), bhi(v.y)}; }
DI v2u pk4(f4 v) { return v2u{pk(v.x, v.y), pk(v.z, v.w)}; }

__global__ void __launch_bounds__(512, 2) fused_fwd(Params p) {
  extern __shared__ __attribute__((aligned(16))) unsigned char smem[];
  const lptr L = (lptr)smem;
  const int tid = threadIdx.x, lane0 = tid & 63, w = __builtin_amdgcn_readfirstlane(tid >> 6);
  const int c = blockIdx.x, xc = c & 7, j = c >> 3, grp = j >> 4, u = j & 15, g = 2 * xc + grp;
  u64 tt[8] = {};
  const unsigned tst_ = now();
#define EV(k) do { if (EVT == (k) && lane0 == 0) *(volatile LAS unsigned*)(L + L_BYTES) = now() - tst_; } while (0)
#define EVV(k, v) do { if (EVT == (k) && lane0 == 0) *(volatile LAS unsigned*)(L + L_BYTES) = (v); } while (0)
#define EVA(k, v) do { if (EVT == (k) && lane0 == 0) __hip_atomic_fetch_add((LAS unsigned*)(L + L_BYTES), (unsigned)(v), __ATOMIC_RELAXED, __HIP_MEMORY_SCOPE_WORKGROUP); } while (0)

  const int ab = u >> 2, as = u & 3;
  const bool hasP1 = u <= 8, hasP3 = u >= 7;
  const unsigned ldsb = (unsigned)(size_t)L;
  const rsrc_t rsW = mkrs((const void*)(((size_t)p.ws + PG - 1) & ~(size_t)(PG - 1)), WS_USED);
  constexpr float CS = 0.08838834764831845f * 1.4426950408889634f;
  v8u K8[8], V8[8];
  int pub1, pub2, pub3, pub4, pub5, gat1, gat2, gat3, gat4, gat5, gatc, mko, cls, slo, wofs, w1ofs, w2ofs, b1ofs;
  bool hasF, isOut;
  lptr qa, pw, pr, aow, mlw, mbr, mmr; int qidx, pidx, mst; v4u pm;
  {
    const int lane = lane0, col = lane & 15, hq = lane >> 4, q4 = col >> 2, pp = lane & 3;
    const size_t kvbase = ((size_t)(ab * 16 + g) * SK + (SK - WIN) + as * 512 + w * 64) * DH;
    const rsrc_t rsK = mkrs(p.kc + kvbase, 16384), rsV = mkrs(p.vc + kvbase, 16384);
    const int rot = __builtin_amdgcn_readfirstlane((w >> 1) + 4 * (j & 3) + 2 * xc);
    const int kvo = q4 * 256 + (4 * hq + pp) * 16, vvo = lane * 16;
    const int r1 = u < 8 ? xc : 8, gq1 = u < 8 ? 8 * grp + u : g, n3 = (u + 9) & 15;
    v4u K[16], V[16], csw = {0, 0, 0, 0};
    u64 tag = 0;
    if (w >= 4) {
      if (w == 4 && hasP1) {
        const rsrc_t rx = mkrs(p.x, 4 * DM * 2);
        const int xo_ = (hq * DM + col * 8) * 2;
        v4u xv[5];
#pragma unroll
        for (int k = 0; k < 4; ++k) xv[k] = __builtin_amdgcn_raw_buffer_load_b128(rx, xo_ + 256 * k, 0, 0);
        xv[4] = __builtin_amdgcn_raw_buffer_load_b128(rx, col < 8 ? xo_ + 1024 : 0x40000000, 0, 0);
        if (lane == 0) *(volatile LAS unsigned*)(L + L_RDY) = 0u;
        bar();
        SB();
        const int ks = r1 >> 1;
        const v4u xs = ks == 0 ? xv[0] : (ks == 1 ? xv[1] : (ks == 2 ? xv[2] : (ks == 3 ? xv[3] : xv[4])));
        if ((col >> 3) == (r1 & 1)) *(LAS v4u*)(L + L_XB + hq * XBS + (col & 7) * 16) = xs;
        float s1 = 0.f, s2 = 0.f, s3 = 0.f, s4 = 0.f;
#pragma unroll
        for (int k = 0; k < 5; ++k) {
          s1 = dot2(xv[k][0], 0x3F803F80u, s1); s2 = dot2(xv[k][0], xv[k][0], s2); s3 = dot2(xv[k][1], 0x3F803F80u, s3); s4 = dot2(xv[k][1], xv[k][1], s4);
          s1 = dot2(xv[k][2], 0x3F803F80u, s1); s2 = dot2(xv[k][2], xv[k][2], s2); s3 = dot2(xv[k][3], 0x3F803F80u, s3); s4 = dot2(xv[k][3], xv[k][3], s4);
        }
        s1 += s3; s2 += s4;
        s1 += dppf<XOR1>(s1); s2 += dppf<XOR1>(s2); s1 += dppf<XOR2>(s1); s2 += dppf<XOR2>(s2);
        s1 += dppf<ROR4>(s1); s2 += dppf<ROR4>(s2); s1 += dppf<ROR8>(s1); s2 += dppf<ROR8>(s2);
        const float mu = s1 * (1.f / DM), rs = __builtin_amdgcn_rsqf(fmaxf(s2 * (1.f / DM) - mu * mu, 0.f) + 1e-5f) * CS;
        if (col == 0) *(LAS f2*)(L + L_LN0 + hq * 8) = f2{mu, rs};
        OPAQUE();
        if (lane == 0) *(volatile LAS unsigned*)(L + L_RDY) = 1u;
        OPAQUE(); SB();
#pragma unroll
        for (int i = 0; i < 4; ++i) KLOAD(i);
      } else {
        KLOAD(0); KLOAD(1); KLOAD(2);
        if (hasP1) bar();
        KLOAD(3);
      }
    } else {
      unsigned nearb = 0;
      if (w == 3) {
        const v4u SENT4 = {SENT, SENT, SENT, SENT};
        const int a2 = lane < 40 ? X2_OFF + (2 * g + (ab >> 1)) * BLK + ((ab & 1) * 4 + as) * X2_MSG + lane * 16 : 0x40000000;
        const int a3 = (hasP3 && lane < 32) ? X3_OFF + n3 * BLK + g * 512 + lane * 16 : 0x40000000;
        u64 ta, tb;
        asm volatile("buffer_store_dwordx4 %2, %3, %5, 0 offen sc1\n\tbuffer_store_dwordx4 %2, %3, %5, %6 offen sc1\n\t"
                     "buffer_store_dwordx4 %2, %4, %5, 0 offen sc1\n\tbuffer_store_dwordx4 %2, %4, %5, %6 offen sc1\n\t"
                     "s_waitcnt vmcnt(3)\n\ts_memtime %0\n\ts_waitcnt vmcnt(2)\n\ts_memtime %1\n\ts_waitcnt lgkmcnt(0)"
                     : "=&s"(ta), "=&s"(tb) : "v"(SENT4), "v"(a2), "v"(a3), "s"(rsW), "s"(HALF) : "memory");
        COUNT(56, tb - ta);
        nearb = ((unsigned)(tb - ta) > 140u ? 1u : 0u) | (((unsigned)__builtin_amdgcn_s_getreg(20 | (3 << 11)) >> 1) & 2u);
      }
      v2u ep;
      asm volatile("buffer_load_dwordx2 %0, off, %1, 0 sc1" : "=v"(ep) : "s"(rsW) : "memory");
      {
        const rsrc_t rq = mkrs(hasP1 ? p.wq : p.wo, DM * QN * 2);
        const int rb = hasP1 ? QN * 2 : DM * 2, so = hasP1 ? 64 * r1 * (QN * 2) + gq1 * 512 + w * 128 : (256 * g + 64 * w) * (DM * 2) + n3 * 128;
#pragma unroll
        for (int i = 0; i < 8; ++i) dma128(lane, rq, rb, so, ldsb + (hasP1 ? L_WQ : L_WO) + w * 8192, i);
        if (hasP1) bar();
      }
      OPAQUE(); SB();
      v4u pf = {0, 0, 0, 0};
      if (PREFI) {
        u64 pc; unsigned len;
        asm volatile("s_getpc_b64 %0\n.Lfwd_pc:\n\ts_mov_b32 %1, .Lfwd_end-.Lfwd_pc" : "=s"(pc), "=s"(len));
        pf = __builtin_amdgcn_raw_buffer_load_b128(mkrs((const void*)(pc & ~15ull), len & ~15u), w == 2 ? j * 1024 + lane * 16 : 0x40000000, 0, 0);
      }
      OPAQUE(); SB();
      TT(1);
#pragma unroll
      for (int i = 0; i < 4; ++i) KLOAD(i);
      OPAQUE(); SB();
      asm volatile("s_waitcnt vmcnt(%1)" : "+v"(ep) : "i"(12 + PREFI) : "memory");
      tag = next_tag(ep);
      if (w == 3 && lane == 0) { const u64 f = tag + (u64)c * 0x9E3779B97F4A7C15ull; stx2(rsW, v2u{((unsigned)f & ~3u) | nearb, (unsigned)(f >> 32)}, 0, WS_FLAGS + c * 8); }
      if (hasP1) {
        A128();
        asm volatile("s_waitcnt vmcnt(%0)" ::"i"(4 + PREFI) : "memory");
        TT(2);
        v4u A[8];
#pragma unroll
        for (int t = 0; t < 4; ++t) { const int base = L_WQ + w * 8192 + a128 + (t ^ z128) * 32; A[2 * t] = trfrag(L, base, 512); A[2 * t + 1] = trfrag(L, base + 4096, 512); }
        for (int spin = 0; *(volatile LAS unsigned*)(L + L_RDY) == 0u && spin < (SPIN_CAP << 4); ++spin) __builtin_amdgcn_s_sleep(1);
        OPAQUE();
        TT(3);
        const v4u xf0 = lds16(L, L_XB + pp * XBS + hq * 16), xf1 = lds16(L, L_XB + pp * XBS + hq * 16 + 64);
        const f2 ln = *(LAS f2*)(L + L_LN0 + pp * 8);
        SB();
        f4 d = Z4, e = Z4;
#pragma unroll
        for (int t = 0; t < 4; ++t) {
          const unsigned m = q4 == t ? ~0u : 0u, o = m & 0x3F803F80u;
          d = mfma(A[2 * t], xf0 & m, d); e = mfma(A[2 * t], v4u{o, o, o, o}, e);
          d = mfma(A[2 * t + 1], xf1 & m, d); e = mfma(A[2 * t + 1], v4u{o, o, o, o}, e);
        }
        d = (d - ln.x * e) * ln.y;
        stn(rsW, v4u{pk(d.x, d.y), (unsigned)tag & 0xBFFFBFFFu, pk(d.z, d.w), (unsigned)(tag >> 32) & 0xBFFFBFFFu},
            pp * 1024 + (16 * w + 4 * q4 + hq) * 16, X1A_OFF + (gq1 * 9 + r1) * X1_MSG);
        csw = split4(e);
      }
      TT(4);
      asm volatile("" ::"v"(pf));
    }
    OPAQUE(); SB();
    v4u fa;
#define WIN() do { if (WND) asm volatile("s_waitcnt vmcnt(%0)" ::"i"(WND - 1) : "memory"); } while (0)
#pragma unroll
    for (int i = 4; i < 16; ++i) {
      if (i >= WND) WIN();
      KLOAD(i); SB();
      if (i == FPOS) { if (w < 4) fa = ldn(rsW, lane * 16 + (w & 1) * 1024, WS_FLAGS); OPAQUE(); SB(); }
    }
    {
      const int fi0 = 14 * grp + (u <= 6 ? u : u - 2);
      hasF = (u != 7) && (u != 8) && (fi0 < 27);
      const int fi = fi0 < 27 ? fi0 : 0;
      const int n4 = fi < 24 ? xc : 8, f4i = fi < 24 ? fi : 3 * xc + fi - 24, f5 = 3 * xc + fi / 9, n5 = fi % 9;
      const int sl = u == 8 ? 8 : (hasF ? n4 : xc);
      isOut = grp == 0 && (u == 0 || (u == 8 && xc == 0));
      pub1 = X1_OFF + 5 * gq1 * BLK + (r1 < 8 ? r1 * 1024 : 4 * BLK); pub2 = X2_OFF + (2 * g + (ab >> 1)) * BLK + ((ab & 1) * 4 + as) * X2_MSG;
      pub3 = X3_OFF + n3 * BLK + g * 512; pub4 = X4_OFF + (5 * f4i + (n4 >> 1)) * BLK + (n4 & 1) * 4096; pub5 = X5_OFF + (2 * n5 + (f5 >> 4)) * BLK + (f5 & 15) * 512;
      gat1 = X1_OFF + 5 * g * BLK; gat2 = X2_OFF + (2 * g + ((w & 3) >> 1)) * BLK + (w & 1) * 4 * X2_MSG; gat3 = X3_OFF + sl * BLK;
      gat4 = X4_OFF + 5 * f5 * BLK; gat5 = X5_OFF + 2 * sl * BLK; gatc = X1A_OFF + g * 9 * X1_MSG; mko = WS_MARK + (n5 * 24 + f5) * 128;
      cls = (gq1 >> 1) | (n3 << 4) | ((f4i / 3) << 8) | (n5 << 12) | (r1 << 16) | (u << 20) | (g << 24);
      slo = 64 * sl; wofs = 256 * g * (DM * 2) + n3 * 128; w1ofs = 64 * n4 * (FF * 2) + f4i * 384; w2ofs = 192 * f5 * (DM * 2) + n5 * 128; b1ofs = 192 * f5;
      PINS(pub1); PINS(pub2); PINS(pub3); PINS(pub4); PINS(pub5); PINS(gat1); PINS(gat2); PINS(gat3); PINS(gat4); PINS(gat5); PINS(gatc); PINS(mko); PINS(cls);
      PINS(slo); PINS(wofs); PINS(w1ofs); PINS(w2ofs); PINS(b1ofs);
      qa = L + L_QP + (pp * 4 + hq) * 16;
      qidx = (lane & 1) ? 0xEEEE : 0x4444; pidx = pp * 0x1111;
      pw = L + L_PA + w * 2176 + (2 * hq + (pp >> 1)) * 272 + (2 * (pp & 1) + (q4 >> 1)) * 64 + (q4 & 1) * 8;
      pr = L + L_PA + w * 2176 + (hq * 4 + (col >> 3) * 2 + (q4 & 1)) * 16;
      aow = L + L_AO + w * 512 + ((lane >> 5) * 128 + 8 * col + 4 * (hq & 1)) * 2;
      mlw = L + L_ML + (w * 2 + (lane >> 5)) * 8;
      mbr = L + L_AO + (w & 1) * 256 + hq * 512 + col * 16; mmr = L + L_ML + (w & 1) * 8 + hq * 16;
      mst = hq < 2 ? (w & 1) * 256 + col * 16 + hq * 8 : 0x40000000;
      const unsigned hm = (lane & 1) ? 0xFFFF0000u : 0xFFFFu;
#pragma unroll
      for (int k = 0; k < 4; ++k) pm[k] = ((col & 7) >> 1) == k ? hm : 0u;
      PINI(qa); PINI(pw); PINI(pr); PINI(aow); PINI(mlw); PINI(mbr); PINI(mmr); PINI(qidx); PINI(pidx); PINI(mst); PINI(pm);
      const v4u z = {0, 0, 0, 0};
      const lptr pa = L + L_PA + w * 2176 + lane * 16;
      *(LAS v4u*)pa = z; *(LAS v4u*)(pa + 1024) = z; if (lane < 8) *(LAS v4u*)(pa + 2048) = z;
      if (w == 7 && lane == 0) { *(LAS unsigned*)(L + L_TM + 4) = 0u; *(LAS unsigned*)(L + L_TM + 12) = 0u; }
      if (w == 7 && lane < 36) { *(LAS unsigned*)(L + L_XB + 4 * XBS + lane * 4) = 0x3F803F80u; *(LAS unsigned*)(L + L_XB + 5 * XBS + lane * 4) = 0u; }
    }
    SB();
    auto transp = [&](int T) {
      const bool o1 = lane & 1, o2 = lane & 2;
#pragma unroll
      for (int d = 0; d < 4; ++d) {
        const unsigned m0 = K[4 * T][d], m1 = K[4 * T + 1][d], m2 = K[4 * T + 2][d], m3 = K[4 * T + 3][d];
        const unsigned s0 = dppu<XOR1>(m0), s1 = dppu<XOR1>(m1), s2 = dppu<XOR1>(m2), s3 = dppu<XOR1>(m3);
        const unsigned n0 = o1 ? s1 : m0, n1 = o1 ? m1 : s0, n2 = o1 ? s3 : m2, n3_ = o1 ? m3 : s2;
        const unsigned t0 = dppu<XOR2>(n0), t1 = dppu<XOR2>(n1), t2 = dppu<XOR2>(n2), t3 = dppu<XOR2>(n3_);
        K8[2 * T][d] = o2 ? t2 : n0; K8[2 * T + 1][d] = o2 ? n2 : t0;
        K8[2 * T][4 + d] = o2 ? t3 : n1; K8[2 * T + 1][4 + d] = o2 ? n3_ : t1;
      }
      asm volatile("" : "+v"(K8[2 * T]), "+v"(K8[2 * T + 1]));
    };
    int x1a = hq * X1_MSG + (16 * (w & 3) + col) * 16 + ab * 1024, x1b = x1a + 4 * X1_MSG, x1c = x1a + (8 - hq) * X1_MSG;
    int ta = (w < 4 && hasP1 && pp == 0) ? X1A_OFF + (gq1 * 9 + r1) * X1_MSG + 4096 + (16 * w + 4 * q4 + hq) * 16 : 0x40000000;
    const unsigned one = (lane & 1) ? 0x3F800000u : 0x00003F80u;
    v4u selT = {(col & 2) ? 0u : one, 0u, (col & 2) ? one : 0u, 0u}, selT0 = hq == 0 ? selT : v4u{0, 0, 0, 0};
    unsigned tgx = (unsigned)tag & 0xBFFFBFFFu, tgy = (unsigned)(tag >> 32) & 0xBFFFBFFFu;
    lptr qw = L + QPW(w & 3);
    PINI(x1a); PINI(x1b); PINI(x1c); PINI(ta); PINI(csw); PINI(selT); PINI(selT0); PINI(tgx); PINI(tgy); PINI(qw);
    v4u g0, g1, g2;
#pragma unroll
    for (int i = 0; i < 16; ++i) {
      WIN(); VLOAD(i); SB();
      if (i == FPOS - 16) { if (w < 4) fa = ldn(rsW, lane * 16 + (w & 1) * 1024, WS_FLAGS); OPAQUE(); SB(); }
      if (i == XPOS) {
        if (w < 4) { g0 = ldn(rsW, x1a, gatc); g1 = ldn(rsW, x1b, gatc); g2 = ldn(rsW, x1c, gatc); }
        OPAQUE(); SB();
      }
      if (i == SPOS) {
        PINI(fa);
        if (w < 4) {
          const u64 e0 = tag + (u64)(lane * 2 + (w & 1) * 128) * 0x9E3779B97F4A7C15ull, e1 = e0 + 0x9E3779B97F4A7C15ull;
          int spin = 0;
#pragma nounroll
          for (; spin < SPIN_CAP; ++spin) {
            if (!any(((fa.x ^ (unsigned)e0) > 3u) || fa.y != (unsigned)(e0 >> 32) || ((fa.z ^ (unsigned)e1) > 3u) || fa.w != (unsigned)(e1 >> 32))) break;
            OPAQUE();
            fa = ldn(rsW, lane * 16 + (w & 1) * 1024, WS_FLAGS);
            PINI(fa);
          }
          if (w == 0) COUNT(40, spin);
          stn(rsW, csw, ta, 0);
          if (!(w & 1)) {
            const int g0v = __builtin_popcountll(__builtin_amdgcn_ballot_w64((fa.x & 3u) == 1u)) + __builtin_popcountll(__builtin_amdgcn_ballot_w64((fa.z & 3u) == 1u));
            const int g1v = __builtin_popcountll(__builtin_amdgcn_ballot_w64((fa.x & 3u) == 3u)) + __builtin_popcountll(__builtin_amdgcn_ballot_w64((fa.z & 3u) == 3u));
            const unsigned se = (unsigned)__builtin_amdgcn_ballot_w64(fa.x & 2u), so_ = (unsigned)__builtin_amdgcn_ballot_w64(fa.z & 2u);
            unsigned hm = 0;
#pragma unroll
            for (int k = 0; k < 4; ++k) hm |= ((se >> k) & 1u) << (2 * k) | ((so_ >> k) & 1u) << (2 * k + 1);
            if (g0v > g1v) hm ^= 0xFFu;
            if (w == 0) { EVV(20, max(g0v, g1v) * 10); EVV(21, min(g0v, g1v) * 100); EVV(22, 0); }
            if (!NUMA) hm = 0;
            if (w == 0 && lane == 0) { *(LAS unsigned*)(L + L_TM + 8) = hm; *(LAS u64*)(L + L_TAG + 8) = tag; }
            if (hasF) {
              unsigned sn = SENT; PINI(sn);
              const v4u SENT4 = {sn, sn, sn, sn};
              const int a4 = pub4 + (((hm >> ((cls >> 8) & 15)) & 1) << 13) + w * 1024, n5 = (cls >> 12) & 15;
              stn(rsW, SENT4, lane * 16, a4);
              if (w == 0 || lane < 56) stn(rsW, SENT4, lane * 16, a4 + 1024);
              if (lane < 32 && (n5 == 8 || (((hm >> n5) ^ 1) & 1) == (unsigned)(w >> 1))) stn(rsW, SENT4, lane * 16, pub5 + (w >> 1) * HALF);
            }
          }
        }
        OPAQUE(); SB();
      }
      if (i == TP0) { transp(0); SB(); }
      if (i == TP1) { transp(1); SB(); }
      if (i == TP2) { transp(2); SB(); }
      if (i == TP3) { transp(3); SB(); }
    }
#pragma unroll
    for (int m = 0; m < 8; ++m) V8[m] = cat(V[2 * m], V[2 * m + 1]);
    OPAQUE(); SB();
    STAMPW(0, 3); STAMPW(7, 4);
    if (w == 0) { COUNT(0, tt[0]); COUNT(60, __builtin_amdgcn_s_memtime()); COUNT(59, __builtin_amdgcn_s_memrealtime()); if (hasP1) { COUNT(1, tt[1]); COUNT(2, tt[2]); COUNT(5, tt[3]); COUNT(35, tt[4]); } }
    asm volatile("" : "+v"(g0), "+v"(g1), "+v"(g2));
    if (w < 4) {
      int spin = 0;
#pragma nounroll
      for (; spin < SPIN_CAP; ++spin) {
        if (!any(g0.y != tgx || g0.w != tgy || g1.y != tgx || g1.w != tgy || g2.y != tgx || g2.w != tgy)) break;
        OPAQUE();
        g0 = ldn(rsW, x1a, gatc); g1 = ldn(rsW, x1b, gatc); g2 = ldn(rsW, x1c, gatc);
        asm volatile("" : "+v"(g0), "+v"(g1), "+v"(g2));
      }
      f4 q = mfma(selT, g0, Z4); q = mfma(selT, g1, q); q = mfma(selT0, g2, q);
      if (lane < 16) { LAS unsigned* qp = (LAS unsigned*)qw; qp[0] = pk(q.x, q.y); qp[16] = pk(q.z, q.w); }
      if (w == 0) COUNT(41, spin);
      STAMPW(0, 6);
    }
  }
  f4 S[4] = {Z4, Z4, Z4, Z4}, O = Z4;
  PIN4(S); PINI(O);
  bar();
  STAMPW(0, 7);
  {
    const unsigned hm = __builtin_amdgcn_readfirstlane(*(LAS unsigned*)(L + L_TM + 8));
    if (w == 0) COUNT(55, hm);
    const int own = ((hm >> xc) & 1) << 13;
    pub1 += ((hm >> (cls & 15)) & 1) << 13; pub2 += own ^ HALF; pub3 += (((hm >> ((cls >> 4) & 15)) & 1) << 13) ^ HALF;
    pub4 += ((hm >> ((cls >> 8) & 15)) & 1) << 13; pub5 += (((hm >> ((cls >> 12) & 15)) & 1) << 13) ^ HALF;
    gat1 += own; gat2 += own ^ HALF; gat3 += own ^ HALF; gat4 += own; gat5 += own ^ HALF;
  }

  v2u xp = {0, 0}, b2v = {0, 0}; unsigned b1v = 0;
  f4 csq = Z4; f2 cs1 = {0.f, 0.f};
  unsigned t0 = 0;
  const int w_ = w, ab_ = ab, pub1_ = pub1, pub2_ = pub2, pub3_ = pub3, pub4_ = pub4, pub5_ = pub5, gat1_ = gat1, gat2_ = gat2, gat3_ = gat3, gat4_ = gat4, gat5_ = gat5, gatc_ = gatc, mko_ = mko, cls_ = cls, slo_ = slo, wofs_ = wofs, w1ofs_ = w1ofs, w2ofs_ = w2ofs, b1ofs_ = b1ofs;
#pragma nounroll
  for (int lay = 0; lay < 2; ++lay) {
    const int xs = __builtin_amdgcn_readfirstlane(lay * SM_STRIDE), x4 = __builtin_amdgcn_readfirstlane(lay * (PG + HALF));
    int w = w_, ab = ab_, pub1 = pub1_, pub2 = pub2_, pub3 = pub3_, pub4 = pub4_, pub5 = pub5_, gat1 = gat1_, gat2 = gat2_, gat3 = gat3_, gat4 = gat4_, gat5 = gat5_, gatc = gatc_, mko = mko_, cls = cls_, slo = slo_, wofs = wofs_, w1ofs = w1ofs_, w2ofs = w2ofs_, b1ofs = b1ofs_;
    asm volatile("" : "+s"(w), "+s"(ab), "+s"(pub1), "+s"(pub2), "+s"(pub3), "+s"(pub4), "+s"(pub5), "+s"(gat1), "+s"(gat2), "+s"(gat3), "+s"(gat4), "+s"(gat5), "+s"(gatc), "+s"(mko), "+s"(cls), "+s"(slo), "+s"(wofs), "+s"(w1ofs), "+s"(w2ofs), "+s"(b1ofs));
    const int lay_ = lay;
    {
      v4u AF[8];
      const v4u Q0 = lds16(qa, 0), Q1 = lds16(qa, 256);
      SB();
#pragma unroll
      for (int T = 0; T < 4; ++T) S[T] = smf(Q0, K8[2 * T], S[T], qidx);
#pragma unroll
      for (int T = 0; T < 4; ++T) S[T] = smf(Q1, K8[2 * T + 1], S[T], qidx);
      float s0, s1;
      asm("s_nop 7\n\t"
          "v_add_f32_dpp %0, %2, %3 quad_perm:[0,1,2,3] row_mask:0x1 bank_mask:0xf\n\tv_add_f32_dpp %1, %4, %5 quad_perm:[0,1,2,3] row_mask:0x1 bank_mask:0xf\n\t"
          "v_add_f32_dpp %0, %6, %7 quad_perm:[0,1,2,3] row_mask:0x2 bank_mask:0xf\n\tv_add_f32_dpp %1, %8, %9 quad_perm:[0,1,2,3] row_mask:0x2 bank_mask:0xf\n\t"
          "v_add_f32_dpp %0, %10, %11 quad_perm:[0,1,2,3] row_mask:0x4 bank_mask:0xf\n\tv_add_f32_dpp %1, %12, %13 quad_perm:[0,1,2,3] row_mask:0x4 bank_mask:0xf\n\t"
          "v_add_f32_dpp %0, %14, %15 quad_perm:[0,1,2,3] row_mask:0x8 bank_mask:0xf\n\tv_add_f32_dpp %1, %16, %17 quad_perm:[0,1,2,3] row_mask:0x8 bank_mask:0xf"
          : "=&v"(s0), "=&v"(s1)
          : "v"(S[0].x), "v"(S[0].y), "v"(S[0].z), "v"(S[0].w), "v"(S[1].x), "v"(S[1].y), "v"(S[1].z), "v"(S[1].w),
            "v"(S[2].x), "v"(S[2].y), "v"(S[2].z), "v"(S[2].w), "v"(S[3].x), "v"(S[3].y), "v"(S[3].z), "v"(S[3].w));
      auto sw = __builtin_amdgcn_permlane32_swap(asu(s0), asu(s1), false, false);
      float mx;
      asm("v_max_f32 %0, %1, %2\n\ts_nop 1\n\tv_max_f32_dpp %0, %0, %0 quad_perm:[1,0,3,2] row_mask:0xf bank_mask:0xf\n\ts_nop 1\n\t"
          "v_max_f32_dpp %0, %0, %0 quad_perm:[2,3,0,1] row_mask:0xf bank_mask:0xf\n\ts_nop 1\n\tv_max_f32_dpp %0, %0, %0 row_ror:4 row_mask:0xf bank_mask:0xf\n\ts_nop 1\n\t"
          "v_max_f32_dpp %0, %0, %0 row_ror:8 row_mask:0xf bank_mask:0xf\n\ts_nop 1\n\tv_max_f32_dpp %0, %0, %0 row_bcast:15 row_mask:0xa bank_mask:0xf\n\ts_nop 0"
          : "=&v"(mx) : "v"(asf(sw[0])), "v"(asf(sw[1])));
      const float m0 = asf(__builtin_amdgcn_readlane(asu(mx), 31)), m1 = asf(__builtin_amdgcn_readlane(asu(mx), 63));
      const float p0 = __builtin_amdgcn_exp2f(s0 - m0), p1 = __builtin_amdgcn_exp2f(s1 - m1);
      const unsigned pb = pk(p0, p1);
      *(LAS u16*)(pw) = (u16)pb; *(LAS u16*)(pw + 20) = (u16)pb; *(LAS u16*)(pw + 32) = (u16)(pb >> 16); *(LAS u16*)(pw + 52) = (u16)(pb >> 16);
#pragma unroll
      for (int m = 0; m < 8; ++m) AF[m] = lds16(pr, m * 272);
      SB();
      auto sl2 = __builtin_amdgcn_permlane32_swap(asu(p0), asu(p1), false, false);
      float ls = asf(sl2[0]) + asf(sl2[1]);
      ls += dppf<XOR1>(ls); ls += dppf<XOR2>(ls); ls += dppf<ROR4>(ls); ls += dppf<ROR8>(ls);
      ls += asf((unsigned)__builtin_amdgcn_update_dpp(0, (int)asu(ls), 0x142, 0xa, 0xf, false));
      if ((lane0 & 31) == 31) *(LAS f2*)mlw = f2{mx, ls};
      SB();
#pragma unroll
      for (int m = 0; m < 8; ++m) O = smf(AF[m], V8[m], O, pidx);
      *(LAS v2u*)aow = pk4(O);
    }
    bar();
    STAMPW(0, 8 + lay * 16);
#define XBPTR() lptr xb0 = L + L_XB + (col < 4 ? col : (col == 8 ? 4 : 5)) * XBS + hq * 16, xb1 = L + L_XB + ((col >= 4 && col < 8) ? col - 4 : (col == 12 ? 4 : 5)) * XBS + hq * 16; PINI(xb0); PINI(xb1)
#define STPTR() lptr sra = L + L_XB + (pp == 0 ? 4 : (pp == 1 ? q4 : 5)) * XBS + hq * 16, srb = L + L_XB + pp * XBS + hq * 16; int sso = col == hq ? col * 16 : 0x40000000; PINI(sra); PINI(srb); PINI(sso)
#define STATS(off) do { const unsigned tm_ = now(); const v4u a0 = lds16(sra, 0), a1 = lds16(sra, 64), b0 = lds16(srb, 0), b1 = lds16(srb, 64); SB(); \
      f4 d_ = mfma(a0, b0, Z4); d_ = mfma(a1, b1, d_); stx(rsW, v4u{cl(asu(d_.x)), cl(asu(d_.y)), tm_, 0u}, sso, off); } while (0)
#define STAT1 (pub1 + (((cls >> 16) & 15) < 8 ? 4 * BLK + 4096 - ((cls >> 16) & 15) * 896 : 4096 + 1024))
#define P1PREP() v4u A1[4]; int st1 = col < 8 ? pp * (((cls >> 16) & 15) < 8 ? BLK : 1024) + (8 * w + 4 * q4 + hq) * 16 : 0x40000000, sp1 = pub1; XBPTR(); \
      { const int base = L_WQ + (w >> 1) * 8192 + a128, t0 = 2 * (w & 1); \
        A1[0] = trfrag(L, base + (t0 ^ z128) * 32, 512); A1[1] = trfrag(L, base + (t0 ^ z128) * 32 + 4096, 512); \
        A1[2] = trfrag(L, base + ((t0 + 1) ^ z128) * 32, 512); A1[3] = trfrag(L, base + ((t0 + 1) ^ z128) * 32 + 4096, 512); } \
      PIN4(A1); PINI(st1); PINS(sp1)
#define P1RUN() do { if (hasP1) { const v4u b00 = lds16(xb0, 0), b01 = lds16(xb0, 64), b10 = lds16(xb1, 0), b11 = lds16(xb1, 64); SB(); \
        f4 d_ = mfma(A1[0], b00, Z4); d_ = mfma(A1[1], b01, d_); d_ = mfma(A1[2], b10, d_); d_ = mfma(A1[3], b11, d_); stx(rsW, split4(d_), st1, sp1); } } while (0)
#define CSQLOAD() const int ve = hq * X1_MSG + (16 * (w & 3) + col) * 16 + 4096;     \
      const v4u c0 = ldx(rsW, ve, gatc), c1 = ldx(rsW, ve + 4 * X1_MSG, gatc), c2 = ldx(rsW, ve + (8 - hq) * X1_MSG, gatc)
#define CSQSUM() do { const unsigned one_ = (lane & 1) ? 0x3F800000u : 0x00003F80u; \
      const v4u sel4 = {(col & 2) ? 0u : one_, (col & 2) ? one_ : 0u, (col & 2) ? 0u : one_, (col & 2) ? one_ : 0u}; \
      csq = mfma(sel4, c0, Z4); csq = mfma(sel4, c1, csq); csq = mfma(hq == 0 ? sel4 : v4u{0, 0, 0, 0}, c2, csq); } while (0)
    if (w < 6) {
      int ymsg;
      if (lay == 0) { FRESH(); b1v = *(const unsigned*)(p.b1 + b1ofs + HFO); PINI(b1v); }
      if (w < 4) {
        FRESH(); A128();
        int so = gat2 + xs, vm = hq * X2_MSG + 512 + (col >> 3) * 16;
        ymsg = col < 4 ? (4 * (2 * w + (hq >> 1)) + pp) * 16 + (hq & 1) * 8 : 0x40000000;
        PINS(so); PINI(vm); PINI(ymsg);
        int spin = 0;
        if (hasP3) {
          int vd = hq * X2_MSG + col * 16, sp = pub3 + xs;
          lptr p3a = L + L_WO + a128 + (w ^ z128) * 32, p3b = L + L_OB + pp * OBS + hq * 16;
          v4u pm0 = col < 8 ? pm : v4u{0, 0, 0, 0}, pm1 = col < 8 ? v4u{0, 0, 0, 0} : pm;
          lptr ow = L + L_OB + w * OBS + ((hq >> 1) * 128 + 8 * col + 4 * (hq & 1)) * 2;
          PINI(vd); PINS(sp); PINI(p3a); PINI(p3b); PINI(pm0); PINI(pm1); PINI(ow);
          v4u A[8];
#pragma unroll
          for (int k = 0; k < 8; ++k) A[k] = trfrag(p3a, k * 4096, 512);
          if (lay == 0 || !SCHED) __builtin_amdgcn_s_sleep(D_X2); else LOOKAT(T2B);
          f4 o;
          for (;; ++spin) {
            OPAQUE();
            const v2u ml = ldx2(rsW, vm, so);
            SB();
            const v4u d0 = ldx(rsW, vd, so), d1 = ldx(rsW, vd + 256, so);
            SB();
            const float m = asf(ml.x), M = max32(max16(m));
            const float f = __builtin_amdgcn_exp2f(m - M);
            const float Ls = add32(add16(f * asf(ml.y)));
            const float fs = f * __builtin_amdgcn_rcpf(Ls);
            const unsigned gg = pk(fs, fs);
            o = mfma(pm0 & gg, d0, Z4); o = mfma(pm1 & gg, d1, o);
            if (!any(o.x != o.x)) break;
            if (!any(max(max(mx4(d0), mx4(d1)), max(ml.x, ml.y)) == SENT) || spin > SPIN_CAP) break;
          }
          OPAQUE();
          *(LAS v2u*)ow = pk4(o);
          LATE(T2B);
          STAMPW(0, 10 + lay * 16);
          bar();
          STAMPW(0, 11 + lay * 16);
          v4u B[8];
#pragma unroll
          for (int k = 0; k < 8; ++k) B[k] = lds16(p3b, k * 64);
          if (lay == 0) {
#pragma unroll
            for (int k = 0; k < 8; ++k) A[k] = trfrag(p3a, k * 4096, 512);
          }
          SB();
          f4 d = Z4;
#pragma unroll
          for (int k = 0; k < 8; ++k) d = mfma(A[k], B[k], d);
          const v2u yv = pack4(d);
          stx2(rsW, yv, ymsg, sp);
          if (((cls >> 4) & 15) == 8) stx2(rsW, yv, ymsg, sp ^ HALF);
          STAMPW(0, 20 + lay * 16);
        } else bar();
        if (w == 0) COUNT(42 + lay * 8, spin);
      } else {
        ymsg = 0;
        if (lay == 0) {
          FRESH();
          unsigned sn = SENT; PINI(sn);
          const v4u SENT4 = {sn, sn, sn, sn};
          const int lo = lane * 16, r1 = (cls >> 16) & 15;
          __builtin_amdgcn_s_sleep(D_W);
          if (hasP3 && !hasP1) __builtin_amdgcn_s_sleep(D_X2 + 2 - D_W);
          if (hasP3 && hasP1) {
            const rsrc_t ro = mkrs(p.wo, QN * DM * 2);
#pragma unroll
            for (int i = 0; i < 16; ++i) dma128(lane, ro, DM * 2, wofs + 128 * (w - 4) * (DM * 2), ldsb + L_WO + (w - 4) * 16384, i);
          }
          if (w == 4) {
            if (hasF) {
              const int a0 = (pub4 + PG) ^ HALF;
              stx(rsW, SENT4, lo, a0); stx(rsW, SENT4, lo, a0 + 1024); stx(rsW, SENT4, lo, a0 + 2048);
              if (lane < 8) stx(rsW, SENT4, lo, a0 + 3840);
              if (lane < 32) { stx(rsW, SENT4, lo, pub5 + SM_STRIDE); if (((cls >> 12) & 15) == 8) stx(rsW, SENT4, lo, (pub5 + SM_STRIDE) ^ HALF); }
            }
            if (lane < 40) stx(rsW, SENT4, lo, pub2 + SM_STRIDE);
          } else {
            if (hasP1) {
              const int rs = r1 < 8 ? BLK : 1024;
              stx(rsW, SENT4, lo, pub1); stx(rsW, SENT4, lo, pub1 + rs); stx(rsW, SENT4, lo, pub1 + 2 * rs); stx(rsW, SENT4, lo, pub1 + 3 * rs);
              if (lane < 8) stx(rsW, SENT4, lo, STAT1);
            }
            if (hasP3 && lane < 32) { stx(rsW, SENT4, lo, pub3 + SM_STRIDE); if (((cls >> 4) & 15) == 8) stx(rsW, SENT4, lo, (pub3 + SM_STRIDE) ^ HALF); }
          }
          if (hasF) {
            const rsrc_t r = mkrs(p.w1, DM * FF * 2), r2 = mkrs(p.w2, FF * DM * 2);
            const int vo = (lane >> 2) * (FF * 2) + ((lane & 3) ^ (2 * (lane >> 5))) * 16;
#pragma unroll
            for (int i = 0; i < 12; ++i) { const int t = 3 * (w - 4) + (i >> 2); dma(r, vo, w1ofs + 16 * (i & 3) * (FF * 2) + t * 64, ldsb + L_W1 + t * 4096 + (i & 3) * 1024); }
            asm volatile("s_waitcnt vmcnt(12)" ::: "memory");
            bar();
            for (int sp2 = 0; *(volatile LAS unsigned*)(L + L_TM + 4) == 0u && sp2 < (SPIN_CAP << 4); ++sp2) __builtin_amdgcn_s_sleep(1);
#pragma unroll
            for (int i = 0; i < 12; ++i) dma128(lane, r2, DM * 2, w2ofs + 96 * (w - 4) * (DM * 2), ldsb + L_W2 + (w - 4) * 12288, i);
            asm volatile("s_waitcnt vmcnt(12)" ::: "memory");
          } else { drain(); bar(); }
        } else bar();
      }
      int spin = 0;
      if (hasF) {
        v4u A[6];
        lptr p5a, p5b;
        {
          FRESH();
          XBPTR();
          const int s0 = (hq & 1) * 32;
          lptr p4a0 = L + L_W1 + w * 4096 + hq * 512 + q4 * 64 + pp * 8 + s0, p4a1 = p4a0 + 32 - 2 * s0;
          int st4 = col < 8 ? w * 512 + (16 * q4 + 4 * hq + pp) * 16 : ((lay == 0 && pp == 0) ? 3072 + w * 128 + (col - 8 + hq) * 16 : 0x40000000), sp = (pub4 + (x4 & ~HALF)) ^ (x4 & HALF);
          v4u A4[4] = {trfrag(p4a0, 0, 256), trfrag(p4a0, 2048, 256), trfrag(p4a1, 0, 256), trfrag(p4a1, 2048, 256)};
          PINI(st4); PINS(sp); PINI(p4a0); PINI(p4a1); PIN4(A4);
          bar();
          STAMPW(0, 13 + lay * 16);
          const v4u b00 = lds16(xb0, 0), b01 = lds16(xb0, 64), b10 = lds16(xb1, 0), b11 = lds16(xb1, 64);
          if (lay == 0) { A4[0] = trfrag(p4a0, 0, 256); A4[1] = trfrag(p4a0, 2048, 256); A4[2] = trfrag(p4a1, 0, 256); A4[3] = trfrag(p4a1, 2048, 256); }
          SB();
          f4 d = mfma(A4[0], b00, Z4); d = mfma(A4[1], b01, d); d = mfma(A4[2], b10, d); d = mfma(A4[3], b11, d);
          stx(rsW, split4(d), st4, sp);
          STAMPW(0, 14 + lay * 16);
        }
        FRESH(); A128();
        const int pn = lane >> 2;
        const int vv = 2 * (q4 & 1) + (col & 1);
        const unsigned on = ((col >> 3) == (hq >> 1) && (col & 2) == 0) ? ((vv & 1) ? 0x3F800000u : 0x3F80u) : 0u;
        v4u selh = {(vv & 2) ? 0u : on, (vv & 2) ? on : 0u, (vv & 2) ? 0u : on, (vv & 2) ? on : 0u};
        v4u selh0 = (hq & 1) ? v4u{0, 0, 0, 0} : selh;
        const int pc = pn < 9 ? pn : 8;
        int so = (gat4 + (x4 & ~HALF)) ^ (x4 & HALF), vs = (pc >> 1) * BLK + (pc & 1) * 4096 + 3840 + pp * 16, sp5 = pub5 + xs;
        float sm = pn < 9 ? (1.f / DM) : 0.f;
        lptr hw = L + L_HB + pp * HBS + HFO * 2;
        int vo4 = 4 * BLK + w * 512 + (16 * (hq >> 1) + col) * 16, vo = vo4 - 4 * BLK + (hq & 1) * 4096;
        int sa = so + BLK, sb = so + 2 * BLK, sc = so + 3 * BLK;
        int ve1 = 4 * BLK + 3072 + w * 128 + (4 * (hq >> 1) + q4) * 16, ve0 = ve1 + (2 * (hq & 1) + (pp >> 1) - 4) * BLK + (pp & 1) * 4096;
        p5a = L + L_W2 + a128 + ((w & 3) ^ z128) * 32; p5b = L + L_HB + pp * HBS + hq * 16;
        f2 bb = {blo(b1v), bhi(b1v)};
        PINS(so); PINS(sp5); PINI(selh); PINI(selh0); PINI(vs); PINI(sm); PINI(hw); PINI(vo); PINI(vo4); PINS(sa); PINS(sb); PINS(sc); PINI(ve0); PINI(ve1); PINI(p5a); PINI(p5b); PINI(bb);
#pragma unroll
        for (int k = 0; k < 6; ++k) A[k] = trfrag(p5a, k * 4096, 512);
        if (lay == 0) __builtin_amdgcn_s_sleep(D_X4A); else if (SCHED) LOOKAT(T4B); else __builtin_amdgcn_s_sleep(D_X4);
        f4 a; float rstd; f2 t;
        for (;; ++spin) {
          OPAQUE();
          const v2u st = ldx2(rsW, vs, so);
          v4u e0 = {0, 0, 0, 0}, e1 = e0;
          if (lay == 0) { e0 = ldx(rsW, ve0, so); e1 = ldx(rsW, ve1, so); }
          const v4u d0 = ldx(rsW, vo, so), d1 = ldx(rsW, vo, sa), d2 = ldx(rsW, vo, sb), d3 = ldx(rsW, vo, sc), d4 = ldx(rsW, vo4, so);
          SB();
          float s1 = asf(st.x) * sm, s2 = asf(st.y) * sm;
          s1 += dppf<ROR4>(s1); s2 += dppf<ROR4>(s2); s1 += dppf<ROR8>(s1); s2 += dppf<ROR8>(s2);
          s1 = add32(add16(s1)); s2 = add32(add16(s2));
          rstd = __builtin_amdgcn_rsqf(fmaxf(s2 - s1 * s1, 0.f) + 1e-5f);
          if (lay == 0) {
            f4 e = mfma(selh, e0, Z4); const f4 e8 = mfma(selh0, e1, Z4);
            e.x += dppf<XOR1>(e.x); e.y += dppf<XOR1>(e.y); e.x += dppf<XOR2>(e.x); e.y += dppf<XOR2>(e.y);
            cs1 = f2{e.x + e8.x, e.y + e8.y};
          }
          t = bb - (s1 * rstd) * cs1;
          SB();
          a = mfma(selh, d0, Z4); a = mfma(selh, d1, a); a = mfma(selh, d2, a); a = mfma(selh, d3, a); a = mfma(selh0, d4, a);
          if (!any(a.x != a.x || t.x != t.x)) break;
          if (!any(max(max(max(max(mx4(d0), mx4(d1)), max(mx4(d2), mx4(d3))), max(mx4(d4), max(st.x, st.y))), max(mx4(e0), mx4(e1))) == SENT) || spin > SPIN_CAP) break;
        }
        OPAQUE();
        STAMPW(0, 15 + lay * 16);
        const f2 v = f2{a.x, a.y} * rstd + t;
        const f2 e = v * (v * v * (-2.f * 0.7978845608028654f * 1.4426950408889634f * 0.044715f) + (-2.f * 0.7978845608028654f * 1.4426950408889634f));
        const float h0 = v.x * __builtin_amdgcn_rcpf(1.f + __builtin_amdgcn_exp2f(e.x)), h1 = v.y * __builtin_amdgcn_rcpf(1.f + __builtin_amdgcn_exp2f(e.y));
        *(LAS unsigned*)hw = pk(h0, h1);
        LATE(T4B);
        bar();
        STAMPW(0, 16 + lay * 16);
        if (w < 4) {
          v4u B[6];
#pragma unroll
          for (int k = 0; k < 6; ++k) B[k] = lds16(p5b, k * 64);
          if (lay == 0) {
#pragma unroll
            for (int k = 0; k < 6; ++k) A[k] = trfrag(p5a, k * 4096, 512);
          }
          SB();
          f4 d = Z4;
#pragma unroll
          for (int k = 0; k < 6; ++k) d = mfma(A[k], B[k], d);
          const v2u zv = pack4(d);
          stx2(rsW, zv, ymsg, sp5);
          if (((cls >> 12) & 15) == 8) stx2(rsW, zv, ymsg, sp5 ^ HALF);
          STAMPW(0, 21 + lay * 16);
        }
      } else {
        bar();
        bar();
      }
      if (w == 0 && lay == 1) EVA(22, spin * 100);
      if (lay == 1) return;
      FRESH(); A128();
      P1PREP(); STPTR();
      if (w >= 4) {
        const unsigned tn = now();
        CSQLOAD(); CSQSUM(); PINI(csq);
        if (w == 5) {
          __builtin_amdgcn_s_sleep(D_M);
          int dm;
          for (int sp2 = 0;; ++sp2) {
            OPAQUE();
            const unsigned m = __builtin_amdgcn_raw_buffer_load_b32(rsW, ((slo >> 6) * 24 + (lane < 24 ? lane : 0)) * 128, WS_MARK, AUX_SC1);
            dm = (int)((m - tn) << 1);
            const bool bad = dm > 1600 || dm < -1600;
            if (bad) dm = -1600;
            if (!any(bad) || sp2 > 8) break;
          }
          dm = max(dm, (int)dppu<XOR1>(dm)); dm = max(dm, (int)dppu<XOR2>(dm)); dm = max(dm, (int)dppu<ROR4>(dm)); dm = max(dm, (int)dppu<ROR8>(dm));
          dm = max(__builtin_amdgcn_readlane(dm, 0), __builtin_amdgcn_readlane(dm, 16));
          if (lane == 0) *(LAS unsigned*)(L + L_TM) = (tn + (unsigned)(dm >> 1)) & 0x7FFFFFFFu;
        }
      }
      bar();
      if (w == 0) EV(6);
      P1RUN();
      t0 = __builtin_amdgcn_readfirstlane(*(LAS unsigned*)(L + L_TM));
      if (w == 3 && hasP1) STATS(STAT1);
      STAMPW(0, 19);
    } else {
      {
        const unsigned tm = now();
        const f2 ml0 = *(LAS f2*)mmr, ml1 = *(LAS f2*)(mmr + 64);
        const v4u B0 = lds16(mbr, 0), B1 = lds16(mbr, 2048);
        const int so = pub2 + xs;
        SB();
        const float M = max32(max16(vmax(ml0.x, ml1.x)));
        const float f0 = __builtin_amdgcn_exp2f(ml0.x - M), f1 = __builtin_amdgcn_exp2f(ml1.x - M);
        const unsigned h0 = pk(f0, f0), h1 = pk(f1, f1);
        f4 o = mfma(pm & h0, B0, Z4); o = mfma(pm & h1, B1, o);
        const float Ls = add32(add16(blo(h0) * ml0.y + blo(h1) * ml1.y));
        if (lane0 == 0) stx(rsW, v4u{cl(asu(M)), cl(asu(Ls)), tm, 0u}, 512 + (w & 1) * 16, so);
        stx2(rsW, pack4(o), mst, so);
        STAMPW(6, 9 + lay * 16);
      }
      FRESH(); A128(); SEL8(); STPTR();
      if (lay == 0) { xp = *(const v2u*)(p.x + pp * DM + slo + TKO); b2v = *(const v2u*)(p.b2 + slo + TKO); PINI(xp); PINI(b2v); }
      v4u s8 = sel8;
      int v0 = hq * 512 + (w - 6) * 256 + col * 16;
      lptr xw = L + L_XB + pp * XBS + TKO * 2;
      {
        int so = gat3 + xs, so1 = so + 4096, sp = ((pub4 + (x4 & ~HALF)) ^ (x4 & HALF)) + 3840;
        f4 xr = unp(xp);
        PINS(so); PINS(so1); PINS(sp); PINI(s8); PINI(v0); PINI(xw); PINI(xr);
        if (lay == 0) {
          const unsigned tb = now();
          if (w == 7 && ((cls >> 20) & 15) == 15) {
            __builtin_amdgcn_s_sleep(D_X2 - 10);
            for (int sp2 = 0;; ++sp2) {
              OPAQUE();
              const unsigned t = __builtin_amdgcn_raw_buffer_load_b32(rsW, (col >> 3) * BLK + (col & 7) * X2_MSG + 512 + 16 + 8, gat2 - BLK - 4 * X2_MSG, AUX_SC1);
              if (!any(t == SENT) || sp2 > SPIN_CAP) break;
            }
            if (lane0 == 0) __builtin_amdgcn_raw_buffer_store_b32(now(), rsW, 0, WS_HER + (cls >> 24) * 128, AUX_SC1);
          }
          bar();
          for (int sp2 = 0; sp2 < 64; ++sp2) {
            OPAQUE();
            const unsigned t = __builtin_amdgcn_raw_buffer_load_b32(rsW, col * 128, WS_HER, AUX_SC1);
            if (!any(((t - tb + 800u) & 0x7FFFFFFFu) > 4000u || t > 0x7FFFFFFFu)) break;
          }
          STAMPW(6, 38);
        } else bar();
        if (lay == 0) {} else if (SCHED) LOOKAT(T3B); else { __builtin_amdgcn_s_sleep(D_X3); if (!hasP3) __builtin_amdgcn_s_sleep(36); }
        f4 y;
        int spin = 0;
        for (;; ++spin) {
          OPAQUE();
          const v4u d0 = ldx(rsW, v0, so), d1 = ldx(rsW, v0 + 2048, so), d2 = ldx(rsW, v0, so1), d3 = ldx(rsW, v0 + 2048, so1);
          if (lane0 == 0) *(volatile LAS unsigned*)(L + L_TM + 4) = 1u;
          SB();
          y = mfma(s8, d0, Z4); y = mfma(s8, d1, y); y = mfma(s8, d2, y); y = mfma(s8, d3, y);
          if (!any(y.x != y.x)) break;
          if (!any(max(max(mx4(d0), mx4(d1)), max(mx4(d2), mx4(d3))) == SENT) || spin > SPIN_CAP) break;
        }
        OPAQUE();
        if (w == 6 && lay == 1) EVA(22, spin * 100);
        if (w == 6 && lay == 1) EV(24);
        xp = pk4(xr + unp(pk4(y)));
        if (lane0 < 32) *(LAS v2u*)xw = xp;
        LATE(T3B);
        bar();
        if (w == 6) { if (hasF) STATS(sp); }
        else if (lay == 0 && c == 0 && lane0 == 0) stx2(rsW, *(LAS v2u*)(L + L_TAG + 8), 0, WS_EPOCH);
      }
      if (lay == 1 && !isOut) { bar(); return; }
      int so = gat5 + xs, so1 = so + 4096, so2 = so + BLK;
      int oo = hq < 2 ? (pp * DM + slo + TKO) * 2 : 0x40000000;
      f4 xr = unp(xp), bb = unp(b2v);
      P1PREP();
      PINS(so); PINS(so1); PINS(so2); PINI(xr); PINI(bb); PINI(oo);
      unsigned a4 = 0;
      if (lay == 0) {
        CSQLOAD(); CSQSUM(); PINI(csq);
        if (w == 7) {
          unsigned t;
          for (int sp2 = 0;; ++sp2) { OPAQUE(); t = __builtin_amdgcn_raw_buffer_load_b32(rsW, ((col < 9 ? col : 8) >> 1) * BLK + (col < 9 ? col & 1 : 0) * 4096 + 3840 + 8, gat4, AUX_SC1); if (!any(t == SENT) || sp2 > SPIN_CAP) break; }
          a4 = tmax16(t) + N45;
          if (!hasF) until(a4);
        }
      }
      bar();
      if (lay == 0 && w == 7 && hasF) {
        const unsigned tn = now();
        if (lane0 == 0) __builtin_amdgcn_raw_buffer_store_b32((int)((tn - a4) << 1) > 0 ? tn : a4 & 0x7FFFFFFFu, rsW, 0, mko, AUX_SC1);
      }
      if (lay == 0) __builtin_amdgcn_s_sleep(D_X5A); else if (SCHED) LOOKAT(T5B); else { __builtin_amdgcn_s_sleep(D_X5); if (!hasF) __builtin_amdgcn_s_sleep(44); }
      f4 y;
      int spin = 0;
      for (;; ++spin) {
        OPAQUE();
        const v4u d0 = ldx(rsW, v0, so), d1 = ldx(rsW, v0 + 2048, so), d2 = ldx(rsW, v0, so1), d3 = ldx(rsW, v0 + 2048, so1), d4 = ldx(rsW, v0, so2), d5 = ldx(rsW, v0 + 2048, so2);
        SB();
        y = mfma(s8, d0, Z4); y = mfma(s8, d1, y); y = mfma(s8, d2, y); y = mfma(s8, d3, y); y = mfma(s8, d4, y); y = mfma(s8, d5, y);
        if (!any(y.x != y.x)) break;
        if (!any(max(max(max(mx4(d0), mx4(d1)), max(mx4(d2), mx4(d3))), max(mx4(d4), mx4(d5))) == SENT) || spin > SPIN_CAP) break;
      }
      OPAQUE();
      if (w == 6) COUNT(45 + lay * 8, spin);
      STAMPW(6, 17 + lay * 16);
      xp = pk4(xr + unp(pk4(unp(pk4(y)) + bb)));
      if (lay == 1) {
        if (w == 6) EV(9);
        if (blockIdx.x == 0 && w == 6) { const unsigned e_ = __builtin_amdgcn_readfirstlane(*(volatile LAS unsigned*)(L + L_BYTES)); while (now() - tst_ < 4000 + e_) {} }
        __builtin_amdgcn_raw_buffer_store_b64(xp, mkrs(p.out, 4 * DM * 2), oo, 0, OUT_AUX);
        STAMPW(6, 34);
        if (w == 6) { COUNT(61, __builtin_amdgcn_s_memtime()); }
        return;
      }
      if (lane0 < 32) *(LAS v2u*)xw = xp;
      bar();
      P1RUN();
      t0 = __builtin_amdgcn_readfirstlane(*(LAS unsigned*)(L + L_TM));
    }
#pragma unroll
    for (int T = 0; T < 4; ++T) S[T] = Z4;
    O = Z4;
    PIN4(S); PINI(O);
    if (w >= 4) {
      FRESH();
      const unsigned one = (lane & 1) ? 0x3F800000u : 0x00003F80u;
      v4u sel4 = {(col & 2) ? 0u : one, (col & 2) ? one : 0u, (col & 2) ? 0u : one, (col & 2) ? one : 0u};
      v4u sel40 = hq == 0 ? sel4 : v4u{0, 0, 0, 0};
      int so = gat1 + ab * BLK, so1 = so + 4096, so2 = gat1 + 4 * BLK + ab * 1024, so3 = gat1 + 4 * BLK + 4096 + ab * 16;
      int v2 = (16 * (w - 4) + col) * 16, v0 = v2 + hq * 1024, vs = (col < 9 ? col : 8) * 128;
      float sm = col < 9 ? (1.f / DM) : 0.f;
      lptr qw = L + QPW(w - 4);
      PINS(so); PINS(so1); PINS(so2); PINS(so3); PINI(sel4); PINI(sel40); PINI(v0); PINI(v2); PINI(vs); PINI(qw); PINI(sm);
      const int lay_ = 1;
      if (SCHED) until(t0 + T1B); else __builtin_amdgcn_s_sleep(D_X1);
      f4 d, cc; float rs;
      int spin = 0;
      for (;; ++spin) {
        OPAQUE();
        const v2u st = ldx2(rsW, vs, so3);
        SB();
        const v4u d0 = ldx(rsW, v0, so), d1 = ldx(rsW, v0, so1), d2 = ldx(rsW, v2, so2);
        SB();
        float s1 = asf(st.x) * sm, s2 = asf(st.y) * sm;
        s1 += dppf<XOR1>(s1); s2 += dppf<XOR1>(s2); s1 += dppf<XOR2>(s1); s2 += dppf<XOR2>(s2);
        SB();
        d = mfma(sel4, d0, Z4); d = mfma(sel4, d1, d); d = mfma(sel40, d2, d);
        SB();
        s1 += dppf<ROR4>(s1); s2 += dppf<ROR4>(s2); s1 += dppf<ROR8>(s1); s2 += dppf<ROR8>(s2);
        rs = __builtin_amdgcn_rsqf(fmaxf(s2 - s1 * s1, 0.f) + 1e-5f) * CS;
        cc = (-s1 * rs) * csq;
        d = d * rs + cc;
        if (!any(d.x != d.x)) break;
        if (!any(max(max(max(mx4(d0), mx4(d1)), mx4(d2)), max(st.x, st.y)) == SENT) || spin > SPIN_CAP) break;
      }
      OPAQUE();
      LATE(T1B);
      asm volatile(".Lfwd_end:");
      if (w == 4) EVA(22, spin * 100);
      if (w == 4) EV(23);
      STAMPW(4, 22);
      if (lane0 < 16) { LAS unsigned* qp = (LAS unsigned*)qw; qp[0] = pk(d.x, d.y); qp[16] = pk(d.z, d.w); }
    }
    bar();
    STAMPW(0, 23);
  }
}

extern "C" void kernel_launch(void* const* d_in, const int* in_sizes, int n_in, void* d_out, int out_size, void* d_ws, size_t ws_size, hipStream_t stream) {
  Params p;
  p.x = (const u16*)d_in[0]; p.kc = (const u16*)d_in[1]; p.vc = (const u16*)d_in[2]; p.wq = (const u16*)d_in[3]; p.wo = (const u16*)d_in[4];
  p.w1 = (const u16*)d_in[6]; p.b1 = (const u16*)d_in[7]; p.w2 = (const u16*)d_in[8]; p.b2 = (const u16*)d_in[9];
  p.out = (u16*)d_out; p.ws = (unsigned char*)d_ws;
  (void)hipFuncSetAttribute((const void*)fused_fwd, hipFuncAttributeMaxDynamicSharedMemorySize, L_BYTES + 16);
  hipLaunchKernelGGL(fused_fwd, dim3(256), dim3(512), L_BYTES + 16, stream, p);
}
```

```cpp
#include <hip/hip_runtime.h>
#include <stdint.h>

#define STAMPS 0

typedef unsigned short u16;
typedef unsigned long long u64;
typedef unsigned v4u __attribute__((ext_vector_type(4)));
typedef unsigned v8u __attribute__((ext_vector_type(8)));
typedef unsigned v2u __attribute__((ext_vector_type(2)));
typedef float f4 __attribute__((ext_vector_type(4)));
typedef float f2 __attribute__((ext_vector_type(2)));
typedef __bf16 bf2 __attribute__((ext_vector_type(2)));
typedef __bf16 bf8 __attribute__((ext_vector_type(8)));
typedef __bf16 bf16v __attribute__((ext_vector_type(16)));
typedef short s4v __attribute__((ext_vector_type(4)));
#define LAS __attribute__((address_space(3)))
#define DI __device__ __forceinline__
typedef __amdgpu_buffer_rsrc_t rsrc_t;

constexpr int DM = 576, SK = 8192, WIN = 2048, DH = 128, FF = 4608, QN = 4096;
constexpr unsigned SENT = 0xFFFFFFFFu;
constexpr int AUX_NT = 2, AUX_SC1 = 16;

constexpr int PG = 2 << 20, BLK = 16384, HALF = 8192;
constexpr int WS_EPOCH = 0, WS_FLAGS = 4096, WS_HER = 8192, WS_PROBE = 49152, X1A_OFF = 65536, X1_MSG = 5376;
constexpr int X2_MSG = 640, X2_OFF = PG, X3_OFF = PG + 32 * BLK, X5_OFF = PG + 41 * BLK, SM_STRIDE = 60 * BLK;
constexpr int X4_OFF = 2 * PG;
constexpr int X1_OFF = 4 * PG;
constexpr int WS_USED = 5 * PG;
static_assert(X1A_OFF + 144 * X1_MSG <= PG && 2 * SM_STRIDE <= PG && 120 * BLK <= PG, "pages");
constexpr int WS_STAMP = 16 << 20;

constexpr int L_BASE = 64;
constexpr int L_WQ = 0;
constexpr int L_WO = 32768;
constexpr int L_W2 = 65536;
constexpr int L_W1 = 90112;
constexpr int L_PA = 114688;
constexpr int L_AO = L_PA + 8 * 2176;
constexpr int L_OB = L_AO + 4096;
constexpr int L_HB = L_OB + 4 * 528;
constexpr int L_XB = L_HB + 4 * 400;
constexpr int L_QP = L_XB + 6 * 144;
constexpr int L_ML = L_QP + 512;
constexpr int L_LN0 = L_ML + 128;
constexpr int L_RDY = L_LN0 + 32;
constexpr int L_TAG = L_LN0 + 32;
constexpr int L_TM = L_TAG + 16;
constexpr int L_TB = L_TM + 32;
constexpr int L_BYTES = L_TB + 96;
constexpr int XBS = 144, OBS = 528, HBS = 400;
static_assert(L_BYTES <= 160 * 1024 && L_OB % 16 == 0 && L_HB % 16 == 0 && L_XB % 16 == 0 && L_QP % 16 == 0 && L_ML % 16 == 0, "lds");

#define D_X1 12
#define D_X2 22
#define D_X3 20
#define D_X4 10
#define D_X5 18
#define I3A 90
#define I4A 115
#define I5A 155
#define I1 102
#define I2 160
#define I3 98
#define I4 90
#define I5 100
constexpr unsigned T3A = I3A, T4A = T3A + I4A, T5A = T4A + I5A, T1B = T5A + I1, T2B = T1B + I2, T3B = T2B + I3, T4B = T3B + I4, T5B = T4B + I5;
#define SCHED 1
#define NUMA 1
#define FPOS 19
#define XPOS 3
#define CPOS 8
#define SPOS 14
#define WND 10
#define TP0 2
#define TP1 5
#define TP2 8
#define TP3 11
#define D_W 11
#define PREFI 1
#define OUT_AUX 0
#define SNT 2
#define REWARM 1
#define HSLP 0
constexpr int SPIN_CAP = 1 << 18;

struct Params {
  const u16 *x, *kc, *vc, *wq, *wo, *w1, *b1, *w2, *b2;
  u16* out;
  unsigned char* ws;
};

DI f4 mfma(v4u a, v4u b, f4 c) { return __builtin_amdgcn_mfma_f32_16x16x32_bf16(__builtin_bit_cast(bf8, a), __builtin_bit_cast(bf8, b), c, 0, 0, 0); }
DI f4 smf(v4u a, v8u b, f4 c, int idx) { return __builtin_amdgcn_smfmac_f32_16x16x64_bf16(__builtin_bit_cast(bf8, a), __builtin_bit_cast(bf16v, b), c, idx, 0, 0); }
DI v8u cat(v4u a, v4u b) { return __builtin_shufflevector(a, b, 0, 1, 2, 3, 4, 5, 6, 7); }
DI float asf(unsigned u) { return __builtin_bit_cast(float, u); }
DI unsigned asu(float f) { return __builtin_bit_cast(unsigned, f); }
template <int C> DI unsigned dppu(unsigned x) { return (unsigned)__builtin_amdgcn_mov_dpp((int)x, C, 0xf, 0xf, true); }
template <int C> DI float dppf(float x) { return asf(dppu<C>(asu(x))); }
constexpr int XOR1 = 0xB1, XOR2 = 0x4E, ROR4 = 0x124, ROR8 = 0x128;
DI float add16(float x) { auto s = __builtin_amdgcn_permlane16_swap(asu(x), asu(x), false, false); return asf(s[0]) + asf(s[1]); }
DI float add32(float x) { auto s = __builtin_amdgcn_permlane32_swap(asu(x), asu(x), false, false); return asf(s[0]) + asf(s[1]); }
DI float vmax(float a, float b) { float r; asm("v_max_f32 %0, %1, %2" : "=v"(r) : "v"(a), "v"(b)); return r; }
DI float max16(float x) { auto s = __builtin_amdgcn_permlane16_swap(asu(x), asu(x), false, false); return vmax(asf(s[0]), asf(s[1])); }
DI float max32(float x) { auto s = __builtin_amdgcn_permlane32_swap(asu(x), asu(x), false, false); return vmax(asf(s[0]), asf(s[1])); }
DI unsigned pk(float lo, float hi) { bf2 v = {(__bf16)lo, (__bf16)hi}; return __builtin_bit_cast(unsigned, v); }
DI float rbf(float x) { return (float)(__bf16)x; }
DI float blo(unsigned u) { return asf(u << 16); }
DI float bhi(unsigned u) { return asf(u & 0xffff0000u); }
DI unsigned cl(unsigned u) { return min(u, SENT - 1); }
DI v4u split4(f4 d) {
  const unsigned h0 = pk(d.x, d.y), h1 = pk(d.z, d.w);
  return v4u{cl(h0), cl(h1), cl(pk(d.x - blo(h0), d.y - bhi(h0))), cl(pk(d.z - blo(h1), d.w - bhi(h1)))};
}
DI v2u pack4(f4 d) { return v2u{cl(pk(d.x, d.y)), cl(pk(d.z, d.w))}; }
DI unsigned mx4(v4u v) { return max(max(v.x, v.y), max(v.z, v.w)); }
DI v4u ldx(rsrc_t r, int voff, int soff) { return __builtin_amdgcn_raw_buffer_load_b128(r, voff, soff, AUX_SC1); }
DI v2u ldx2(rsrc_t r, int voff, int soff) { return __builtin_amdgcn_raw_buffer_load_b64(r, voff, soff, AUX_SC1); }
DI void stx(rsrc_t r, v4u v, int voff, int soff) { __builtin_amdgcn_raw_buffer_store_b128(v, r, voff, soff, AUX_SC1); }
DI void stx2(rsrc_t r, v2u v, int voff, int soff) { __builtin_amdgcn_raw_buffer_store_b64(v, r, voff, soff, AUX_SC1); }
DI v4u ldn(rsrc_t r, int voff, int soff) { return __builtin_amdgcn_raw_buffer_load_b128(r, voff, soff, AUX_SC1 | SNT); }
DI void stn(rsrc_t r, v4u v, int voff, int soff) { __builtin_amdgcn_raw_buffer_store_b128(v, r, voff, soff, AUX_SC1 | SNT); }
DI void bar() { asm volatile("s_waitcnt lgkmcnt(0)\n\ts_barrier" ::: "memory"); }
DI void drain() { asm volatile("s_waitcnt vmcnt(0)" ::: "memory"); }
DI rsrc_t mkrs(const void* p, unsigned bytes) { return __builtin_amdgcn_make_buffer_rsrc((void*)p, (short)0, (int)bytes, 0x00020000); }
DI void dma(rsrc_t r, int voff, int soff, unsigned ldsaddr) {
  unsigned keep;
  asm volatile("s_nop 4\n\ts_mov_b32 %0, m0\n\ts_mov_b32 m0, %1\n\ts_nop 0\n\tbuffer_load_dwordx4 %2, %3, %4 offen nt lds\n\ts_mov_b32 m0, %0"
               : "=&s"(keep) : "s"(ldsaddr), "v"(voff), "s"(r), "s"(soff) : "memory");
}
typedef LAS unsigned char* lptr;
DI v4u lds16(lptr L, int off) { return *(LAS v4u*)(L + off); }
#define SB() __builtin_amdgcn_sched_barrier(0)
DI float dot2(unsigned a, unsigned b, float c) { return __builtin_amdgcn_fdot2_f32_bf16(__builtin_bit_cast(bf2, a), __builtin_bit_cast(bf2, b), c, false); }
DI v4u trfrag(LAS unsigned char* L, int off, int step) {
  s4v a = __builtin_amdgcn_ds_read_tr16_b64_v4i16((LAS s4v*)(L + off));
  s4v b = __builtin_amdgcn_ds_read_tr16_b64_v4i16((LAS s4v*)(L + off + step));
  v2u x = __builtin_bit_cast(v2u, a), y = __builtin_bit_cast(v2u, b);
  return v4u{x.x, x.y, y.x, y.y};
}
DI bool any(bool b) { return __builtin_amdgcn_ballot_w64(b) != 0; }
#define OPAQUE() asm volatile("" ::: "memory")
#define Z4 f4{0.f, 0.f, 0.f, 0.f}
#define FRESH() int lane = lane0; asm volatile("" : "+v"(lane)); const int col = lane & 15, hq = lane >> 4, q4 = col >> 2, pp = lane & 3; (void)col; (void)hq; (void)q4; (void)pp
#define SEL8() const unsigned one = (lane & 1) ? 0x3F800000u : 0x00003F80u; \
  const v4u sel8 = {((col & 7) >> 1) == 0 ? one : 0u, ((col & 7) >> 1) == 1 ? one : 0u, ((col & 7) >> 1) == 2 ? one : 0u, ((col & 7) >> 1) == 3 ? one : 0u}
#define A128() const int z128 = ((q4 >> 1) & 1) | ((hq & 1) << 1), a128 = hq * 1024 + q4 * 128 + pp * 8
#define STAMP(i) do {} while (0)
#define STAMPW(ww, i) do {} while (0)
#define COUNT(i, v) do {} while (0)

#define PIN4(a) asm volatile("" : "+v"(a[0]), "+v"(a[1]), "+v"(a[2]), "+v"(a[3]))
#define PINI(x) asm volatile("" : "+v"(x))
#define PINS(x) asm volatile("" : "+s"(x))
#define TKO (32 * (w & 1) + 8 * q4 + 4 * (hq & 1))
#define HFO (32 * w + 16 * (hq >> 1) + 4 * q4 + 2 * (hq & 1))
#define KLOAD(i) K[i] = __builtin_amdgcn_raw_buffer_load_b128(rsK, kvo, __builtin_amdgcn_readfirstlane((((i) + rot) & 15) * 1024), AUX_NT)
#define VLOAD(i) V[i] = __builtin_amdgcn_raw_buffer_load_b128(rsV, vvo, __builtin_amdgcn_readfirstlane((((i) + rot) & 15) * 1024), AUX_NT)
#define QPW(ww) (L_QP + (q4 & 1) * 256 + (((ww) >> 1) * 8 + 2 * ((col >> 1) & 1) + ((ww) & 1)) * 16 + (2 * (col >> 3) + (col & 1)) * 4)
#define TT(i) do { if (STAMPS == 1 || (STAMPS && (i) == 0)) { SB(); tt[i] = __builtin_amdgcn_s_memrealtime(); asm volatile("s_waitcnt lgkmcnt(0)" : "+s"(tt[i])); SB(); } } while (0)
DI unsigned now() { return (unsigned)__builtin_amdgcn_s_memrealtime() & 0x7FFFFFFFu; }
DI void until(unsigned t) { for (int i = 0; i < 4096 && ((t - now()) & 0x7FFFFFFFu) - 1u < 0x3FFFFFFFu; ++i) {} }
DI unsigned tmax16(unsigned t) {
  const unsigned ref = __builtin_amdgcn_readfirstlane(t);
  int d = (int)((t - ref) << 1);
  d = max(d, (int)dppu<XOR1>(d)); d = max(d, (int)dppu<XOR2>(d)); d = max(d, (int)dppu<ROR4>(d)); d = max(d, (int)dppu<ROR8>(d));
  return (ref + (unsigned)(__builtin_amdgcn_readfirstlane(d) >> 1)) & 0x7FFFFFFFu;
}
#define RTTN 50
#define LATE(T) do { if (spin > 0 && lane0 == 0) __hip_atomic_fetch_max((LAS int*)(CT + (L_TM - L_TAG) + 12), (int)(((now() - ((T) + RTTN)) << 1)) >> 1, __ATOMIC_RELAXED, __HIP_MEMORY_SCOPE_WORKGROUP); } while (0)
#define DLAT() (*(LAS int*)(CT + (L_TM - L_TAG) + 12))
#define LOOKAT(T) until((T) + (unsigned)__builtin_amdgcn_readfirstlane(DLAT()))
#define RS(k) __builtin_amdgcn_readfirstlane(*(LAS int*)(CT + (L_TB - L_TAG) + 4 * (k)))
DI u64 next_tag(v2u e) { return (((u64)e.y << 32) | e.x) * 6364136223846793005ull + 1442695040888963407ull; }
DI void dma128(int ln, rsrc_t r, int rowbytes, int soff, unsigned lds, int i) {
  dma(r, (ln >> 3) * rowbytes + ((((ln & 7) >> 1) ^ (((ln >> 4) & 1) | ((i & 1) << 1))) * 32) + (ln & 1) * 16, soff + 8 * i * rowbytes, lds + i * 1024);
}
DI f4 unp(v2u v) { return f4{blo(v.x), bhi(v.x), blo(v.y), bhi(v.y)}; }
DI v2u pk4(f4 v) { return v2u{pk(v.x, v.y), pk(v.z, v.w)}; }

__global__ void __launch_bounds__(512, 2) fused_fwd(Params p) {
  const lptr L = (lptr)(size_t)L_BASE;
  const int tid = threadIdx.x, lane0 = tid & 63, w = __builtin_amdgcn_readfirstlane(tid >> 6);
  const int c = blockIdx.x, xc = c & 7, j = c >> 3, grp = j >> 4, u = j & 15, g = 2 * xc + grp;
  u64 tt[8] = {};
  TT(0);
  const int ab = u >> 2, as = u & 3;
  const bool hasP1 = u <= 8, hasP3 = u >= 7;
  constexpr unsigned ldsb = L_BASE;
  const rsrc_t rsW = mkrs((const void*)(((size_t)p.ws + PG - 1) & ~(size_t)(PG - 1)), WS_USED);
  constexpr float CS = 0.08838834764831845f * 1.4426950408889634f;
  v8u K8[8], V8[8];
  bool hasF, isOut; unsigned b1v;
  lptr qa, pw, pr, aow, mlw, mbr, mmr; int qidx, pidx, mst; v4u pm;
  {
    const int lane = lane0, col = lane & 15, hq = lane >> 4, q4 = col >> 2, pp = lane & 3;
    const size_t kvbase = ((size_t)(ab * 16 + g) * SK + (SK - WIN) + as * 512 + w * 64) * DH;
    const rsrc_t rsK = mkrs(p.kc + kvbase, 16384), rsV = mkrs(p.vc + kvbase, 16384);
    const int rot = __builtin_amdgcn_readfirstlane((w >> 1) + 4 * (j & 3) + 2 * xc);
    const int kvo = q4 * 256 + (4 * hq + pp) * 16, vvo = lane * 16;
    const int r1 = u < 8 ? xc : 8, gq1 = u < 8 ? 8 * grp + u : g, n3 = (u + 9) & 15;
    v4u K[16], V[16], csw = {0, 0, 0, 0};
    int pub4, pub5, gatc;
    u64 tag = 0;
    if (w >= 4) {
      if (w == 4 && hasP1) {
        const rsrc_t rx = mkrs(p.x, 4 * DM * 2);
        const int xo_ = (hq * DM + col * 8) * 2;
        v4u xv[5];
#pragma unroll
        for (int k = 0; k < 4; ++k) xv[k] = __builtin_amdgcn_raw_buffer_load_b128(rx, xo_ + 256 * k, 0, 0);
        xv[4] = __builtin_amdgcn_raw_buffer_load_b128(rx, col < 8 ? xo_ + 1024 : 0x40000000, 0, 0);
        if (lane == 0) *(volatile LAS unsigned*)(L + L_RDY) = 0u;
        bar();
        SB();
        const int ks = r1 >> 1;
        const v4u xs = ks == 0 ? xv[0] : (ks == 1 ? xv[1] : (ks == 2 ? xv[2] : (ks == 3 ? xv[3] : xv[4])));
        if ((col >> 3) == (r1 & 1)) *(LAS v4u*)(L + L_XB + hq * XBS + (col & 7) * 16) = xs;
        float s1 = 0.f, s2 = 0.f, s3 = 0.f, s4 = 0.f;
#pragma unroll
        for (int k = 0; k < 5; ++k) {
          s1 = dot2(xv[k][0], 0x3F803F80u, s1); s2 = dot2(xv[k][0], xv[k][0], s2); s3 = dot2(xv[k][1], 0x3F803F80u, s3); s4 = dot2(xv[k][1], xv[k][1], s4);
          s1 = dot2(xv[k][2], 0x3F803F80u, s1); s2 = dot2(xv[k][2], xv[k][2], s2); s3 = dot2(xv[k][3], 0x3F803F80u, s3); s4 = dot2(xv[k][3], xv[k][3], s4);
        }
        s1 += s3; s2 += s4;
        s1 += dppf<XOR1>(s1); s2 += dppf<XOR1>(s2); s1 += dppf<XOR2>(s1); s2 += dppf<XOR2>(s2);
        s1 += dppf<ROR4>(s1); s2 += dppf<ROR4>(s2); s1 += dppf<ROR8>(s1); s2 += dppf<ROR8>(s2);
        const float mu = s1 * (1.f / DM), rs = __builtin_amdgcn_rsqf(fmaxf(s2 * (1.f / DM) - mu * mu, 0.f) + 1e-5f) * CS;
        if (col == 0) *(LAS f2*)(L + L_LN0 + hq * 8) = f2{mu, rs};
        OPAQUE();
        if (lane == 0) *(volatile LAS unsigned*)(L + L_RDY) = 1u;
        OPAQUE(); SB();
#pragma unroll
        for (int i = 0; i < 4; ++i) KLOAD(i);
      } else {
        KLOAD(0); KLOAD(1); KLOAD(2);
        if (hasP1) bar();
        KLOAD(3);
      }
    } else {
      if (w == 3) {
        const v4u SENT4 = {SENT, SENT, SENT, SENT};
        if (lane < 40) stx(rsW, SENT4, lane * 16, X2_OFF + (2 * g + (ab >> 1)) * BLK + ((ab & 1) * 4 + as) * X2_MSG);
        if (hasP3 && lane < 32) stx(rsW, SENT4, lane * 16, X3_OFF + n3 * BLK + g * 512);
        if (u == 15 && lane < 8) stx(rsW, SENT4, lane * 2048, WS_HER + g * 128);
      }
      const unsigned nearb = ((unsigned)__builtin_amdgcn_s_getreg(20 | (3 << 11)) >> 2) & 1u;
      v2u ep;
      asm volatile("buffer_load_dwordx2 %0, off, %1, 0 sc1" : "=v"(ep) : "s"(rsW) : "memory");
      {
        const rsrc_t rq = mkrs(hasP1 ? p.wq : p.wo, DM * QN * 2);
        const int rb = hasP1 ? QN * 2 : DM * 2, so = hasP1 ? 64 * r1 * (QN * 2) + gq1 * 512 + w * 128 : (256 * g + 64 * w) * (DM * 2) + n3 * 128;
#pragma unroll
        for (int i = 0; i < 8; ++i) dma128(lane, rq, rb, so, ldsb + (hasP1 ? L_WQ : L_WO) + w * 8192, i);
        if (hasP1) bar();
      }
      OPAQUE(); SB();
      v4u pf = {0, 0, 0, 0};
      if (PREFI) {
        u64 pc; unsigned len;
        asm volatile("s_getpc_b64 %0\n.Lfwd_pc:\n\ts_mov_b32 %1, .Lfwd_end-.Lfwd_pc" : "=s"(pc), "=s"(len));
        pf = __builtin_amdgcn_raw_buffer_load_b128(mkrs((const void*)(pc & ~15ull), len & ~15u), w == 2 ? j * 1024 + lane * 16 : 0x40000000, 0, 0);
      }
      OPAQUE(); SB();
      TT(1);
#pragma unroll
      for (int i = 0; i < 4; ++i) KLOAD(i);
      OPAQUE(); SB();
      asm volatile("s_waitcnt vmcnt(%1)" : "+v"(ep) : "i"(12 + PREFI) : "memory");
      tag = next_tag(ep);
      if (w == 3 && lane == 0) { const u64 f = tag + (u64)c * 0x9E3779B97F4A7C15ull; stx2(rsW, v2u{((unsigned)f & ~1u) | nearb, (unsigned)(f >> 32)}, 0, WS_FLAGS + c * 8); }
      if (hasP1) {
        A128();
        asm volatile("s_waitcnt vmcnt(%0)" ::"i"(4 + PREFI) : "memory");
        TT(2);
        v4u A[8];
#pragma unroll
        for (int t = 0; t < 4; ++t) { const int base = L_WQ + w * 8192 + a128 + (t ^ z128) * 32; A[2 * t] = trfrag(L, base, 512); A[2 * t + 1] = trfrag(L, base + 4096, 512); }
        for (int spin = 0; *(volatile LAS unsigned*)(L + L_RDY) == 0u && spin < (SPIN_CAP << 4); ++spin) __builtin_amdgcn_s_sleep(1);
        OPAQUE();
        TT(3);
        const v4u xf0 = lds16(L, L_XB + pp * XBS + hq * 16), xf1 = lds16(L, L_XB + pp * XBS + hq * 16 + 64);
        const f2 ln = *(LAS f2*)(L + L_LN0 + pp * 8);
        SB();
        f4 d = Z4, e = Z4;
#pragma unroll
        for (int t = 0; t < 4; ++t) {
          const unsigned m = q4 == t ? ~0u : 0u, o = m & 0x3F803F80u;
          d = mfma(A[2 * t], xf0 & m, d); e = mfma(A[2 * t], v4u{o, o, o, o}, e);
          d = mfma(A[2 * t + 1], xf1 & m, d); e = mfma(A[2 * t + 1], v4u{o, o, o, o}, e);
        }
        d = (d - ln.x * e) * ln.y;
        stn(rsW, v4u{pk(d.x, d.y), (unsigned)tag & 0xBFFFBFFFu, pk(d.z, d.w), (unsigned)(tag >> 32) & 0xBFFFBFFFu},
            pp * 1024 + (16 * w + 4 * q4 + hq) * 16, X1A_OFF + (gq1 * 9 + r1) * X1_MSG);
        csw = split4(e);
      }
      TT(4);
      asm volatile("" ::"v"(pf));
    }
    OPAQUE(); SB();
    v4u fa;
#define WIN() do { if (WND) asm volatile("s_waitcnt vmcnt(%0)" ::"i"(WND - 1) : "memory"); } while (0)
#pragma unroll
    for (int i = 4; i < 16; ++i) {
      if (i >= WND) WIN();
      KLOAD(i); SB();
      if (i == FPOS) { if (w < 4) fa = ldn(rsW, lane * 16 + (w & 1) * 1024, WS_FLAGS); OPAQUE(); SB(); }
    }
    {
      const int fi0 = 14 * grp + (u <= 6 ? u : u - 2);
      hasF = (u != 7) && (u != 8) && (fi0 < 27);
      const int fi = fi0 < 27 ? fi0 : 0;
      const int n4 = fi < 24 ? xc : 8, f4i = fi < 24 ? fi : 3 * xc + fi - 24, f5 = 3 * xc + fi / 9, n5 = fi % 9;
      const int sl = u == 8 ? 8 : (hasF ? n4 : xc);
      isOut = grp == 0 && (u == 0 || (u == 8 && xc == 0));
      int pub1, pub2, pub3, gat1, gat2, gat3, gat4, gat5, mko, cls, slo, wofs, w1ofs, w2ofs, b1ofs;
      pub1 = X1_OFF + 5 * gq1 * BLK + (r1 < 8 ? r1 * 1024 : 4 * BLK); pub2 = X2_OFF + (2 * g + (ab >> 1)) * BLK + ((ab & 1) * 4 + as) * X2_MSG;
      pub3 = X3_OFF + n3 * BLK + g * 512; pub4 = X4_OFF + (5 * f4i + (n4 >> 1)) * BLK + (n4 & 1) * 4096; pub5 = X5_OFF + (2 * n5 + (f5 >> 4)) * BLK + (f5 & 15) * 512;
      gat1 = X1_OFF + 5 * g * BLK; gat2 = X2_OFF + 2 * g * BLK; gat3 = X3_OFF + sl * BLK;
      gat4 = X4_OFF + 5 * f5 * BLK; gat5 = X5_OFF + 2 * sl * BLK; gatc = X1A_OFF + g * 9 * X1_MSG; mko = 0;
      cls = (gq1 >> 1) | (n3 << 4) | ((f4i / 3) << 8) | (n5 << 12) | (r1 << 16) | (u << 20) | (g << 24);
      slo = 64 * sl; wofs = 256 * g * (DM * 2) + n3 * 128; w1ofs = 64 * n4 * (FF * 2) + f4i * 384; w2ofs = 192 * f5 * (DM * 2) + n5 * 128; b1ofs = 192 * f5;
      b1v = __builtin_amdgcn_raw_buffer_load_b32(mkrs(p.b1, FF * 2), w < 6 ? (b1ofs + HFO) * 2 : 0x40000000, 0, AUX_NT);
      if (w == 6 && lane == 0) {
        LAS int* tbl = (LAS int*)(L + L_TB);
        tbl[0] = pub1; tbl[1] = pub2; tbl[2] = pub3; tbl[3] = pub4; tbl[4] = pub5; tbl[5] = gat1; tbl[6] = gat2; tbl[7] = gat3; tbl[8] = gat4; tbl[9] = gat5; tbl[10] = gatc; tbl[11] = mko; tbl[12] = cls; tbl[13] = slo; tbl[14] = wofs; tbl[15] = w1ofs; tbl[16] = w2ofs; tbl[17] = b1ofs; tbl[18] = xc; tbl[19] = ab;
      }
      if (hasF) {
        const rsrc_t ra = mkrs(p.w1, DM * FF * 2), rb = mkrs(p.w2, FF * DM * 2);
        const int vo = (lane >> 2) * (FF * 2) + ((lane & 3) ^ (2 * (lane >> 5))) * 16;
#pragma unroll
        for (int i = 0; i < 3; ++i) {
          const int k = 3 * w + i;
          WIN(); dma(ra, vo, w1ofs + 16 * (k & 3) * (FF * 2) + (k >> 2) * 64, ldsb + L_W1 + k * 1024);
          WIN(); dma(rb, (lane >> 3) * (DM * 2) + ((((lane & 7) >> 1) ^ (((lane >> 4) & 1) | ((k & 1) << 1))) * 32) + (lane & 1) * 16, w2ofs + 8 * k * (DM * 2), ldsb + L_W2 + k * 1024);
        }
      } else if (hasP1 && hasP3) {
        const rsrc_t ro = mkrs(p.wo, QN * DM * 2);
#pragma unroll
        for (int i = 0; i < 4; ++i) { WIN(); dma128(lane, ro, DM * 2, wofs + 32 * w * (DM * 2), ldsb + L_WO + w * 4096, i); }
      }
      qa = L + L_QP + (pp * 4 + hq) * 16;
      qidx = (lane & 1) ? 0xEEEE : 0x4444; pidx = pp * 0x1111;
      pw = L + L_PA + w * 2176 + (2 * hq + (pp >> 1)) * 272 + (2 * (pp & 1) + (q4 >> 1)) * 64 + (q4 & 1) * 8;
      pr = L + L_PA + w * 2176 + (hq * 4 + (col >> 3) * 2 + (q4 & 1)) * 16;
      aow = L + L_AO + w * 512 + ((lane >> 5) * 128 + 8 * col + 4 * (hq & 1)) * 2;
      mlw = L + L_ML + (w * 2 + (lane >> 5)) * 8;
      mbr = L + L_AO + (w & 1) * 256 + hq * 512 + col * 16; mmr = L + L_ML + (w & 1) * 8 + hq * 16;
      mst = hq < 2 ? (w & 1) * 256 + col * 16 + hq * 8 : 0x40000000;
      const unsigned hm = (lane & 1) ? 0xFFFF0000u : 0xFFFFu;
#pragma unroll
      for (int k = 0; k < 4; ++k) pm[k] = ((col & 7) >> 1) == k ? hm : 0u;
      PINI(qa); PINI(pw); PINI(pr); PINI(aow); PINI(mlw); PINI(mbr); PINI(mmr); PINI(qidx); PINI(pidx); PINI(mst); PINI(pm);
      const v4u z = {0, 0, 0, 0};
      const lptr pa = L + L_PA + w * 2176 + lane * 16;
      *(LAS v4u*)pa = z; *(LAS v4u*)(pa + 1024) = z; if (lane < 8) *(LAS v4u*)(pa + 2048) = z;
      if (w == 7 && lane == 0) { *(LAS unsigned*)(L + L_TM + 4) = 0u; *(LAS unsigned*)(L + L_TM + 12) = 0u; }
      if (w == 7 && lane < 36) { *(LAS unsigned*)(L + L_XB + 4 * XBS + lane * 4) = 0x3F803F80u; *(LAS unsigned*)(L + L_XB + 5 * XBS + lane * 4) = 0u; }
    }
    SB();
    auto transp = [&](int T) {
      const bool o1 = lane & 1, o2 = lane & 2;
#pragma unroll
      for (int d = 0; d < 4; ++d) {
        const unsigned m0 = K[4 * T][d], m1 = K[4 * T + 1][d], m2 = K[4 * T + 2][d], m3 = K[4 * T + 3][d];
        const unsigned s0 = dppu<XOR1>(m0), s1 = dppu<XOR1>(m1), s2 = dppu<XOR1>(m2), s3 = dppu<XOR1>(m3);
        const unsigned n0 = o1 ? s1 : m0, n1 = o1 ? m1 : s0, n2 = o1 ? s3 : m2, n3_ = o1 ? m3 : s2;
        const unsigned t0 = dppu<XOR2>(n0), t1 = dppu<XOR2>(n1), t2 = dppu<XOR2>(n2), t3 = dppu<XOR2>(n3_);
        K8[2 * T][d] = o2 ? t2 : n0; K8[2 * T + 1][d] = o2 ? n2 : t0;
        K8[2 * T][4 + d] = o2 ? t3 : n1; K8[2 * T + 1][4 + d] = o2 ? n3_ : t1;
      }
      asm volatile("" : "+v"(K8[2 * T]), "+v"(K8[2 * T + 1]));
    };
    int x1a = hq * X1_MSG + (16 * (w & 3) + col) * 16 + ab * 1024, x1b = x1a + 4 * X1_MSG, x1c = x1a + (8 - hq) * X1_MSG;
    int ta = w >= 4 ? ((hasF && lane < (w == 7 ? 56 : 64)) ? pub4 + (w - 4) * 1024 + lane * 16 : 0x40000000)
                    : ((hasP1 && pp == 0) ? X1A_OFF + (gq1 * 9 + r1) * X1_MSG + 4096 + (16 * w + 4 * q4 + hq) * 16 : 0x40000000);
    int tb = (hasF && lane < 32) ? pub5 + (w & 1) * HALF + lane * 16 : 0x40000000;
    if (w >= 4) csw = v4u{SENT, SENT, SENT, SENT};
    const unsigned one = (lane & 1) ? 0x3F800000u : 0x00003F80u;
    v4u selT = {(col & 2) ? 0u : one, 0u, (col & 2) ? one : 0u, 0u}, selT0 = hq == 0 ? selT : v4u{0, 0, 0, 0};
    unsigned tgx = (unsigned)tag & 0xBFFFBFFFu, tgy = (unsigned)(tag >> 32) & 0xBFFFBFFFu;
    lptr qw = L + QPW(w & 3);
    PINI(x1a); PINI(x1b); PINI(x1c); PINI(ta); PINI(tb); PINI(csw); PINI(selT); PINI(selT0); PINI(tgx); PINI(tgy); PINI(qw);
    v4u g0, g1, g2;
#pragma unroll
    for (int i = 0; i < 16; ++i) {
      WIN(); VLOAD(i); SB();
      if (i == FPOS - 16) { if (w < 4) fa = ldn(rsW, lane * 16 + (w & 1) * 1024, WS_FLAGS); OPAQUE(); SB(); }
      if (i == XPOS) {
        if (w < 4) { g0 = ldn(rsW, x1a, gatc); g1 = ldn(rsW, x1b, gatc); g2 = ldn(rsW, x1c, gatc); }
        OPAQUE(); SB();
      }
      if (i == CPOS) {
        stn(rsW, csw, ta, 0);
        if (w == 4 || w == 5) stn(rsW, csw, tb, 0);
        OPAQUE(); SB();
      }
      if (i == SPOS) {
        PINI(fa);
        if (w < 4) {
          const u64 e0 = tag + (u64)(lane * 2 + (w & 1) * 128) * 0x9E3779B97F4A7C15ull, e1 = e0 + 0x9E3779B97F4A7C15ull;
          int spin = 0;
#pragma nounroll
          for (; spin < SPIN_CAP; ++spin) {
            if (!any(((fa.x ^ (unsigned)e0) > 1u) || fa.y != (unsigned)(e0 >> 32) || ((fa.z ^ (unsigned)e1) > 1u) || fa.w != (unsigned)(e1 >> 32))) break;
            OPAQUE();
            fa = ldn(rsW, lane * 16 + (w & 1) * 1024, WS_FLAGS);
            PINI(fa);
          }
          if (w == 0) COUNT(40, spin);
          if (w == 0) {
            const unsigned se = (unsigned)__builtin_amdgcn_ballot_w64(fa.x & 1u), so_ = (unsigned)__builtin_amdgcn_ballot_w64(fa.z & 1u);
            unsigned dm = 0;
#pragma unroll
            for (int k = 0; k < 4; ++k) dm |= ((se >> k) & 1u) << (2 * k) | ((so_ >> k) & 1u) << (2 * k + 1);
            if (lane == 0) { *(LAS unsigned*)(L + L_TM + 8) = dm; *(LAS u64*)(L + L_TAG + 8) = tag; }
          }
        }
        OPAQUE(); SB();
      }
      if (i == TP0) { transp(0); SB(); }
      if (i == TP1) { transp(1); SB(); }
      if (i == TP2) { transp(2); SB(); }
      if (i == TP3) { transp(3); SB(); }
    }
#pragma unroll
    for (int m = 0; m < 8; ++m) V8[m] = cat(V[2 * m], V[2 * m + 1]);
    OPAQUE(); SB();
    STAMPW(0, 3); STAMPW(7, 4);
    if (w == 0) { COUNT(0, tt[0]); COUNT(60, __builtin_amdgcn_s_memtime()); COUNT(59, __builtin_amdgcn_s_memrealtime()); if (hasP1) { COUNT(1, tt[1]); COUNT(2, tt[2]); COUNT(5, tt[3]); COUNT(35, tt[4]); } }
    asm volatile("" : "+v"(g0), "+v"(g1), "+v"(g2));
    if (w < 4) {
      int spin = 0;
#pragma nounroll
      for (; spin < SPIN_CAP; ++spin) {
        if (!any(g0.y != tgx || g0.w != tgy || g1.y != tgx || g1.w != tgy || g2.y != tgx || g2.w != tgy)) break;
        OPAQUE();
        g0 = ldn(rsW, x1a, gatc); g1 = ldn(rsW, x1b, gatc); g2 = ldn(rsW, x1c, gatc);
        asm volatile("" : "+v"(g0), "+v"(g1), "+v"(g2));
      }
      f4 q = mfma(selT, g0, Z4); q = mfma(selT, g1, q); q = mfma(selT0, g2, q);
      if (lane < 16) { LAS unsigned* qp = (LAS unsigned*)qw; qp[0] = pk(q.x, q.y); qp[16] = pk(q.z, q.w); }
      if (w == 0) COUNT(41, spin);
      STAMPW(0, 6);
    }
  }
  f4 S[4] = {Z4, Z4, Z4, Z4}, O = Z4;
  PIN4(S); PINI(O);
  bar();
  STAMPW(0, 7);
  v2u xp = {0, 0}, b2v = {0, 0};
  f4 csq = Z4; f2 cs1 = {0.f, 0.f};
  unsigned t0 = 0, hm = 0;
#define HB(k) __builtin_amdgcn_readfirstlane((int)(((hm >> (k)) & 1u) << 13))
#define HOWN HB(xc_)
#define H1 HB(cls & 15)
#define H3 HB((cls >> 4) & 15)
#define H4 HB((cls >> 8) & 15)
#define H5 HB((cls >> 12) & 15)
  const int w_ = w;
  lptr CT = L + L_TAG;
  PINI(CT);
#define pub1 RS(0)
#define pub2 RS(1)
#define pub3 RS(2)
#define pub4 RS(3)
#define pub5 RS(4)
#define gat1 RS(5)
#define gat2 RS(6)
#define gat3 RS(7)
#define gat4 RS(8)
#define gat5 RS(9)
#define gatc RS(10)
#define mko RS(11)
#define cls RS(12)
#define slo RS(13)
#define wofs RS(14)
#define w1ofs RS(15)
#define w2ofs RS(16)
#define b1ofs RS(17)
#define xc_ RS(18)
#define ab RS(19)
#define KARG() const __attribute__((address_space(4))) Params* kp = (const __attribute__((address_space(4))) Params*)__builtin_amdgcn_kernarg_segment_ptr(); asm volatile("" : "+s"(kp))
  int mso = pub2;
  PINS(mso);
#pragma nounroll
  for (int lay = 0; lay < 2; ++lay) {
    const int xs = __builtin_amdgcn_readfirstlane(lay * SM_STRIDE), x4 = __builtin_amdgcn_readfirstlane(lay * PG), l1 = __builtin_amdgcn_readfirstlane(lay ? HALF : 0);
    int w = w_;
    PINS(w);
    {
      v4u AF[8];
      const v4u Q0 = lds16(qa, 0), Q1 = lds16(qa, 256);
      SB();
#pragma unroll
      for (int T = 0; T < 4; ++T) S[T] = smf(Q0, K8[2 * T], S[T], qidx);
#pragma unroll
      for (int T = 0; T < 4; ++T) S[T] = smf(Q1, K8[2 * T + 1], S[T], qidx);
      float s0, s1;
      asm("s_nop 7\n\t"
          "v_add_f32_dpp %0, %2, %3 quad_perm:[0,1,2,3] row_mask:0x1 bank_mask:0xf\n\tv_add_f32_dpp %1, %4, %5 quad_perm:[0,1,2,3] row_mask:0x1 bank_mask:0xf\n\t"
          "v_add_f32_dpp %0, %6, %7 quad_perm:[0,1,2,3] row_mask:0x2 bank_mask:0xf\n\tv_add_f32_dpp %1, %8, %9 quad_perm:[0,1,2,3] row_mask:0x2 bank_mask:0xf\n\t"
          "v_add_f32_dpp %0, %10, %11 quad_perm:[0,1,2,3] row_mask:0x4 bank_mask:0xf\n\tv_add_f32_dpp %1, %12, %13 quad_perm:[0,1,2,3] row_mask:0x4 bank_mask:0xf\n\t"
          "v_add_f32_dpp %0, %14, %15 quad_perm:[0,1,2,3] row_mask:0x8 bank_mask:0xf\n\tv_add_f32_dpp %1, %16, %17 quad_perm:[0,1,2,3] row_mask:0x8 bank_mask:0xf"
          : "=&v"(s0), "=&v"(s1)
          : "v"(S[0].x), "v"(S[0].y), "v"(S[0].z), "v"(S[0].w), "v"(S[1].x), "v"(S[1].y), "v"(S[1].z), "v"(S[1].w),
            "v"(S[2].x), "v"(S[2].y), "v"(S[2].z), "v"(S[2].w), "v"(S[3].x), "v"(S[3].y), "v"(S[3].z), "v"(S[3].w));
      auto sw = __builtin_amdgcn_permlane32_swap(asu(s0), asu(s1), false, false);
      float mx;
      asm("v_max_f32 %0, %1, %2\n\ts_nop 1\n\tv_max_f32_dpp %0, %0, %0 quad_perm:[1,0,3,2] row_mask:0xf bank_mask:0xf\n\ts_nop 1\n\t"
          "v_max_f32_dpp %0, %0, %0 quad_perm:[2,3,0,1] row_mask:0xf bank_mask:0xf\n\ts_nop 1\n\tv_max_f32_dpp %0, %0, %0 row_ror:4 row_mask:0xf bank_mask:0xf\n\ts_nop 1\n\t"
          "v_max_f32_dpp %0, %0, %0 row_ror:8 row_mask:0xf bank_mask:0xf\n\ts_nop 1\n\tv_max_f32_dpp %0, %0, %0 row_bcast:15 row_mask:0xa bank_mask:0xf\n\ts_nop 0"
          : "=&v"(mx) : "v"(asf(sw[0])), "v"(asf(sw[1])));
      const float m0 = asf(__builtin_amdgcn_readlane(asu(mx), 31)), m1 = asf(__builtin_amdgcn_readlane(asu(mx), 63));
      const float p0 = __builtin_amdgcn_exp2f(s0 - m0), p1 = __builtin_amdgcn_exp2f(s1 - m1);
      const unsigned pb = pk(p0, p1);
      *(LAS u16*)(pw) = (u16)pb; *(LAS u16*)(pw + 20) = (u16)pb; *(LAS u16*)(pw + 32) = (u16)(pb >> 16); *(LAS u16*)(pw + 52) = (u16)(pb >> 16);
#pragma unroll
      for (int m = 0; m < 8; ++m) AF[m] = lds16(pr, m * 272);
      SB();
      auto sl2 = __builtin_amdgcn_permlane32_swap(asu(p0), asu(p1), false, false);
      float ls = asf(sl2[0]) + asf(sl2[1]);
      ls += dppf<XOR1>(ls); ls += dppf<XOR2>(ls); ls += dppf<ROR4>(ls); ls += dppf<ROR8>(ls);
      ls += asf((unsigned)__builtin_amdgcn_update_dpp(0, (int)asu(ls), 0x142, 0xa, 0xf, false));
      if ((lane0 & 31) == 31) *(LAS f2*)mlw = f2{mx, ls};
      SB();
#pragma unroll
      for (int m = 0; m < 8; ++m) O = smf(AF[m], V8[m], O, pidx);
      *(LAS v2u*)aow = pk4(O);
    }
    bar();
    STAMPW(0, 8 + lay * 16);
#define XBPTR() lptr xb0 = L + L_XB + (col < 4 ? col : 5) * XBS + hq * 16, xb1 = L + L_XB + ((col >= 4 && col < 8) ? col - 4 : 5) * XBS + hq * 16; PINI(xb0); PINI(xb1)
#define STPTR() lptr sra = L + L_XB + (pp == 0 ? 4 : (pp == 1 ? q4 : 5)) * XBS + hq * 16, srb = L + L_XB + pp * XBS + hq * 16; int sso = col == hq ? col * 16 : 0x40000000; PINI(sra); PINI(srb); PINI(sso)
#define STATS(off) do { const v4u a0 = lds16(sra, 0), a1 = lds16(sra, 64), b0 = lds16(srb, 0), b1 = lds16(srb, 64); SB(); \
      f4 d_ = mfma(a0, b0, Z4); d_ = mfma(a1, b1, d_); stx2(rsW, v2u{cl(asu(d_.x)), cl(asu(d_.y))}, sso, off); } while (0)
#define STAT1 (pub1 + H1 + (((cls >> 16) & 15) < 8 ? 4 * BLK + 4096 - ((cls >> 16) & 15) * 896 : 4096 + 1024))
#define P1PREP() v4u A1[4]; int st1 = col < 8 ? pp * (((cls >> 16) & 15) < 8 ? BLK : 1024) + (8 * w + 4 * q4 + hq) * 16 : 0x40000000, sp1 = pub1 + H1; XBPTR(); \
      { const int base = L_WQ + (w >> 1) * 8192 + a128, t0 = 2 * (w & 1); \
        A1[0] = trfrag(L, base + (t0 ^ z128) * 32, 512); A1[1] = trfrag(L, base + (t0 ^ z128) * 32 + 4096, 512); \
        A1[2] = trfrag(L, base + ((t0 + 1) ^ z128) * 32, 512); A1[3] = trfrag(L, base + ((t0 + 1) ^ z128) * 32 + 4096, 512); } \
      PIN4(A1); PINI(st1); PINS(sp1)
#define P1RUN() do { if (hasP1) { const v4u b00 = lds16(xb0, 0), b01 = lds16(xb0, 64), b10 = lds16(xb1, 0), b11 = lds16(xb1, 64); SB(); \
        f4 d_ = mfma(A1[0], b00, Z4); d_ = mfma(A1[1], b01, d_); d_ = mfma(A1[2], b10, d_); d_ = mfma(A1[3], b11, d_); stx(rsW, split4(d_), st1, sp1); } } while (0)
#define X1PREP() const unsigned one1 = (lane & 1) ? 0x3F800000u : 0x00003F80u; \
      v4u sel4 = {(col & 2) ? 0u : one1, (col & 2) ? one1 : 0u, (col & 2) ? 0u : one1, (col & 2) ? one1 : 0u}, sel40 = hq == 0 ? sel4 : v4u{0, 0, 0, 0}; \
      const int g1_ = gat1 + HOWN, ab_ = ab; \
      int qo = g1_ + ab_ * BLK, qo1 = qo + 4096, qo2 = g1_ + 4 * BLK + ab_ * 1024, qo3 = g1_ + 4 * BLK + 4096 + ab_ * 16; \
      int qv2 = (16 * (w - 4) + col) * 16, qv0 = qv2 + hq * 1024, qvs = (col < 9 ? col : 8) * 128; \
      float qsm = col < 9 ? (1.f / DM) : 0.f; lptr qw = L + QPW(w - 4); unsigned T1 = t0 + T1B; \
      PINS(qo); PINS(qo1); PINS(qo2); PINS(qo3); PINS(T1); PINI(sel4); PINI(sel40); PINI(qv0); PINI(qv2); PINI(qvs); PINI(qw); PINI(qsm)
#define X1RUN() do { LOOKAT(T1); f4 d; int spin = 0; \
      for (;; ++spin) { OPAQUE(); \
        const v2u st = ldx2(rsW, qvs, qo3); SB(); \
        const v4u d0 = ldx(rsW, qv0, qo), d1 = ldx(rsW, qv0, qo1), d2 = ldx(rsW, qv2, qo2); SB(); \
        float s1 = asf(st.x) * qsm, s2 = asf(st.y) * qsm; \
        s1 += dppf<XOR1>(s1); s2 += dppf<XOR1>(s2); s1 += dppf<XOR2>(s1); s2 += dppf<XOR2>(s2); SB(); \
        d = mfma(sel4, d0, Z4); d = mfma(sel4, d1, d); d = mfma(sel40, d2, d); SB(); \
        s1 += dppf<ROR4>(s1); s2 += dppf<ROR4>(s2); s1 += dppf<ROR8>(s1); s2 += dppf<ROR8>(s2); \
        const float rs = __builtin_amdgcn_rsqf(fmaxf(s2 - s1 * s1, 0.f) + 1e-5f) * CS; \
        d = d * rs + (-s1 * rs) * csq; \
        if (!any(d.x != d.x)) break;                               \
        if (!any(max(max(max(mx4(d0), mx4(d1)), mx4(d2)), max(st.x, st.y)) == SENT) || spin > SPIN_CAP) break; } \
      OPAQUE(); \
      if (lane0 < 16) { LAS unsigned* qp = (LAS unsigned*)qw; qp[0] = pk(d.x, d.y); qp[16] = pk(d.z, d.w); } \
      LATE(T1); if (w == 4) COUNT(49, spin); STAMPW(4, 22); } while (0)
#define CSQLOAD() const int ve = hq * X1_MSG + (16 * (w & 3) + col) * 16 + 4096;     \
      const int gc_ = gatc; const v4u c0 = ldx(rsW, ve, gc_), c1 = ldx(rsW, ve + 4 * X1_MSG, gc_), c2 = ldx(rsW, ve + (8 - hq) * X1_MSG, gc_)
#define CSQSUM() do { const unsigned one_ = (lane & 1) ? 0x3F800000u : 0x00003F80u; \
      const v4u sl4 = {(col & 2) ? 0u : one_, (col & 2) ? one_ : 0u, (col & 2) ? 0u : one_, (col & 2) ? one_ : 0u}; \
      csq = mfma(sl4, c0, Z4); csq = mfma(sl4, c1, csq); csq = mfma(hq == 0 ? sl4 : v4u{0, 0, 0, 0}, c2, csq); } while (0)
    if (w < 6) {
      int ymsg;
      if (w < 4) {
        FRESH(); A128();
        int so = gat2 + (w >> 1) * BLK + (w & 1) * 4 * X2_MSG + xs + ((HOWN ^ HALF) & l1), vm = hq * X2_MSG + 512 + (col >> 3) * 16;
        ymsg = col < 4 ? (4 * (2 * w + (hq >> 1)) + pp) * 16 + (hq & 1) * 8 : 0x40000000;
        PINS(so); PINI(vm); PINI(ymsg);
        int spin = 0;
        if (hasP3) {
          int vd = hq * X2_MSG + col * 16, sp = pub3 + xs + ((H3 ^ HALF) & l1);
          lptr p3a = L + L_WO + a128 + (w ^ z128) * 32, p3b = L + L_OB + pp * OBS + hq * 16;
          v4u pm0 = col < 8 ? pm : v4u{0, 0, 0, 0}, pm1 = col < 8 ? v4u{0, 0, 0, 0} : pm;
          lptr ow = L + L_OB + w * OBS + ((hq >> 1) * 128 + 8 * col + 4 * (hq & 1)) * 2;
          int dual3 = __builtin_amdgcn_readfirstlane(lay == 1 && ((cls >> 4) & 15) == 8);
          PINS(dual3); PINI(vd); PINS(sp); PINI(p3a); PINI(p3b); PINI(pm0); PINI(pm1); PINI(ow);
          v4u A[8];
#pragma unroll
          for (int k = 0; k < 8; ++k) A[k] = trfrag(p3a, k * 4096, 512);
          if (lay == 0) __builtin_amdgcn_s_sleep(D_X2); else LOOKAT(t0 + T2B);
          f4 o;
          for (;; ++spin) {
            OPAQUE();
            const v2u ml = ldx2(rsW, vm, so);
            SB();
            const v4u d0 = ldx(rsW, vd, so), d1 = ldx(rsW, vd + 256, so);
            SB();
            const float m = asf(ml.x), M = max32(max16(m));
            const float f = __builtin_amdgcn_exp2f(m - M);
            const float Ls = add32(add16(f * asf(ml.y)));
            const float fs = f * __builtin_amdgcn_rcpf(Ls);
            const unsigned gg = pk(fs, fs);
            o = mfma(pm0 & gg, d0, Z4); o = mfma(pm1 & gg, d1, o);
            if (!any(o.x != o.x)) break;
            if (!any(max(max(mx4(d0), mx4(d1)), max(ml.x, ml.y)) == SENT) || spin > SPIN_CAP) break;
          }
          OPAQUE();
          *(LAS v2u*)ow = pk4(o);
          if (lay == 1) LATE(t0 + T2B);
          STAMPW(0, 10 + lay * 16);
          bar();
          STAMPW(0, 11 + lay * 16);
          v4u B[8];
#pragma unroll
          for (int k = 0; k < 8; ++k) B[k] = lds16(p3b, k * 64);
          SB();
          f4 d = Z4;
#pragma unroll
          for (int k = 0; k < 8; ++k) d = mfma(A[k], B[k], d);
          const v2u yv = pack4(d);
          stx2(rsW, yv, ymsg, sp);
          if (dual3) stx2(rsW, yv, ymsg, sp ^ HALF);
          STAMPW(0, 20 + lay * 16);
        } else bar();
        if (w == 0) COUNT(42 + lay * 8, spin);
      } else {
        ymsg = 0;
        if (lay == 0) {
          FRESH();
          unsigned sn = SENT; PINI(sn);
          const v4u SENT4 = {sn, sn, sn, sn};
          const int lo = lane * 16, r1 = (cls >> 16) & 15;
          __builtin_amdgcn_s_sleep(D_W);
          const int OOB = 0x40000000;
          if (hasF && REWARM) {
            const int a4 = pub4 + (w - 4) * 2048;
            stx(rsW, SENT4, lo, a4); stx(rsW, SENT4, (w == 4 || (lane >= 48 && lane < 56)) ? lo : OOB, a4 + 1024); stx(rsW, SENT4, lane < 32 ? lo : OOB, pub5 + (w - 4) * HALF);
          }
          if (hasF) {
            const unsigned o0 = col == 0 ? 0x3F803F80u : 0u, o1 = col == 4 ? 0x3F803F80u : 0u;
            const int s0 = (hq & 1) * 32, p4_ = pub4;
#pragma unroll
            for (int i = 0; i < 3; ++i) {
              const int t = 3 * (w - 4) + i;
              const lptr pa0 = L + L_W1 + t * 4096 + hq * 512 + q4 * 64 + pp * 8 + s0, pa1 = pa0 + 32 - 2 * s0;
              f4 e = mfma(trfrag(pa0, 0, 256), v4u{o0, o0, o0, o0}, Z4); e = mfma(trfrag(pa0, 2048, 256), v4u{o0, o0, o0, o0}, e);
              e = mfma(trfrag(pa1, 0, 256), v4u{o1, o1, o1, o1}, e); e = mfma(trfrag(pa1, 2048, 256), v4u{o1, o1, o1, o1}, e);
              stx(rsW, split4(e), (col & 11) == 0 ? 3072 + t * 128 + (col + hq) * 16 : OOB, p4_);
            }
          }
          if (hasP1) {
            const int rs = r1 < 8 ? BLK : 1024, a1 = pub1 + (w - 4) * HALF;
            stx(rsW, SENT4, lo, a1); stx(rsW, SENT4, lo, a1 + rs); stx(rsW, SENT4, lo, a1 + 2 * rs); stx(rsW, SENT4, lo, a1 + 3 * rs);
            stx(rsW, SENT4, lane < 8 ? lo : OOB, STAT1 + (w - 4) * HALF);
          }
          if (hasF) {
            drain();
            bar();
          } else {
            stx(rsW, SENT4, lane < 40 ? lo : OOB, pub2 + SM_STRIDE + (w - 4) * HALF);
            stx(rsW, SENT4, lane < 32 ? lo : OOB, pub3 + SM_STRIDE + (w - 4) * HALF);
            drain(); bar();
          }
        } else bar();
      }
      int spin = 0;
      if (hasF) {
        FRESH(); A128(); XBPTR();
        const int s0 = (hq & 1) * 32;
        const lptr p4a0 = L + L_W1 + w * 4096 + hq * 512 + q4 * 64 + pp * 8 + s0, p4a1 = p4a0 + 32 - 2 * s0;
        v4u A4[4] = {trfrag(p4a0, 0, 256), trfrag(p4a0, 2048, 256), trfrag(p4a1, 0, 256), trfrag(p4a1, 2048, 256)}, A[6];
        const lptr p5a = L + L_W2 + a128 + ((w & 3) ^ z128) * 32;
#pragma unroll
        for (int k = 0; k < 6; ++k) A[k] = trfrag(p5a, k * 4096, 512);
        const int pn = lane >> 2, pc = pn < 9 ? pn : 8, cl_ = cls;
        const int vv = 2 * (q4 & 1) + (col & 1);
        const unsigned on = ((col >> 3) == (hq >> 1) && (col & 2) == 0) ? ((vv & 1) ? 0x3F800000u : 0x3F80u) : 0u;
        v4u selh = {(vv & 2) ? 0u : on, (vv & 2) ? on : 0u, (vv & 2) ? 0u : on, (vv & 2) ? on : 0u};
        v4u selh0 = (hq & 1) ? v4u{0, 0, 0, 0} : selh;
        int st4 = col < 8 ? w * 512 + (16 * q4 + 4 * hq + pp) * 16 : 0x40000000, sp = pub4 + x4 + ((HB((cl_ >> 8) & 15) ^ HALF) & l1);
        int so = gat4 + x4 + ((HOWN ^ HALF) & l1), vs = (pc >> 1) * BLK + (pc & 1) * 4096 + 3840 + pp * 16, sp5 = pub5 + xs, c5 = (cl_ >> 12) & 15;
        float sm = pn < 9 ? (1.f / DM) : 0.f;
        lptr hw = L + L_HB + pp * HBS + HFO * 2, p5b = L + L_HB + pp * HBS + hq * 16;
        int vo4 = 4 * BLK + w * 512 + (16 * (hq >> 1) + col) * 16, vo = vo4 - 4 * BLK + (hq & 1) * 4096;
        int sa = so + BLK, sb = so + 2 * BLK, sc = so + 3 * BLK;
        f2 bb = {blo(b1v), bhi(b1v)};
        if (lay == 0) {
          f4 e;
          int p5_ = pub5, p2_ = pub2, p3_ = pub3, xo_ = xc_;
          asm volatile("" : "+s"(p5_), "+s"(p2_), "+s"(p3_), "+s"(xo_));
          PIN4(A4); PIN4(A); PINI(A[4]); PINI(A[5]); PINI(selh); PINI(selh0); PINI(st4); PINI(vs); PINI(hw); PINI(p5b); PINI(vo); PINI(vo4); PINI(bb); PINI(sm);
          for (int sp2 = 0; *(volatile LAS unsigned*)(CT + (L_TM - L_TAG) + 4) == 0u && sp2 < (SPIN_CAP << 4); ++sp2) __builtin_amdgcn_s_sleep(1);
          const int ve1 = 4 * BLK + 3072 + w * 128 + (4 * (hq >> 1) + q4) * 16, ve0 = ve1 + (2 * (hq & 1) + (pp >> 1) - 4) * BLK + (pp & 1) * 4096;
          for (int sp2 = 0;; ++sp2) {
            OPAQUE();
            const v4u e0 = ldx(rsW, ve0, so), e1 = ldx(rsW, ve1, so);
            e = mfma(selh, e0, Z4); const f4 e8 = mfma(selh0, e1, Z4);
            e.x += dppf<XOR1>(e.x); e.y += dppf<XOR1>(e.y); e.x += dppf<XOR2>(e.x); e.y += dppf<XOR2>(e.y);
            cs1 = f2{e.x + e8.x, e.y + e8.y};
            if (!any(max(mx4(e0), mx4(e1)) == SENT) || sp2 > SPIN_CAP) break;
          }
          hm = __builtin_amdgcn_readfirstlane(*(LAS unsigned*)(CT + (L_TM - L_TAG) + 16)); t0 = __builtin_amdgcn_readfirstlane(*(LAS unsigned*)(CT + (L_TM - L_TAG)));
          if (w >= 4) {
            unsigned sn = SENT; PINI(sn);
            const v4u SENT4 = {sn, sn, sn, sn};
            const int OOB = 0x40000000, lo = lane * 16, a0 = sp + PG + (HB((cl_ >> 8) & 15) ^ HALF), hi = (lane >> 5) << 13, l32 = (lane & 31) * 16;
            if (w == 4) {
              stx(rsW, SENT4, lo, a0); stx(rsW, SENT4, lo, a0 + 1024); stx(rsW, SENT4, lo, a0 + 2048); stx(rsW, SENT4, lane < 8 ? lo : OOB, a0 + 3840);
            } else {
              stx(rsW, SENT4, (lane < 32 || c5 == 8) ? l32 + (hi ^ HB(c5) ^ HALF) : OOB, p5_ + SM_STRIDE);
              stx(rsW, SENT4, lane < 40 ? lo : OOB, p2_ + SM_STRIDE + (HB(xo_) ^ HALF));
              stx(rsW, SENT4, (hasP3 && (lane < 32 || ((cl_ >> 4) & 15) == 8)) ? l32 + (hi ^ HB((cl_ >> 4) & 15) ^ HALF) : OOB, p3_ + SM_STRIDE);
            }
          }
        }
        sp5 += HB(c5) ^ HALF;
        unsigned T4 = t0 + (lay == 0 ? T4A : T4B);
        PINS(c5); PINS(sp); PINS(so); PINS(sp5); PINS(sa); PINS(sb); PINS(sc); PINS(T4); PINI(st4); PINI(selh); PINI(selh0); PINI(vs); PINI(sm); PINI(hw); PINI(vo); PINI(vo4); PINI(p5b); PINI(bb);
        PIN4(A4); PIN4(A); PINI(A[4]); PINI(A[5]); PINI(cs1);
        bar();
        STAMPW(0, 13 + lay * 16);
        {
          const v4u b00 = lds16(xb0, 0), b01 = lds16(xb0, 64), b10 = lds16(xb1, 0), b11 = lds16(xb1, 64);
          const int dl = DLAT();
          SB();
          f4 d = mfma(A4[0], b00, Z4); d = mfma(A4[1], b01, d); d = mfma(A4[2], b10, d); d = mfma(A4[3], b11, d);
          stx(rsW, split4(d), st4, sp);
          STAMPW(0, 14 + lay * 16);
          until(T4 + __builtin_amdgcn_readfirstlane(dl));
        }
        f4 a; float rstd; f2 t;
        for (;; ++spin) {
          OPAQUE();
          const v2u st = ldx2(rsW, vs, so);
          SB();
          const v4u d0 = ldx(rsW, vo, so), d1 = ldx(rsW, vo, sa), d2 = ldx(rsW, vo, sb), d3 = ldx(rsW, vo, sc), d4 = ldx(rsW, vo4, so);
          SB();
          float s1 = asf(st.x) * sm, s2 = asf(st.y) * sm;
          s1 += dppf<ROR4>(s1); s2 += dppf<ROR4>(s2); s1 += dppf<ROR8>(s1); s2 += dppf<ROR8>(s2);
          s1 = add32(add16(s1)); s2 = add32(add16(s2));
          rstd = __builtin_amdgcn_rsqf(fmaxf(s2 - s1 * s1, 0.f) + 1e-5f);
          t = bb - (s1 * rstd) * cs1;
          SB();
          a = mfma(selh, d0, Z4); a = mfma(selh, d1, a); a = mfma(selh, d2, a); a = mfma(selh, d3, a); a = mfma(selh0, d4, a);
          if (!any(a.x != a.x || t.x != t.x)) break;
          if (!any(max(max(max(mx4(d0), mx4(d1)), max(mx4(d2), mx4(d3))), max(mx4(d4), max(st.x, st.y))) == SENT) || spin > SPIN_CAP) break;
        }
        OPAQUE();
        STAMPW(0, 15 + lay * 16);
        const f2 v = f2{a.x, a.y} * rstd + t;
        const f2 e = v * (v * v * (-2.f * 0.7978845608028654f * 1.4426950408889634f * 0.044715f) + (-2.f * 0.7978845608028654f * 1.4426950408889634f));
        const float h0 = v.x * __builtin_amdgcn_rcpf(1.f + __builtin_amdgcn_exp2f(e.x)), h1 = v.y * __builtin_amdgcn_rcpf(1.f + __builtin_amdgcn_exp2f(e.y));
        *(LAS unsigned*)hw = pk(h0, h1);
        LATE(T4);
        bar();
        STAMPW(0, 16 + lay * 16);
        if (w < 4) {
          v4u B[6];
#pragma unroll
          for (int k = 0; k < 6; ++k) B[k] = lds16(p5b, k * 64);
          SB();
          f4 d = Z4;
#pragma unroll
          for (int k = 0; k < 6; ++k) d = mfma(A[k], B[k], d);
          const v2u zv = pack4(d);
          stx2(rsW, zv, ymsg, sp5);
          if (c5 == 8) stx2(rsW, zv, ymsg, sp5 ^ HALF);
          STAMPW(0, 21 + lay * 16);
        }
      } else {
        bar();
        hm = __builtin_amdgcn_readfirstlane(*(LAS unsigned*)(CT + (L_TM - L_TAG) + 16)); t0 = __builtin_amdgcn_readfirstlane(*(LAS unsigned*)(CT + (L_TM - L_TAG)));
        bar();
      }
      if (w == 0) COUNT(44 + lay * 8, spin);
      if (lay == 1) return;
      FRESH(); A128();
      P1PREP();
      if (w < 4) {
        STPTR();
        int sst = STAT1; PINS(sst);
        bar();
        STAMPW(0, 18);
        P1RUN();
        if (w == 3 && hasP1) STATS(sst);
        STAMPW(0, 19);
      } else {
        CSQLOAD(); CSQSUM(); PINI(csq);
        X1PREP();
        bar();
        P1RUN();
        X1RUN();
        asm volatile(".Lfwd_end:");
      }
    } else {
      {
        const unsigned tm = now();
        const f2 ml0 = *(LAS f2*)mmr, ml1 = *(LAS f2*)(mmr + 64);
        const v4u B0 = lds16(mbr, 0), B1 = lds16(mbr, 2048);
        const int so = __builtin_amdgcn_readfirstlane(mso);
        SB();
        const float M = max32(max16(vmax(ml0.x, ml1.x)));
        const float f0 = __builtin_amdgcn_exp2f(ml0.x - M), f1 = __builtin_amdgcn_exp2f(ml1.x - M);
        const unsigned h0 = pk(f0, f0), h1 = pk(f1, f1);
        f4 o = mfma(pm & h0, B0, Z4); o = mfma(pm & h1, B1, o);
        const float Ls = add32(add16(blo(h0) * ml0.y + blo(h1) * ml1.y));
        if (lane0 == 0) stx(rsW, v4u{cl(asu(M)), cl(asu(Ls)), tm, 0u}, 512 + (w & 1) * 16, so);
        stx2(rsW, pack4(o), mst, so);
        STAMPW(6, 9 + lay * 16);
      }
      FRESH(); A128(); SEL8(); STPTR();
      if (lay == 0) { KARG(); const int o_ = (slo + TKO) * 2; xp = __builtin_amdgcn_raw_buffer_load_b64(mkrs(kp->x, 4 * DM * 2), o_ + pp * DM * 2, 0, 0); b2v = __builtin_amdgcn_raw_buffer_load_b64(mkrs(kp->b2, DM * 2), o_, 0, 0); PINI(xp); PINI(b2v); }
      v4u s8 = sel8;
      int v0 = hq * 512 + (w - 6) * 256 + col * 16;
      lptr xw = L + L_XB + pp * XBS + TKO * 2;
      {
        int so = gat3 + xs + ((HOWN ^ HALF) & l1), so1 = so + 4096, sp = pub4 + x4 + ((H4 ^ HALF) & l1) + 3840;
        f4 xr = unp(xp);
        PINS(so); PINS(so1); PINS(sp); PINI(s8); PINI(v0); PINI(xw); PINI(xr);
        if (lay == 0) {
          const unsigned tg = *(LAS unsigned*)(CT + 0 + 8) & ~3u, dm = __builtin_amdgcn_readfirstlane(*(LAS unsigned*)(CT + (L_TM - L_TAG) + 8));
          if (w == 7 && ((cls >> 20) & 15) == 15) {
            unsigned vote = 0;
            if (!hasF) {
              const int po = lane < 8 ? WS_PROBE + (cls >> 24) * 128 + lane * 16 : 0x40000000;
              u64 ta, tb, tc, td;
              asm volatile("s_waitcnt vmcnt(0)\n\tbuffer_store_dwordx4 %4, %5, %6, 0 offen sc1\n\tbuffer_store_dwordx4 %4, %5, %6, %7 offen sc1\n\t"
                           "s_waitcnt vmcnt(1)\n\ts_memtime %0\n\ts_waitcnt vmcnt(0)\n\ts_memtime %1\n\t"
                           "buffer_store_dwordx4 %4, %5, %6, %7 offen sc1\n\tbuffer_store_dwordx4 %4, %5, %6, 0 offen sc1\n\t"
                           "s_waitcnt vmcnt(1)\n\ts_memtime %2\n\ts_waitcnt vmcnt(0)\n\ts_memtime %3\n\ts_waitcnt lgkmcnt(0)"
                           : "=&s"(ta), "=&s"(tb), "=&s"(tc), "=&s"(td) : "v"(s8), "v"(po), "s"(rsW), "s"(HALF) : "memory");
              vote = 2u | (((unsigned)(tb - ta) > (unsigned)(td - tc) ? 0u : 1u) ^ ((dm >> xc_) & 1u));
            } else __builtin_amdgcn_s_sleep(D_X2 - 12);
            const int g2 = gat2;
            for (int sp2 = 0;; ++sp2) {
              OPAQUE();
              const unsigned t = __builtin_amdgcn_raw_buffer_load_b32(rsW, (col >> 3) * BLK + (col & 7) * X2_MSG + 512 + 16 + 8, g2, AUX_SC1);
              if (!any(t == SENT) || sp2 > SPIN_CAP) break;
            }
            if (lane0 < 8) stx(rsW, v4u{tg | vote, ~tg, now(), 0u}, lane * 2048, WS_HER + (cls >> 24) * 128);
          }
          bar();
          if (w == 6) {
            const int ho = WS_HER + xc_ * 2048;
            v4u hv;
            for (int sp2 = 0;; ++sp2) {
              OPAQUE();
              hv = ldx(rsW, col * 128, ho);
              if (!any((hv.x & ~3u) != tg || hv.y != ~tg) || sp2 > SPIN_CAP) break;
              if (HSLP) __builtin_amdgcn_s_sleep(HSLP);
            }
            hm = NUMA ? dm ^ (__builtin_popcountll(__builtin_amdgcn_ballot_w64((hv.x & 3u) == 3u) & 0xFFFFull) > 4 ? 0xFFu : 0u) : 0u;
            t0 = tmax16(hv.z & 0x7FFFFFFFu);
            if (lane0 == 0) { *(volatile LAS unsigned*)(CT + (L_TM - L_TAG)) = t0; *(volatile LAS unsigned*)(CT + (L_TM - L_TAG) + 16) = hm; *(volatile LAS unsigned*)(CT + (L_TM - L_TAG) + 4) = 1u; }
          } else {
            for (int sp2 = 0; *(volatile LAS unsigned*)(CT + (L_TM - L_TAG) + 4) == 0u && sp2 < (SPIN_CAP << 4); ++sp2) {}
            OPAQUE();
            hm = __builtin_amdgcn_readfirstlane(*(LAS unsigned*)(CT + (L_TM - L_TAG) + 16)); t0 = __builtin_amdgcn_readfirstlane(*(LAS unsigned*)(CT + (L_TM - L_TAG)));
          }
          STAMPW(6, 38);
        } else bar();
        const unsigned T3 = t0 + (lay == 0 ? T3A : T3B);
        LOOKAT(T3);
        f4 y;
        int spin = 0;
        for (;; ++spin) {
          OPAQUE();
          const v4u d0 = ldx(rsW, v0, so), d1 = ldx(rsW, v0 + 2048, so), d2 = ldx(rsW, v0, so1), d3 = ldx(rsW, v0 + 2048, so1);
          SB();
          y = mfma(s8, d0, Z4); y = mfma(s8, d1, y); y = mfma(s8, d2, y); y = mfma(s8, d3, y);
          if (!any(y.x != y.x)) break;
          if (!any(max(max(mx4(d0), mx4(d1)), max(mx4(d2), mx4(d3))) == SENT) || spin > SPIN_CAP) break;
        }
        OPAQUE();
        if (w == 6) { COUNT(43 + lay * 8, spin); COUNT(55, hm); }
        STAMPW(6, 12 + lay * 16);
        xp = pk4(xr + unp(pk4(y)));
        if (lane0 < 32) *(LAS v2u*)xw = xp;
        LATE(T3);
        bar();
        if (w == 6) { if (hasF) STATS(sp); }
        else if (lay == 0 && c == 0 && lane0 == 0) stx2(rsW, *(LAS v2u*)(CT + 0 + 8), 0, WS_EPOCH);
      }
      if (lay == 1 && !isOut) { bar(); return; }
      int so = gat5 + xs + (HOWN ^ HALF), so1 = so + 4096, so2 = so + BLK;
      int oo = hq < 2 ? (pp * DM + slo + TKO) * 2 : 0x40000000;
      f4 xr = unp(xp), bb = unp(b2v);
      KARG();
      rsrc_t rout = mkrs(kp->out, 4 * DM * 2);
      asm volatile("" : "+s"(rout));
      P1PREP();
      PINS(so); PINS(so1); PINS(so2); PINI(xr); PINI(bb); PINI(oo);
      if (lay == 0) { CSQLOAD(); CSQSUM(); PINI(csq); mso = pub2 + SM_STRIDE + (HOWN ^ HALF); }
      X1PREP();
      bar();
      const unsigned T5 = t0 + (lay == 0 ? T5A : T5B);
      LOOKAT(T5);
      f4 y;
      int spin = 0;
      for (;; ++spin) {
        OPAQUE();
        const v4u d0 = ldx(rsW, v0, so), d1 = ldx(rsW, v0 + 2048, so), d2 = ldx(rsW, v0, so1), d3 = ldx(rsW, v0 + 2048, so1), d4 = ldx(rsW, v0, so2), d5 = ldx(rsW, v0 + 2048, so2);
        SB();
        y = mfma(s8, d0, Z4); y = mfma(s8, d1, y); y = mfma(s8, d2, y); y = mfma(s8, d3, y); y = mfma(s8, d4, y); y = mfma(s8, d5, y);
        if (!any(y.x != y.x)) break;
        if (!any(max(max(max(mx4(d0), mx4(d1)), max(mx4(d2), mx4(d3))), max(mx4(d4), mx4(d5))) == SENT) || spin > SPIN_CAP) break;
      }
      OPAQUE();
      if (w == 6) COUNT(45 + lay * 8, spin);
      STAMPW(6, 17 + lay * 16);
      xp = pk4(xr + unp(pk4(unp(pk4(y)) + bb)));
      if (lay == 1) {
        __builtin_amdgcn_raw_buffer_store_b64(xp, rout, oo, 0, OUT_AUX);
        STAMPW(6, 34);
        if (w == 6) { COUNT(61, __builtin_amdgcn_s_memtime()); }
        return;
      }
      if (lane0 < 32) *(LAS v2u*)xw = xp;
      LATE(T5);
      bar();
      P1RUN();
      X1RUN();
    }
#pragma unroll
    for (int T = 0; T < 4; ++T) S[T] = Z4;
    O = Z4;
    PIN4(S); PINI(O);
    bar();
    STAMPW(0, 23);
  }
}

extern "C" void kernel_launch(void* const* d_in, const int* in_sizes, int n_in, void* d_out, int out_size, void* d_ws, size_t ws_size, hipStream_t stream) {
  Params p;
  p.x = (const u16*)d_in[0]; p.kc = (const u16*)d_in[1]; p.vc = (const u16*)d_in[2]; p.wq = (const u16*)d_in[3]; p.wo = (const u16*)d_in[4];
  p.w1 = (const u16*)d_in[6]; p.b1 = (const u16*)d_in[7]; p.w2 = (const u16*)d_in[8]; p.b2 = (const u16*)d_in[9];
  p.out = (u16*)d_out; p.ws = (unsigned char*)d_ws;
  (void)hipFuncSetAttribute((const void*)fused_fwd, hipFuncAttributeMaxDynamicSharedMemorySize, L_BASE + L_BYTES);
  hipLaunchKernelGGL(fused_fwd, dim3(256), dim3(512), L_BASE + L_BYTES, stream, p);
}
```

```cpp
#include <hip/hip_runtime.h>
#include <stdint.h>

#define STAMPS 0

typedef unsigned short u16;
typedef unsigned long long u64;
typedef unsigned v4u __attribute__((ext_vector_type(4)));
typedef unsigned v8u __attribute__((ext_vector_type(8)));
typedef unsigned v2u __attribute__((ext_vector_type(2)));
typedef float f4 __attribute__((ext_vector_type(4)));
typedef float f2 __attribute__((ext_vector_type(2)));
typedef __bf16 bf2 __attribute__((ext_vector_type(2)));
typedef __bf16 bf8 __attribute__((ext_vector_type(8)));
typedef __bf16 bf16v __attribute__((ext_vector_type(16)));
typedef short s4v __attribute__((ext_vector_type(4)));
#define LAS __attribute__((address_space(3)))
#define DI __device__ __forceinline__
typedef __amdgpu_buffer_rsrc_t rsrc_t;

constexpr int DM = 576, SK = 8192, WIN = 2048, DH = 128, FF = 4608, QN = 4096;
constexpr unsigned SENT = 0xFFFFFFFFu;
constexpr int AUX_NT = 2, AUX_SC1 = 16;

constexpr int PG = 2 << 20, BLK = 16384, HALF = 8192;
constexpr int WS_EPOCH = 0, WS_FLAGS = 4096, WS_MARK = 8192, WS_HER = 40960, WS_PROBE = 49152, X1A_OFF = 65536, X1_MSG = 5376;
constexpr int X2_MSG = 640, X2_OFF = PG, X3_OFF = PG + 32 * BLK, X5_OFF = PG + 41 * BLK, SM_STRIDE = 60 * BLK;
constexpr int X4_OFF = 2 * PG;
constexpr int X1_OFF = 4 * PG;
constexpr int WS_USED = 5 * PG;
static_assert(X1A_OFF + 144 * X1_MSG <= PG && 2 * SM_STRIDE <= PG && 120 * BLK <= PG, "pages");
constexpr int WS_STAMP = 16 << 20;

constexpr int L_BASE = 64;
constexpr int L_WQ = 0;
constexpr int L_WO = 32768;
constexpr int L_W2 = 65536;
constexpr int L_W1 = 90112;
constexpr int L_PA = 114688;
constexpr int L_AO = L_PA + 8 * 2176;
constexpr int L_OB = L_AO + 4096;
constexpr int L_HB = L_OB + 4 * 528;
constexpr int L_XB = L_HB + 4 * 400;
constexpr int L_QP = L_XB + 6 * 144;
constexpr int L_ML = L_QP + 512;
constexpr int L_LN0 = L_ML + 128;
constexpr int L_RDY = L_LN0 + 32;
constexpr int L_TAG = L_LN0 + 32;
constexpr int L_TM = L_TAG + 16;
constexpr int L_TB = L_TM + 32;
constexpr int L_BYTES = L_TB + 96;
constexpr int XBS = 144, OBS = 528, HBS = 400;
static_assert(L_BYTES <= 160 * 1024 && L_OB % 16 == 0 && L_HB % 16 == 0 && L_XB % 16 == 0 && L_QP % 16 == 0 && L_ML % 16 == 0, "lds");

#define D_X1 12
#define D_X2 22
#define D_X3 20
#define D_X4 10
#define D_X5 18
#define I3A 95
#define I4A 115
#define I5A 160
#define I1 105
#define I2 160
#define I3 92
#define I4 92
#define I5 100
constexpr unsigned T3A = I3A, T4A = T3A + I4A, T5A = T4A + I5A, T1B = T5A + I1, T2B = T1B + I2, T3B = T2B + I3, T4B = T3B + I4, T5B = T4B + I5;
#define SCHED 1
#define NUMA 1
#define FPOS 19
#define XPOS 3
#define CPOS 8
#define SPOS 14
#define WND 10
#define TP0 2
#define TP1 5
#define TP2 8
#define TP3 11
#define D_W 11
#define PREFI 1
#define OUT_AUX 0
#define SNT 2
#define REWARM 1
constexpr int SPIN_CAP = 1 << 18;

struct Params {
  const u16 *x, *kc, *vc, *wq, *wo, *w1, *b1, *w2, *b2;
  u16* out;
  unsigned char* ws;
};

DI f4 mfma(v4u a, v4u b, f4 c) { return __builtin_amdgcn_mfma_f32_16x16x32_bf16(__builtin_bit_cast(bf8, a), __builtin_bit_cast(bf8, b), c, 0, 0, 0); }
DI f4 smf(v4u a, v8u b, f4 c, int idx) { return __builtin_amdgcn_smfmac_f32_16x16x64_bf16(__builtin_bit_cast(bf8, a), __builtin_bit_cast(bf16v, b), c, idx, 0, 0); }
DI v8u cat(v4u a, v4u b) { return __builtin_shufflevector(a, b, 0, 1, 2, 3, 4, 5, 6, 7); }
DI float asf(unsigned u) { return __builtin_bit_cast(float, u); }
DI unsigned asu(float f) { return __builtin_bit_cast(unsigned, f); }
template <int C> DI unsigned dppu(unsigned x) { return (unsigned)__builtin_amdgcn_mov_dpp((int)x, C, 0xf, 0xf, true); }
template <int C> DI float dppf(float x) { return asf(dppu<C>(asu(x))); }
constexpr int XOR1 = 0xB1, XOR2 = 0x4E, ROR4 = 0x124, ROR8 = 0x128;
DI float add16(float x) { auto s = __builtin_amdgcn_permlane16_swap(asu(x), asu(x), false, false); return asf(s[0]) + asf(s[1]); }
DI float add32(float x) { auto s = __builtin_amdgcn_permlane32_swap(asu(x), asu(x), false, false); return asf(s[0]) + asf(s[1]); }
DI float vmax(float a, float b) { float r; asm("v_max_f32 %0, %1, %2" : "=v"(r) : "v"(a), "v"(b)); return r; }
DI float max16(float x) { auto s = __builtin_amdgcn_permlane16_swap(asu(x), asu(x), false, false); return vmax(asf(s[0]), asf(s[1])); }
DI float max32(float x) { auto s = __builtin_amdgcn_permlane32_swap(asu(x), asu(x), false, false); return vmax(asf(s[0]), asf(s[1])); }
DI unsigned pk(float lo, float hi) { bf2 v = {(__bf16)lo, (__bf16)hi}; return __builtin_bit_cast(unsigned, v); }
DI float rbf(float x) { return (float)(__bf16)x; }
DI float blo(unsigned u) { return asf(u << 16); }
DI float bhi(unsigned u) { return asf(u & 0xffff0000u); }
DI unsigned cl(unsigned u) { return min(u, SENT - 1); }
DI v4u split4(f4 d) {
  const unsigned h0 = pk(d.x, d.y), h1 = pk(d.z, d.w);
  return v4u{cl(h0), cl(h1), cl(pk(d.x - blo(h0), d.y - bhi(h0))), cl(pk(d.z - blo(h1), d.w - bhi(h1)))};
}
DI v2u pack4(f4 d) { return v2u{cl(pk(d.x, d.y)), cl(pk(d.z, d.w))}; }
DI unsigned mx4(v4u v) { return max(max(v.x, v.y), max(v.z, v.w)); }
DI v4u ldx(rsrc_t r, int voff, int soff) { return __builtin_amdgcn_raw_buffer_load_b128(r, voff, soff, AUX_SC1); }
DI v2u ldx2(rsrc_t r, int voff, int soff) { return __builtin_amdgcn_raw_buffer_load_b64(r, voff, soff, AUX_SC1); }
DI void stx(rsrc_t r, v4u v, int voff, int soff) { __builtin_amdgcn_raw_buffer_store_b128(v, r, voff, soff, AUX_SC1); }
DI void stx2(rsrc_t r, v2u v, int voff, int soff) { __builtin_amdgcn_raw_buffer_store_b64(v, r, voff, soff, AUX_SC1); }
DI v4u ldn(rsrc_t r, int voff, int soff) { return __builtin_amdgcn_raw_buffer_load_b128(r, voff, soff, AUX_SC1 | SNT); }
DI void stn(rsrc_t r, v4u v, int voff, int soff) { __builtin_amdgcn_raw_buffer_store_b128(v, r, voff, soff, AUX_SC1 | SNT); }
DI void bar() { asm volatile("s_waitcnt lgkmcnt(0)\n\ts_barrier" ::: "memory"); }
DI void drain() { asm volatile("s_waitcnt vmcnt(0)" ::: "memory"); }
DI rsrc_t mkrs(const void* p, unsigned bytes) { return __builtin_amdgcn_make_buffer_rsrc((void*)p, (short)0, (int)bytes, 0x00020000); }
DI void dma(rsrc_t r, int voff, int soff, unsigned ldsaddr) {
  unsigned keep;
  asm volatile("s_nop 4\n\ts_mov_b32 %0, m0\n\ts_mov_b32 m0, %1\n\ts_nop 0\n\tbuffer_load_dwordx4 %2, %3, %4 offen nt lds\n\ts_mov_b32 m0, %0"
               : "=&s"(keep) : "s"(ldsaddr), "v"(voff), "s"(r), "s"(soff) : "memory");
}
typedef LAS unsigned char* lptr;
DI v4u lds16(lptr L, int off) { return *(LAS v4u*)(L + off); }
#define SB() __builtin_amdgcn_sched_barrier(0)
DI float dot2(unsigned a, unsigned b, float c) { return __builtin_amdgcn_fdot2_f32_bf16(__builtin_bit_cast(bf2, a), __builtin_bit_cast(bf2, b), c, false); }
DI v4u trfrag(LAS unsigned char* L, int off, int step) {
  s4v a = __builtin_amdgcn_ds_read_tr16_b64_v4i16((LAS s4v*)(L + off));
  s4v b = __builtin_amdgcn_ds_read_tr16_b64_v4i16((LAS s4v*)(L + off + step));
  v2u x = __builtin_bit_cast(v2u, a), y = __builtin_bit_cast(v2u, b);
  return v4u{x.x, x.y, y.x, y.y};
}
DI bool any(bool b) { return __builtin_amdgcn_ballot_w64(b) != 0; }
#define OPAQUE() asm volatile("" ::: "memory")
#define Z4 f4{0.f, 0.f, 0.f, 0.f}
#define FRESH() int lane = lane0; asm volatile("" : "+v"(lane)); const int col = lane & 15, hq = lane >> 4, q4 = col >> 2, pp = lane & 3; (void)col; (void)hq; (void)q4; (void)pp
#define SEL8() const unsigned one = (lane & 1) ? 0x3F800000u : 0x00003F80u; \
  const v4u sel8 = {((col & 7) >> 1) == 0 ? one : 0u, ((col & 7) >> 1) == 1 ? one : 0u, ((col & 7) >> 1) == 2 ? one : 0u, ((col & 7) >> 1) == 3 ? one : 0u}
#define A128() const int z128 = ((q4 >> 1) & 1) | ((hq & 1) << 1), a128 = hq * 1024 + q4 * 128 + pp * 8
#define STAMP(i) do {} while (0)
#define STAMPW(ww, i) do {} while (0)
#define COUNT(i, v) do {} while (0)

#define PIN4(a) asm volatile("" : "+v"(a[0]), "+v"(a[1]), "+v"(a[2]), "+v"(a[3]))
#define PINI(x) asm volatile("" : "+v"(x))
#define PINS(x) asm volatile("" : "+s"(x))
#define TKO (32 * (w & 1) + 8 * q4 + 4 * (hq & 1))
#define HFO (32 * w + 16 * (hq >> 1) + 4 * q4 + 2 * (hq & 1))
#define KLOAD(i) K[i] = __builtin_amdgcn_raw_buffer_load_b128(rsK, kvo, __builtin_amdgcn_readfirstlane((((i) + rot) & 15) * 1024), AUX_NT)
#define VLOAD(i) V[i] = __builtin_amdgcn_raw_buffer_load_b128(rsV, vvo, __builtin_amdgcn_readfirstlane((((i) + rot) & 15) * 1024), AUX_NT)
#define QPW(ww) (L_QP + (q4 & 1) * 256 + (((ww) >> 1) * 8 + 2 * ((col >> 1) & 1) + ((ww) & 1)) * 16 + (2 * (col >> 3) + (col & 1)) * 4)
#define TT(i) do { if (STAMPS == 1 || (STAMPS && (i) == 0)) { SB(); tt[i] = __builtin_amdgcn_s_memrealtime(); asm volatile("s_waitcnt lgkmcnt(0)" : "+s"(tt[i])); SB(); } } while (0)
DI unsigned now() { return (unsigned)__builtin_amdgcn_s_memrealtime() & 0x7FFFFFFFu; }
DI void until(unsigned t) { for (int i = 0; i < 4096 && ((t - now()) & 0x7FFFFFFFu) - 1u < 0x3FFFFFFFu; ++i) {} }
DI unsigned tmax16(unsigned t) {
  const unsigned ref = __builtin_amdgcn_readfirstlane(t);
  int d = (int)((t - ref) << 1);
  d = max(d, (int)dppu<XOR1>(d)); d = max(d, (int)dppu<XOR2>(d)); d = max(d, (int)dppu<ROR4>(d)); d = max(d, (int)dppu<ROR8>(d));
  return (ref + (unsigned)(__builtin_amdgcn_readfirstlane(d) >> 1)) & 0x7FFFFFFFu;
}
#define RTTN 50
#define LATE(T) do { if (spin > 0 && lane0 == 0) __hip_atomic_fetch_max((LAS int*)(CT + (L_TM - L_TAG) + 12), (int)(((now() - (t0 + (T) + RTTN)) << 1)) >> 1, __ATOMIC_RELAXED, __HIP_MEMORY_SCOPE_WORKGROUP); } while (0)
#define LOOKAT(T) until(t0 + (T) + (unsigned)__builtin_amdgcn_readfirstlane(*(volatile LAS int*)(CT + (L_TM - L_TAG) + 12)))
#define RS(k) __builtin_amdgcn_readfirstlane(*(LAS int*)(CT + (L_TB - L_TAG) + 4 * (k)))
DI u64 next_tag(v2u e) { return (((u64)e.y << 32) | e.x) * 6364136223846793005ull + 1442695040888963407ull; }
DI void dma128(int ln, rsrc_t r, int rowbytes, int soff, unsigned lds, int i) {
  dma(r, (ln >> 3) * rowbytes + ((((ln & 7) >> 1) ^ (((ln >> 4) & 1) | ((i & 1) << 1))) * 32) + (ln & 1) * 16, soff + 8 * i * rowbytes, lds + i * 1024);
}
DI f4 unp(v2u v) { return f4{blo(v.x), bhi(v.x), blo(v.y), bhi(v.y)}; }
DI v2u pk4(f4 v) { return v2u{pk(v.x, v.y), pk(v.z, v.w)}; }

__global__ void __launch_bounds__(512, 2) fused_fwd(Params p) {
  const lptr L = (lptr)(size_t)L_BASE;
  const int tid = threadIdx.x, lane0 = tid & 63, w = __builtin_amdgcn_readfirstlane(tid >> 6);
  const int c = blockIdx.x, xc = c & 7, j = c >> 3, grp = j >> 4, u = j & 15, g = 2 * xc + grp;
  u64 tt[8] = {};
  TT(0);
  const int ab = u >> 2, as = u & 3;
  const bool hasP1 = u <= 8, hasP3 = u >= 7;
  constexpr unsigned ldsb = L_BASE;
  const rsrc_t rsW = mkrs((const void*)(((size_t)p.ws + PG - 1) & ~(size_t)(PG - 1)), WS_USED);
  constexpr float CS = 0.08838834764831845f * 1.4426950408889634f;
  v8u K8[8], V8[8];
  bool hasF, isOut; unsigned b1v;
  lptr qa, pw, pr, aow, mlw, mbr, mmr; int qidx, pidx, mst; v4u pm;
  {
    const int lane = lane0, col = lane & 15, hq = lane >> 4, q4 = col >> 2, pp = lane & 3;
    const size_t kvbase = ((size_t)(ab * 16 + g) * SK + (SK - WIN) + as * 512 + w * 64) * DH;
    const rsrc_t rsK = mkrs(p.kc + kvbase, 16384), rsV = mkrs(p.vc + kvbase, 16384);
    const int rot = __builtin_amdgcn_readfirstlane((w >> 1) + 4 * (j & 3) + 2 * xc);
    const int kvo = q4 * 256 + (4 * hq + pp) * 16, vvo = lane * 16;
    const int r1 = u < 8 ? xc : 8, gq1 = u < 8 ? 8 * grp + u : g, n3 = (u + 9) & 15;
    v4u K[16], V[16], csw = {0, 0, 0, 0};
    int pub4, pub5, gatc;
    u64 tag = 0;
    if (w >= 4) {
      if (w == 4 && hasP1) {
        const rsrc_t rx = mkrs(p.x, 4 * DM * 2);
        const int xo_ = (hq * DM + col * 8) * 2;
        v4u xv[5];
#pragma unroll
        for (int k = 0; k < 4; ++k) xv[k] = __builtin_amdgcn_raw_buffer_load_b128(rx, xo_ + 256 * k, 0, 0);
        xv[4] = __builtin_amdgcn_raw_buffer_load_b128(rx, col < 8 ? xo_ + 1024 : 0x40000000, 0, 0);
        if (lane == 0) *(volatile LAS unsigned*)(L + L_RDY) = 0u;
        bar();
        SB();
        const int ks = r1 >> 1;
        const v4u xs = ks == 0 ? xv[0] : (ks == 1 ? xv[1] : (ks == 2 ? xv[2] : (ks == 3 ? xv[3] : xv[4])));
        if ((col >> 3) == (r1 & 1)) *(LAS v4u*)(L + L_XB + hq * XBS + (col & 7) * 16) = xs;
        float s1 = 0.f, s2 = 0.f, s3 = 0.f, s4 = 0.f;
#pragma unroll
        for (int k = 0; k < 5; ++k) {
          s1 = dot2(xv[k][0], 0x3F803F80u, s1); s2 = dot2(xv[k][0], xv[k][0], s2); s3 = dot2(xv[k][1], 0x3F803F80u, s3); s4 = dot2(xv[k][1], xv[k][1], s4);
          s1 = dot2(xv[k][2], 0x3F803F80u, s1); s2 = dot2(xv[k][2], xv[k][2], s2); s3 = dot2(xv[k][3], 0x3F803F80u, s3); s4 = dot2(xv[k][3], xv[k][3], s4);
        }
        s1 += s3; s2 += s4;
        s1 += dppf<XOR1>(s1); s2 += dppf<XOR1>(s2); s1 += dppf<XOR2>(s1); s2 += dppf<XOR2>(s2);
        s1 += dppf<ROR4>(s1); s2 += dppf<ROR4>(s2); s1 += dppf<ROR8>(s1); s2 += dppf<ROR8>(s2);
        const float mu = s1 * (1.f / DM), rs = __builtin_amdgcn_rsqf(fmaxf(s2 * (1.f / DM) - mu * mu, 0.f) + 1e-5f) * CS;
        if (col == 0) *(LAS f2*)(L + L_LN0 + hq * 8) = f2{mu, rs};
        OPAQUE();
        if (lane == 0) *(volatile LAS unsigned*)(L + L_RDY) = 1u;
        OPAQUE(); SB();
#pragma unroll
        for (int i = 0; i < 4; ++i) KLOAD(i);
      } else {
        KLOAD(0); KLOAD(1); KLOAD(2);
        if (hasP1) bar();
        KLOAD(3);
      }
    } else {
      if (w == 3) {
        const v4u SENT4 = {SENT, SENT, SENT, SENT};
        if (lane < 40) stx(rsW, SENT4, lane * 16, X2_OFF + (2 * g + (ab >> 1)) * BLK + ((ab & 1) * 4 + as) * X2_MSG);
        if (hasP3 && lane < 32) stx(rsW, SENT4, lane * 16, X3_OFF + n3 * BLK + g * 512);
        if (u == 15 && lane == 0) stx(rsW, SENT4, 0, WS_HER + g * 128);
      }
      const unsigned nearb = ((unsigned)__builtin_amdgcn_s_getreg(20 | (3 << 11)) >> 2) & 1u;
      v2u ep;
      asm volatile("buffer_load_dwordx2 %0, off, %1, 0 sc1" : "=v"(ep) : "s"(rsW) : "memory");
      {
        const rsrc_t rq = mkrs(hasP1 ? p.wq : p.wo, DM * QN * 2);
        const int rb = hasP1 ? QN * 2 : DM * 2, so = hasP1 ? 64 * r1 * (QN * 2) + gq1 * 512 + w * 128 : (256 * g + 64 * w) * (DM * 2) + n3 * 128;
#pragma unroll
        for (int i = 0; i < 8; ++i) dma128(lane, rq, rb, so, ldsb + (hasP1 ? L_WQ : L_WO) + w * 8192, i);
        if (hasP1) bar();
      }
      OPAQUE(); SB();
      v4u pf = {0, 0, 0, 0};
      if (PREFI) {
        u64 pc; unsigned len;
        asm volatile("s_getpc_b64 %0\n.Lfwd_pc:\n\ts_mov_b32 %1, .Lfwd_end-.Lfwd_pc" : "=s"(pc), "=s"(len));
        pf = __builtin_amdgcn_raw_buffer_load_b128(mkrs((const void*)(pc & ~15ull), len & ~15u), w == 2 ? j * 1024 + lane * 16 : 0x40000000, 0, 0);
      }
      OPAQUE(); SB();
      TT(1);
#pragma unroll
      for (int i = 0; i < 4; ++i) KLOAD(i);
      OPAQUE(); SB();
      asm volatile("s_waitcnt vmcnt(%1)" : "+v"(ep) : "i"(12 + PREFI) : "memory");
      tag = next_tag(ep);
      if (w == 3 && lane == 0) { const u64 f = tag + (u64)c * 0x9E3779B97F4A7C15ull; stx2(rsW, v2u{((unsigned)f & ~1u) | nearb, (unsigned)(f >> 32)}, 0, WS_FLAGS + c * 8); }
      if (hasP1) {
        A128();
        asm volatile("s_waitcnt vmcnt(%0)" ::"i"(4 + PREFI) : "memory");
        TT(2);
        v4u A[8];
#pragma unroll
        for (int t = 0; t < 4; ++t) { const int base = L_WQ + w * 8192 + a128 + (t ^ z128) * 32; A[2 * t] = trfrag(L, base, 512); A[2 * t + 1] = trfrag(L, base + 4096, 512); }
        for (int spin = 0; *(volatile LAS unsigned*)(L + L_RDY) == 0u && spin < (SPIN_CAP << 4); ++spin) __builtin_amdgcn_s_sleep(1);
        OPAQUE();
        TT(3);
        const v4u xf0 = lds16(L, L_XB + pp * XBS + hq * 16), xf1 = lds16(L, L_XB + pp * XBS + hq * 16 + 64);
        const f2 ln = *(LAS f2*)(L + L_LN0 + pp * 8);
        SB();
        f4 d = Z4, e = Z4;
#pragma unroll
        for (int t = 0; t < 4; ++t) {
          const unsigned m = q4 == t ? ~0u : 0u, o = m & 0x3F803F80u;
          d = mfma(A[2 * t], xf0 & m, d); e = mfma(A[2 * t], v4u{o, o, o, o}, e);
          d = mfma(A[2 * t + 1], xf1 & m, d); e = mfma(A[2 * t + 1], v4u{o, o, o, o}, e);
        }
        d = (d - ln.x * e) * ln.y;
        stn(rsW, v4u{pk(d.x, d.y), (unsigned)tag & 0xBFFFBFFFu, pk(d.z, d.w), (unsigned)(tag >> 32) & 0xBFFFBFFFu},
            pp * 1024 + (16 * w + 4 * q4 + hq) * 16, X1A_OFF + (gq1 * 9 + r1) * X1_MSG);
        csw = split4(e);
      }
      TT(4);
      asm volatile("" ::"v"(pf));
    }
    OPAQUE(); SB();
    v4u fa;
#define WIN() do { if (WND) asm volatile("s_waitcnt vmcnt(%0)" ::"i"(WND - 1) : "memory"); } while (0)
#pragma unroll
    for (int i = 4; i < 16; ++i) {
      if (i >= WND) WIN();
      KLOAD(i); SB();
      if (i == FPOS) { if (w < 4) fa = ldn(rsW, lane * 16 + (w & 1) * 1024, WS_FLAGS); OPAQUE(); SB(); }
    }
    {
      const int fi0 = 14 * grp + (u <= 6 ? u : u - 2);
      hasF = (u != 7) && (u != 8) && (fi0 < 27);
      const int fi = fi0 < 27 ? fi0 : 0;
      const int n4 = fi < 24 ? xc : 8, f4i = fi < 24 ? fi : 3 * xc + fi - 24, f5 = 3 * xc + fi / 9, n5 = fi % 9;
      const int sl = u == 8 ? 8 : (hasF ? n4 : xc);
      isOut = grp == 0 && (u == 0 || (u == 8 && xc == 0));
      int pub1, pub2, pub3, gat1, gat2, gat3, gat4, gat5, mko, cls, slo, wofs, w1ofs, w2ofs, b1ofs;
      pub1 = X1_OFF + 5 * gq1 * BLK + (r1 < 8 ? r1 * 1024 : 4 * BLK); pub2 = X2_OFF + (2 * g + (ab >> 1)) * BLK + ((ab & 1) * 4 + as) * X2_MSG;
      pub3 = X3_OFF + n3 * BLK + g * 512; pub4 = X4_OFF + (5 * f4i + (n4 >> 1)) * BLK + (n4 & 1) * 4096; pub5 = X5_OFF + (2 * n5 + (f5 >> 4)) * BLK + (f5 & 15) * 512;
      gat1 = X1_OFF + 5 * g * BLK; gat2 = X2_OFF + 2 * g * BLK; gat3 = X3_OFF + sl * BLK;
      gat4 = X4_OFF + 5 * f5 * BLK; gat5 = X5_OFF + 2 * sl * BLK; gatc = X1A_OFF + g * 9 * X1_MSG; mko = WS_MARK + (n5 * 24 + f5) * 128;
      cls = (gq1 >> 1) | (n3 << 4) | ((f4i / 3) << 8) | (n5 << 12) | (r1 << 16) | (u << 20) | (g << 24);
      slo = 64 * sl; wofs = 256 * g * (DM * 2) + n3 * 128; w1ofs = 64 * n4 * (FF * 2) + f4i * 384; w2ofs = 192 * f5 * (DM * 2) + n5 * 128; b1ofs = 192 * f5;
      b1v = __builtin_amdgcn_raw_buffer_load_b32(mkrs(p.b1, FF * 2), w < 6 ? (b1ofs + HFO) * 2 : 0x40000000, 0, AUX_NT);
      if (w == 6 && lane == 0) {
        LAS int* tbl = (LAS int*)(L + L_TB);
        tbl[0] = pub1; tbl[1] = pub2; tbl[2] = pub3; tbl[3] = pub4; tbl[4] = pub5; tbl[5] = gat1; tbl[6] = gat2; tbl[7] = gat3; tbl[8] = gat4; tbl[9] = gat5; tbl[10] = gatc; tbl[11] = mko; tbl[12] = cls; tbl[13] = slo; tbl[14] = wofs; tbl[15] = w1ofs; tbl[16] = w2ofs; tbl[17] = b1ofs; tbl[18] = xc; tbl[19] = ab;
      }
      if (hasF) {
        const rsrc_t ra = mkrs(p.w1, DM * FF * 2), rb = mkrs(p.w2, FF * DM * 2);
        const int vo = (lane >> 2) * (FF * 2) + ((lane & 3) ^ (2 * (lane >> 5))) * 16;
#pragma unroll
        for (int i = 0; i < 3; ++i) {
          const int k = 3 * w + i;
          WIN(); dma(ra, vo, w1ofs + 16 * (k & 3) * (FF * 2) + (k >> 2) * 64, ldsb + L_W1 + k * 1024);
          WIN(); dma(rb, (lane >> 3) * (DM * 2) + ((((lane & 7) >> 1) ^ (((lane >> 4) & 1) | ((k & 1) << 1))) * 32) + (lane & 1) * 16, w2ofs + 8 * k * (DM * 2), ldsb + L_W2 + k * 1024);
        }
      } else if (hasP1 && hasP3) {
        const rsrc_t ro = mkrs(p.wo, QN * DM * 2);
#pragma unroll
        for (int i = 0; i < 4; ++i) { WIN(); dma128(lane, ro, DM * 2, wofs + 32 * w * (DM * 2), ldsb + L_WO + w * 4096, i); }
      }
      qa = L + L_QP + (pp * 4 + hq) * 16;
      qidx = (lane & 1) ? 0xEEEE : 0x4444; pidx = pp * 0x1111;
      pw = L + L_PA + w * 2176 + (2 * hq + (pp >> 1)) * 272 + (2 * (pp & 1) + (q4 >> 1)) * 64 + (q4 & 1) * 8;
      pr = L + L_PA + w * 2176 + (hq * 4 + (col >> 3) * 2 + (q4 & 1)) * 16;
      aow = L + L_AO + w * 512 + ((lane >> 5) * 128 + 8 * col + 4 * (hq & 1)) * 2;
      mlw = L + L_ML + (w * 2 + (lane >> 5)) * 8;
      mbr = L + L_AO + (w & 1) * 256 + hq * 512 + col * 16; mmr = L + L_ML + (w & 1) * 8 + hq * 16;
      mst = hq < 2 ? (w & 1) * 256 + col * 16 + hq * 8 : 0x40000000;
      const unsigned hm = (lane & 1) ? 0xFFFF0000u : 0xFFFFu;
#pragma unroll
      for (int k = 0; k < 4; ++k) pm[k] = ((col & 7) >> 1) == k ? hm : 0u;
      PINI(qa); PINI(pw); PINI(pr); PINI(aow); PINI(mlw); PINI(mbr); PINI(mmr); PINI(qidx); PINI(pidx); PINI(mst); PINI(pm);
      const v4u z = {0, 0, 0, 0};
      const lptr pa = L + L_PA + w * 2176 + lane * 16;
      *(LAS v4u*)pa = z; *(LAS v4u*)(pa + 1024) = z; if (lane < 8) *(LAS v4u*)(pa + 2048) = z;
      if (w == 7 && lane == 0) { *(LAS unsigned*)(L + L_TM + 4) = 0u; *(LAS unsigned*)(L + L_TM + 12) = 0u; }
      if (w == 7 && lane < 36) { *(LAS unsigned*)(L + L_XB + 4 * XBS + lane * 4) = 0x3F803F80u; *(LAS unsigned*)(L + L_XB + 5 * XBS + lane * 4) = 0u; }
    }
    SB();
    auto transp = [&](int T) {
      const bool o1 = lane & 1, o2 = lane & 2;
#pragma unroll
      for (int d = 0; d < 4; ++d) {
        const unsigned m0 = K[4 * T][d], m1 = K[4 * T + 1][d], m2 = K[4 * T + 2][d], m3 = K[4 * T + 3][d];
        const unsigned s0 = dppu<XOR1>(m0), s1 = dppu<XOR1>(m1), s2 = dppu<XOR1>(m2), s3 = dppu<XOR1>(m3);
        const unsigned n0 = o1 ? s1 : m0, n1 = o1 ? m1 : s0, n2 = o1 ? s3 : m2, n3_ = o1 ? m3 : s2;
        const unsigned t0 = dppu<XOR2>(n0), t1 = dppu<XOR2>(n1), t2 = dppu<XOR2>(n2), t3 = dppu<XOR2>(n3_);
        K8[2 * T][d] = o2 ? t2 : n0; K8[2 * T + 1][d] = o2 ? n2 : t0;
        K8[2 * T][4 + d] = o2 ? t3 : n1; K8[2 * T + 1][4 + d] = o2 ? n3_ : t1;
      }
      asm volatile("" : "+v"(K8[2 * T]), "+v"(K8[2 * T + 1]));
    };
    int x1a = hq * X1_MSG + (16 * (w & 3) + col) * 16 + ab * 1024, x1b = x1a + 4 * X1_MSG, x1c = x1a + (8 - hq) * X1_MSG;
    int ta = w >= 4 ? ((hasF && lane < (w == 7 ? 56 : 64)) ? pub4 + (w - 4) * 1024 + lane * 16 : 0x40000000)
                    : ((hasP1 && pp == 0) ? X1A_OFF + (gq1 * 9 + r1) * X1_MSG + 4096 + (16 * w + 4 * q4 + hq) * 16 : 0x40000000);
    int tb = (hasF && lane < 32) ? pub5 + (w & 1) * HALF + lane * 16 : 0x40000000;
    if (w >= 4) csw = v4u{SENT, SENT, SENT, SENT};
    const unsigned one = (lane & 1) ? 0x3F800000u : 0x00003F80u;
    v4u selT = {(col & 2) ? 0u : one, 0u, (col & 2) ? one : 0u, 0u}, selT0 = hq == 0 ? selT : v4u{0, 0, 0, 0};
    unsigned tgx = (unsigned)tag & 0xBFFFBFFFu, tgy = (unsigned)(tag >> 32) & 0xBFFFBFFFu;
    lptr qw = L + QPW(w & 3);
    PINI(x1a); PINI(x1b); PINI(x1c); PINI(ta); PINI(tb); PINI(csw); PINI(selT); PINI(selT0); PINI(tgx); PINI(tgy); PINI(qw);
    v4u g0, g1, g2;
#pragma unroll
    for (int i = 0; i < 16; ++i) {
      WIN(); VLOAD(i); SB();
      if (i == FPOS - 16) { if (w < 4) fa = ldn(rsW, lane * 16 + (w & 1) * 1024, WS_FLAGS); OPAQUE(); SB(); }
      if (i == XPOS) {
        if (w < 4) { g0 = ldn(rsW, x1a, gatc); g1 = ldn(rsW, x1b, gatc); g2 = ldn(rsW, x1c, gatc); }
        OPAQUE(); SB();
      }
      if (i == CPOS) {
        stn(rsW, csw, ta, 0);
        if (w == 4 || w == 5) stn(rsW, csw, tb, 0);
        OPAQUE(); SB();
      }
      if (i == SPOS) {
        PINI(fa);
        if (w < 4) {
          const u64 e0 = tag + (u64)(lane * 2 + (w & 1) * 128) * 0x9E3779B97F4A7C15ull, e1 = e0 + 0x9E3779B97F4A7C15ull;
          int spin = 0;
#pragma nounroll
          for (; spin < SPIN_CAP; ++spin) {
            if (!any(((fa.x ^ (unsigned)e0) > 1u) || fa.y != (unsigned)(e0 >> 32) || ((fa.z ^ (unsigned)e1) > 1u) || fa.w != (unsigned)(e1 >> 32))) break;
            OPAQUE();
            fa = ldn(rsW, lane * 16 + (w & 1) * 1024, WS_FLAGS);
            PINI(fa);
          }
          if (w == 0) COUNT(40, spin);
          if (w == 0) {
            const unsigned se = (unsigned)__builtin_amdgcn_ballot_w64(fa.x & 1u), so_ = (unsigned)__builtin_amdgcn_ballot_w64(fa.z & 1u);
            unsigned dm = 0;
#pragma unroll
            for (int k = 0; k < 4; ++k) dm |= ((se >> k) & 1u) << (2 * k) | ((so_ >> k) & 1u) << (2 * k + 1);
            if (lane == 0) { *(LAS unsigned*)(L + L_TM + 8) = dm; *(LAS u64*)(L + L_TAG + 8) = tag; }
          }
        }
        OPAQUE(); SB();
      }
      if (i == TP0) { transp(0); SB(); }
      if (i == TP1) { transp(1); SB(); }
      if (i == TP2) { transp(2); SB(); }
      if (i == TP3) { transp(3); SB(); }
    }
#pragma unroll
    for (int m = 0; m < 8; ++m) V8[m] = cat(V[2 * m], V[2 * m + 1]);
    OPAQUE(); SB();
    STAMPW(0, 3); STAMPW(7, 4);
    if (w == 0) { COUNT(0, tt[0]); COUNT(60, __builtin_amdgcn_s_memtime()); COUNT(59, __builtin_amdgcn_s_memrealtime()); if (hasP1) { COUNT(1, tt[1]); COUNT(2, tt[2]); COUNT(5, tt[3]); COUNT(35, tt[4]); } }
    asm volatile("" : "+v"(g0), "+v"(g1), "+v"(g2));
    if (w < 4) {
      int spin = 0;
#pragma nounroll
      for (; spin < SPIN_CAP; ++spin) {
        if (!any(g0.y != tgx || g0.w != tgy || g1.y != tgx || g1.w != tgy || g2.y != tgx || g2.w != tgy)) break;
        OPAQUE();
        g0 = ldn(rsW, x1a, gatc); g1 = ldn(rsW, x1b, gatc); g2 = ldn(rsW, x1c, gatc);
        asm volatile("" : "+v"(g0), "+v"(g1), "+v"(g2));
      }
      f4 q = mfma(selT, g0, Z4); q = mfma(selT, g1, q); q = mfma(selT0, g2, q);
      if (lane < 16) { LAS unsigned* qp = (LAS unsigned*)qw; qp[0] = pk(q.x, q.y); qp[16] = pk(q.z, q.w); }
      if (w == 0) COUNT(41, spin);
      STAMPW(0, 6);
    }
  }
  f4 S[4] = {Z4, Z4, Z4, Z4}, O = Z4;
  PIN4(S); PINI(O);
  bar();
  STAMPW(0, 7);
  v2u xp = {0, 0}, b2v = {0, 0};
  f4 csq = Z4; f2 cs1 = {0.f, 0.f};
  unsigned t0 = 0, hm = 0;
#define HB(k) __builtin_amdgcn_readfirstlane((int)(((hm >> (k)) & 1u) << 13))
#define HOWN HB(xc_)
#define H1 HB(cls & 15)
#define H3 HB((cls >> 4) & 15)
#define H4 HB((cls >> 8) & 15)
#define H5 HB((cls >> 12) & 15)
  const int w_ = w;
  lptr CT = L + L_TAG;
  PINI(CT);
#define pub1 RS(0)
#define pub2 RS(1)
#define pub3 RS(2)
#define pub4 RS(3)
#define pub5 RS(4)
#define gat1 RS(5)
#define gat2 RS(6)
#define gat3 RS(7)
#define gat4 RS(8)
#define gat5 RS(9)
#define gatc RS(10)
#define mko RS(11)
#define cls RS(12)
#define slo RS(13)
#define wofs RS(14)
#define w1ofs RS(15)
#define w2ofs RS(16)
#define b1ofs RS(17)
#define xc_ RS(18)
#define ab RS(19)
#define KARG() const __attribute__((address_space(4))) Params* kp = (const __attribute__((address_space(4))) Params*)__builtin_amdgcn_kernarg_segment_ptr(); asm volatile("" : "+s"(kp))
  int mso = pub2;
  PINS(mso);
#pragma nounroll
  for (int lay = 0; lay < 2; ++lay) {
    const int xs = __builtin_amdgcn_readfirstlane(lay * SM_STRIDE), x4 = __builtin_amdgcn_readfirstlane(lay * PG), l1 = __builtin_amdgcn_readfirstlane(lay ? HALF : 0);
    int w = w_;
    PINS(w);
    {
      v4u AF[8];
      const v4u Q0 = lds16(qa, 0), Q1 = lds16(qa, 256);
      SB();
#pragma unroll
      for (int T = 0; T < 4; ++T) S[T] = smf(Q0, K8[2 * T], S[T], qidx);
#pragma unroll
      for (int T = 0; T < 4; ++T) S[T] = smf(Q1, K8[2 * T + 1], S[T], qidx);
      float s0, s1;
      asm("s_nop 7\n\t"
          "v_add_f32_dpp %0, %2, %3 quad_perm:[0,1,2,3] row_mask:0x1 bank_mask:0xf\n\tv_add_f32_dpp %1, %4, %5 quad_perm:[0,1,2,3] row_mask:0x1 bank_mask:0xf\n\t"
          "v_add_f32_dpp %0, %6, %7 quad_perm:[0,1,2,3] row_mask:0x2 bank_mask:0xf\n\tv_add_f32_dpp %1, %8, %9 quad_perm:[0,1,2,3] row_mask:0x2 bank_mask:0xf\n\t"
          "v_add_f32_dpp %0, %10, %11 quad_perm:[0,1,2,3] row_mask:0x4 bank_mask:0xf\n\tv_add_f32_dpp %1, %12, %13 quad_perm:[0,1,2,3] row_mask:0x4 bank_mask:0xf\n\t"
          "v_add_f32_dpp %0, %14, %15 quad_perm:[0,1,2,3] row_mask:0x8 bank_mask:0xf\n\tv_add_f32_dpp %1, %16, %17 quad_perm:[0,1,2,3] row_mask:0x8 bank_mask:0xf"
          : "=&v"(s0), "=&v"(s1)
          : "v"(S[0].x), "v"(S[0].y), "v"(S[0].z), "v"(S[0].w), "v"(S[1].x), "v"(S[1].y), "v"(S[1].z), "v"(S[1].w),
            "v"(S[2].x), "v"(S[2].y), "v"(S[2].z), "v"(S[2].w), "v"(S[3].x), "v"(S[3].y), "v"(S[3].z), "v"(S[3].w));
      auto sw = __builtin_amdgcn_permlane32_swap(asu(s0), asu(s1), false, false);
      float mx;
      asm("v_max_f32 %0, %1, %2\n\ts_nop 1\n\tv_max_f32_dpp %0, %0, %0 quad_perm:[1,0,3,2] row_mask:0xf bank_mask:0xf\n\ts_nop 1\n\t"
          "v_max_f32_dpp %0, %0, %0 quad_perm:[2,3,0,1] row_mask:0xf bank_mask:0xf\n\ts_nop 1\n\tv_max_f32_dpp %0, %0, %0 row_ror:4 row_mask:0xf bank_mask:0xf\n\ts_nop 1\n\t"
          "v_max_f32_dpp %0, %0, %0 row_ror:8 row_mask:0xf bank_mask:0xf\n\ts_nop 1\n\tv_max_f32_dpp %0, %0, %0 row_bcast:15 row_mask:0xa bank_mask:0xf\n\ts_nop 0"
          : "=&v"(mx) : "v"(asf(sw[0])), "v"(asf(sw[1])));
      const float m0 = asf(__builtin_amdgcn_readlane(asu(mx), 31)), m1 = asf(__builtin_amdgcn_readlane(asu(mx), 63));
      const float p0 = __builtin_amdgcn_exp2f(s0 - m0), p1 = __builtin_amdgcn_exp2f(s1 - m1);
      const unsigned pb = pk(p0, p1);
      *(LAS u16*)(pw) = (u16)pb; *(LAS u16*)(pw + 20) = (u16)pb; *(LAS u16*)(pw + 32) = (u16)(pb >> 16); *(LAS u16*)(pw + 52) = (u16)(pb >> 16);
#pragma unroll
      for (int m = 0; m < 8; ++m) AF[m] = lds16(pr, m * 272);
      SB();
      auto sl2 = __builtin_amdgcn_permlane32_swap(asu(p0), asu(p1), false, false);
      float ls = asf(sl2[0]) + asf(sl2[1]);
      ls += dppf<XOR1>(ls); ls += dppf<XOR2>(ls); ls += dppf<ROR4>(ls); ls += dppf<ROR8>(ls);
      ls += asf((unsigned)__builtin_amdgcn_update_dpp(0, (int)asu(ls), 0x142, 0xa, 0xf, false));
      if ((lane0 & 31) == 31) *(LAS f2*)mlw = f2{mx, ls};
      SB();
#pragma unroll
      for (int m = 0; m < 8; ++m) O = smf(AF[m], V8[m], O, pidx);
      *(LAS v2u*)aow = pk4(O);
    }
    bar();
    STAMPW(0, 8 + lay * 16);
#define XBPTR() lptr xb0 = L + L_XB + (col < 4 ? col : (col == 8 ? 4 : 5)) * XBS + hq * 16, xb1 = L + L_XB + ((col >= 4 && col < 8) ? col - 4 : (col == 12 ? 4 : 5)) * XBS + hq * 16; PINI(xb0); PINI(xb1)
#define STPTR() lptr sra = L + L_XB + (pp == 0 ? 4 : (pp == 1 ? q4 : 5)) * XBS + hq * 16, srb = L + L_XB + pp * XBS + hq * 16; int sso = col == hq ? col * 16 : 0x40000000; PINI(sra); PINI(srb); PINI(sso)
#define STATS(off) do { const v4u a0 = lds16(sra, 0), a1 = lds16(sra, 64), b0 = lds16(srb, 0), b1 = lds16(srb, 64); SB(); \
      f4 d_ = mfma(a0, b0, Z4); d_ = mfma(a1, b1, d_); stx2(rsW, v2u{cl(asu(d_.x)), cl(asu(d_.y))}, sso, off); } while (0)
#define STAT1 (pub1 + H1 + (((cls >> 16) & 15) < 8 ? 4 * BLK + 4096 - ((cls >> 16) & 15) * 896 : 4096 + 1024))
#define P1PREP() v4u A1[4]; int st1 = col < 8 ? pp * (((cls >> 16) & 15) < 8 ? BLK : 1024) + (8 * w + 4 * q4 + hq) * 16 : 0x40000000, sp1 = pub1 + H1; XBPTR(); \
      { const int base = L_WQ + (w >> 1) * 8192 + a128, t0 = 2 * (w & 1); \
        A1[0] = trfrag(L, base + (t0 ^ z128) * 32, 512); A1[1] = trfrag(L, base + (t0 ^ z128) * 32 + 4096, 512); \
        A1[2] = trfrag(L, base + ((t0 + 1) ^ z128) * 32, 512); A1[3] = trfrag(L, base + ((t0 + 1) ^ z128) * 32 + 4096, 512); } \
      PIN4(A1); PINI(st1); PINS(sp1)
#define P1RUN() do { if (hasP1) { const v4u b00 = lds16(xb0, 0), b01 = lds16(xb0, 64), b10 = lds16(xb1, 0), b11 = lds16(xb1, 64); SB(); \
        f4 d_ = mfma(A1[0], b00, Z4); d_ = mfma(A1[1], b01, d_); d_ = mfma(A1[2], b10, d_); d_ = mfma(A1[3], b11, d_); stx(rsW, split4(d_), st1, sp1); } } while (0)
#define CSQLOAD() const int ve = hq * X1_MSG + (16 * (w & 3) + col) * 16 + 4096;     \
      const int gc_ = gatc; const v4u c0 = ldx(rsW, ve, gc_), c1 = ldx(rsW, ve + 4 * X1_MSG, gc_), c2 = ldx(rsW, ve + (8 - hq) * X1_MSG, gc_)
#define CSQSUM() do { const unsigned one_ = (lane & 1) ? 0x3F800000u : 0x00003F80u; \
      const v4u sel4 = {(col & 2) ? 0u : one_, (col & 2) ? one_ : 0u, (col & 2) ? 0u : one_, (col & 2) ? one_ : 0u}; \
      csq = mfma(sel4, c0, Z4); csq = mfma(sel4, c1, csq); csq = mfma(hq == 0 ? sel4 : v4u{0, 0, 0, 0}, c2, csq); } while (0)
    if (w < 6) {
      int ymsg;
      if (w < 4) {
        FRESH(); A128();
        int so = gat2 + (w >> 1) * BLK + (w & 1) * 4 * X2_MSG + xs + ((HOWN ^ HALF) & l1), vm = hq * X2_MSG + 512 + (col >> 3) * 16;
        ymsg = col < 4 ? (4 * (2 * w + (hq >> 1)) + pp) * 16 + (hq & 1) * 8 : 0x40000000;
        PINS(so); PINI(vm); PINI(ymsg);
        int spin = 0;
        if (hasP3) {
          int vd = hq * X2_MSG + col * 16, sp = pub3 + xs + ((H3 ^ HALF) & l1);
          lptr p3a = L + L_WO + a128 + (w ^ z128) * 32, p3b = L + L_OB + pp * OBS + hq * 16;
          v4u pm0 = col < 8 ? pm : v4u{0, 0, 0, 0}, pm1 = col < 8 ? v4u{0, 0, 0, 0} : pm;
          lptr ow = L + L_OB + w * OBS + ((hq >> 1) * 128 + 8 * col + 4 * (hq & 1)) * 2;
          int dual3 = __builtin_amdgcn_readfirstlane(lay == 1 && ((cls >> 4) & 15) == 8);
          PINS(dual3); PINI(vd); PINS(sp); PINI(p3a); PINI(p3b); PINI(pm0); PINI(pm1); PINI(ow);
          v4u A[8];
#pragma unroll
          for (int k = 0; k < 8; ++k) A[k] = trfrag(p3a, k * 4096, 512);
          if (lay == 0) __builtin_amdgcn_s_sleep(D_X2); else LOOKAT(T2B);
          f4 o;
          for (;; ++spin) {
            OPAQUE();
            const v2u ml = ldx2(rsW, vm, so);
            SB();
            const v4u d0 = ldx(rsW, vd, so), d1 = ldx(rsW, vd + 256, so);
            SB();
            const float m = asf(ml.x), M = max32(max16(m));
            const float f = __builtin_amdgcn_exp2f(m - M);
            const float Ls = add32(add16(f * asf(ml.y)));
            const float fs = f * __builtin_amdgcn_rcpf(Ls);
            const unsigned gg = pk(fs, fs);
            o = mfma(pm0 & gg, d0, Z4); o = mfma(pm1 & gg, d1, o);
            if (!any(o.x != o.x)) break;
            if (!any(max(max(mx4(d0), mx4(d1)), max(ml.x, ml.y)) == SENT) || spin > SPIN_CAP) break;
          }
          OPAQUE();
          *(LAS v2u*)ow = pk4(o);
          if (lay == 1) LATE(T2B);
          STAMPW(0, 10 + lay * 16);
          bar();
          STAMPW(0, 11 + lay * 16);
          v4u B[8];
#pragma unroll
          for (int k = 0; k < 8; ++k) B[k] = lds16(p3b, k * 64);
          SB();
          f4 d = Z4;
#pragma unroll
          for (int k = 0; k < 8; ++k) d = mfma(A[k], B[k], d);
          const v2u yv = pack4(d);
          stx2(rsW, yv, ymsg, sp);
          if (dual3) stx2(rsW, yv, ymsg, sp ^ HALF);
          STAMPW(0, 20 + lay * 16);
        } else bar();
        if (w == 0) COUNT(42 + lay * 8, spin);
      } else {
        ymsg = 0;
        if (lay == 0) {
          FRESH();
          unsigned sn = SENT; PINI(sn);
          const v4u SENT4 = {sn, sn, sn, sn};
          const int lo = lane * 16, r1 = (cls >> 16) & 15;
          __builtin_amdgcn_s_sleep(D_W);
          const int OOB = 0x40000000;
          if (hasF && REWARM) {
            const int a4 = pub4 + (w - 4) * 2048;
            stx(rsW, SENT4, lo, a4); stx(rsW, SENT4, (w == 4 || lane < 56) ? lo : OOB, a4 + 1024); stx(rsW, SENT4, lane < 32 ? lo : OOB, pub5 + (w - 4) * HALF);
          }
          if (hasP1) {
            const int rs = r1 < 8 ? BLK : 1024, a1 = pub1 + (w - 4) * HALF;
            stx(rsW, SENT4, lo, a1); stx(rsW, SENT4, lo, a1 + rs); stx(rsW, SENT4, lo, a1 + 2 * rs); stx(rsW, SENT4, lo, a1 + 3 * rs);
            stx(rsW, SENT4, lane < 8 ? lo : OOB, STAT1 + (w - 4) * HALF);
          }
          if (hasF) {
            const int p4_ = pub4, p5_ = pub5, p2_ = pub2, p3_ = pub3, cl_ = cls, xo_ = xc_;
            drain();
            bar();
            for (int sp2 = 0; *(volatile LAS unsigned*)(CT + (L_TM - L_TAG) + 4) == 0u && sp2 < (SPIN_CAP << 4); ++sp2) __builtin_amdgcn_s_sleep(1);
            OPAQUE();
            hm = __builtin_amdgcn_readfirstlane(*(volatile LAS unsigned*)(CT + (L_TM - L_TAG) + 16));
            const int a0 = p4_ + PG + (HB((cl_ >> 8) & 15) ^ HALF), hi = (lane >> 5) << 13, l32 = (lane & 31) * 16;
            if (w == 4) {
              stx(rsW, SENT4, lo, a0); stx(rsW, SENT4, lo, a0 + 1024); stx(rsW, SENT4, lo, a0 + 2048); stx(rsW, SENT4, lane < 8 ? lo : OOB, a0 + 3840);
            } else {
              stx(rsW, SENT4, (lane < 32 || ((cl_ >> 12) & 15) == 8) ? l32 + (hi ^ HB((cl_ >> 12) & 15) ^ HALF) : OOB, p5_ + SM_STRIDE);
              stx(rsW, SENT4, lane < 40 ? lo : OOB, p2_ + SM_STRIDE + (HB(xo_) ^ HALF));
              stx(rsW, SENT4, (hasP3 && (lane < 32 || ((cl_ >> 4) & 15) == 8)) ? l32 + (hi ^ HB((cl_ >> 4) & 15) ^ HALF) : OOB, p3_ + SM_STRIDE);
            }
          } else {
            stx(rsW, SENT4, lane < 40 ? lo : OOB, pub2 + SM_STRIDE + (w - 4) * HALF);
            stx(rsW, SENT4, lane < 32 ? lo : OOB, pub3 + SM_STRIDE + (w - 4) * HALF);
            drain(); bar();
          }
        } else bar();
      }
      int spin = 0;
      if (hasF) {
        v4u A[6];
        lptr p5a, p5b;
        {
          FRESH();
          XBPTR();
          const int s0 = (hq & 1) * 32;
          lptr p4a0 = L + L_W1 + w * 4096 + hq * 512 + q4 * 64 + pp * 8 + s0, p4a1 = p4a0 + 32 - 2 * s0;
          int st4 = col < 8 ? w * 512 + (16 * q4 + 4 * hq + pp) * 16 : ((lay == 0 && pp == 0) ? 3072 + w * 128 + (col - 8 + hq) * 16 : 0x40000000), sp = pub4 + x4 + ((H4 ^ HALF) & l1);
          v4u A4[4] = {trfrag(p4a0, 0, 256), trfrag(p4a0, 2048, 256), trfrag(p4a1, 0, 256), trfrag(p4a1, 2048, 256)};
          PINI(st4); PINS(sp); PINI(p4a0); PINI(p4a1); PIN4(A4);
          bar();
          STAMPW(0, 13 + lay * 16);
          const v4u b00 = lds16(xb0, 0), b01 = lds16(xb0, 64), b10 = lds16(xb1, 0), b11 = lds16(xb1, 64);
          SB();
          f4 d = mfma(A4[0], b00, Z4); d = mfma(A4[1], b01, d); d = mfma(A4[2], b10, d); d = mfma(A4[3], b11, d);
          stx(rsW, split4(d), st4, sp);
          STAMPW(0, 14 + lay * 16);
        }
        if (lay == 0) { hm = __builtin_amdgcn_readfirstlane(*(LAS unsigned*)(CT + (L_TM - L_TAG) + 16)); t0 = __builtin_amdgcn_readfirstlane(*(LAS unsigned*)(CT + (L_TM - L_TAG))); }
        FRESH(); A128();
        const int pn = lane >> 2;
        const int vv = 2 * (q4 & 1) + (col & 1);
        const unsigned on = ((col >> 3) == (hq >> 1) && (col & 2) == 0) ? ((vv & 1) ? 0x3F800000u : 0x3F80u) : 0u;
        v4u selh = {(vv & 2) ? 0u : on, (vv & 2) ? on : 0u, (vv & 2) ? 0u : on, (vv & 2) ? on : 0u};
        v4u selh0 = (hq & 1) ? v4u{0, 0, 0, 0} : selh;
        const int pc = pn < 9 ? pn : 8;
        int so = gat4 + x4 + ((HOWN ^ HALF) & l1), vs = (pc >> 1) * BLK + (pc & 1) * 4096 + 3840 + pp * 16, sp5 = pub5 + xs + (H5 ^ HALF);
        float sm = pn < 9 ? (1.f / DM) : 0.f;
        lptr hw = L + L_HB + pp * HBS + HFO * 2;
        int vo4 = 4 * BLK + w * 512 + (16 * (hq >> 1) + col) * 16, vo = vo4 - 4 * BLK + (hq & 1) * 4096;
        int sa = so + BLK, sb = so + 2 * BLK, sc = so + 3 * BLK;
        int ve1 = 4 * BLK + 3072 + w * 128 + (4 * (hq >> 1) + q4) * 16, ve0 = ve1 + (2 * (hq & 1) + (pp >> 1) - 4) * BLK + (pp & 1) * 4096;
        p5a = L + L_W2 + a128 + ((w & 3) ^ z128) * 32; p5b = L + L_HB + pp * HBS + hq * 16;
        f2 bb = {blo(b1v), bhi(b1v)};
        int dual5 = __builtin_amdgcn_readfirstlane(((cls >> 12) & 15) == 8);
        PINS(dual5); PINS(so); PINS(sp5); PINI(selh); PINI(selh0); PINI(vs); PINI(sm); PINI(hw); PINI(vo); PINI(vo4); PINS(sa); PINS(sb); PINS(sc); PINI(ve0); PINI(ve1); PINI(p5a); PINI(p5b); PINI(bb);
#pragma unroll
        for (int k = 0; k < 6; ++k) A[k] = trfrag(p5a, k * 4096, 512);
        const unsigned T4 = lay == 0 ? T4A : T4B;
        LOOKAT(T4);
        f4 a; float rstd; f2 t;
        for (;; ++spin) {
          OPAQUE();
          const v2u st = ldx2(rsW, vs, so);
          v4u e0 = {0, 0, 0, 0}, e1 = e0;
          if (lay == 0) { e0 = ldx(rsW, ve0, so); e1 = ldx(rsW, ve1, so); }
          const v4u d0 = ldx(rsW, vo, so), d1 = ldx(rsW, vo, sa), d2 = ldx(rsW, vo, sb), d3 = ldx(rsW, vo, sc), d4 = ldx(rsW, vo4, so);
          SB();
          float s1 = asf(st.x) * sm, s2 = asf(st.y) * sm;
          s1 += dppf<ROR4>(s1); s2 += dppf<ROR4>(s2); s1 += dppf<ROR8>(s1); s2 += dppf<ROR8>(s2);
          s1 = add32(add16(s1)); s2 = add32(add16(s2));
          rstd = __builtin_amdgcn_rsqf(fmaxf(s2 - s1 * s1, 0.f) + 1e-5f);
          if (lay == 0) {
            f4 e = mfma(selh, e0, Z4); const f4 e8 = mfma(selh0, e1, Z4);
            e.x += dppf<XOR1>(e.x); e.y += dppf<XOR1>(e.y); e.x += dppf<XOR2>(e.x); e.y += dppf<XOR2>(e.y);
            cs1 = f2{e.x + e8.x, e.y + e8.y};
          }
          t = bb - (s1 * rstd) * cs1;
          SB();
          a = mfma(selh, d0, Z4); a = mfma(selh, d1, a); a = mfma(selh, d2, a); a = mfma(selh, d3, a); a = mfma(selh0, d4, a);
          if (!any(a.x != a.x || t.x != t.x)) break;
          if (!any(max(max(max(max(mx4(d0), mx4(d1)), max(mx4(d2), mx4(d3))), max(mx4(d4), max(st.x, st.y))), max(mx4(e0), mx4(e1))) == SENT) || spin > SPIN_CAP) break;
        }
        OPAQUE();
        STAMPW(0, 15 + lay * 16);
        const f2 v = f2{a.x, a.y} * rstd + t;
        const f2 e = v * (v * v * (-2.f * 0.7978845608028654f * 1.4426950408889634f * 0.044715f) + (-2.f * 0.7978845608028654f * 1.4426950408889634f));
        const float h0 = v.x * __builtin_amdgcn_rcpf(1.f + __builtin_amdgcn_exp2f(e.x)), h1 = v.y * __builtin_amdgcn_rcpf(1.f + __builtin_amdgcn_exp2f(e.y));
        *(LAS unsigned*)hw = pk(h0, h1);
        LATE(T4);
        bar();
        STAMPW(0, 16 + lay * 16);
        if (w < 4) {
          v4u B[6];
#pragma unroll
          for (int k = 0; k < 6; ++k) B[k] = lds16(p5b, k * 64);
          SB();
          f4 d = Z4;
#pragma unroll
          for (int k = 0; k < 6; ++k) d = mfma(A[k], B[k], d);
          const v2u zv = pack4(d);
          stx2(rsW, zv, ymsg, sp5);
          if (dual5) stx2(rsW, zv, ymsg, sp5 ^ HALF);
          STAMPW(0, 21 + lay * 16);
        }
      } else {
        bar();
        hm = __builtin_amdgcn_readfirstlane(*(LAS unsigned*)(CT + (L_TM - L_TAG) + 16)); t0 = __builtin_amdgcn_readfirstlane(*(LAS unsigned*)(CT + (L_TM - L_TAG)));
        bar();
      }
      if (w == 0) COUNT(44 + lay * 8, spin);
      if (lay == 1) return;
      FRESH(); A128();
      P1PREP(); STPTR();
      int sst = STAT1; PINS(sst);
      if (w >= 4) {
        CSQLOAD(); CSQSUM(); PINI(csq);
      }
      bar();
      STAMPW(0, 18);
      P1RUN();
      if (w == 3 && hasP1) STATS(sst);
      STAMPW(0, 19);
    } else {
      {
        const unsigned tm = now();
        const f2 ml0 = *(LAS f2*)mmr, ml1 = *(LAS f2*)(mmr + 64);
        const v4u B0 = lds16(mbr, 0), B1 = lds16(mbr, 2048);
        const int so = __builtin_amdgcn_readfirstlane(mso);
        SB();
        const float M = max32(max16(vmax(ml0.x, ml1.x)));
        const float f0 = __builtin_amdgcn_exp2f(ml0.x - M), f1 = __builtin_amdgcn_exp2f(ml1.x - M);
        const unsigned h0 = pk(f0, f0), h1 = pk(f1, f1);
        f4 o = mfma(pm & h0, B0, Z4); o = mfma(pm & h1, B1, o);
        const float Ls = add32(add16(blo(h0) * ml0.y + blo(h1) * ml1.y));
        if (lane0 == 0) stx(rsW, v4u{cl(asu(M)), cl(asu(Ls)), tm, 0u}, 512 + (w & 1) * 16, so);
        stx2(rsW, pack4(o), mst, so);
        STAMPW(6, 9 + lay * 16);
      }
      FRESH(); A128(); SEL8(); STPTR();
      if (lay == 0) { KARG(); const int o_ = (slo + TKO) * 2; xp = __builtin_amdgcn_raw_buffer_load_b64(mkrs(kp->x, 4 * DM * 2), o_ + pp * DM * 2, 0, 0); b2v = __builtin_amdgcn_raw_buffer_load_b64(mkrs(kp->b2, DM * 2), o_, 0, 0); PINI(xp); PINI(b2v); }
      v4u s8 = sel8;
      int v0 = hq * 512 + (w - 6) * 256 + col * 16;
      lptr xw = L + L_XB + pp * XBS + TKO * 2;
      {
        int so = gat3 + xs + ((HOWN ^ HALF) & l1), so1 = so + 4096, sp = pub4 + x4 + ((H4 ^ HALF) & l1) + 3840;
        f4 xr = unp(xp);
        PINS(so); PINS(so1); PINS(sp); PINI(s8); PINI(v0); PINI(xw); PINI(xr);
        if (lay == 0) {
          const unsigned tg = *(LAS unsigned*)(CT + 0 + 8) & ~3u, dm = __builtin_amdgcn_readfirstlane(*(LAS unsigned*)(CT + (L_TM - L_TAG) + 8));
          if (w == 7 && ((cls >> 20) & 15) == 15) {
            unsigned vote = 0;
            if (!hasF) {
              const int po = lane < 8 ? WS_PROBE + (cls >> 24) * 128 + lane * 16 : 0x40000000;
              u64 ta, tb, tc, td;
              asm volatile("s_waitcnt vmcnt(0)\n\tbuffer_store_dwordx4 %4, %5, %6, 0 offen sc1\n\tbuffer_store_dwordx4 %4, %5, %6, %7 offen sc1\n\t"
                           "s_waitcnt vmcnt(1)\n\ts_memtime %0\n\ts_waitcnt vmcnt(0)\n\ts_memtime %1\n\t"
                           "buffer_store_dwordx4 %4, %5, %6, %7 offen sc1\n\tbuffer_store_dwordx4 %4, %5, %6, 0 offen sc1\n\t"
                           "s_waitcnt vmcnt(1)\n\ts_memtime %2\n\ts_waitcnt vmcnt(0)\n\ts_memtime %3\n\ts_waitcnt lgkmcnt(0)"
                           : "=&s"(ta), "=&s"(tb), "=&s"(tc), "=&s"(td) : "v"(s8), "v"(po), "s"(rsW), "s"(HALF) : "memory");
              vote = 2u | (((unsigned)(tb - ta) > (unsigned)(td - tc) ? 0u : 1u) ^ ((dm >> xc_) & 1u));
            } else __builtin_amdgcn_s_sleep(D_X2 - 12);
            const int g2 = gat2;
            for (int sp2 = 0;; ++sp2) {
              OPAQUE();
              const unsigned t = __builtin_amdgcn_raw_buffer_load_b32(rsW, (col >> 3) * BLK + (col & 7) * X2_MSG + 512 + 16 + 8, g2, AUX_SC1);
              if (!any(t == SENT) || sp2 > SPIN_CAP) break;
            }
            if (lane0 == 0) stx(rsW, v4u{tg | vote, ~tg, now(), 0u}, 0, WS_HER + (cls >> 24) * 128);
          }
          bar();
          v4u hv;
          for (int sp2 = 0;; ++sp2) {
            OPAQUE();
            hv = ldx(rsW, col * 128, WS_HER);
            if (!any((hv.x & ~3u) != tg || hv.y != ~tg) || sp2 > SPIN_CAP) break;
          }
          hm = NUMA ? dm ^ (__builtin_popcountll(__builtin_amdgcn_ballot_w64((hv.x & 3u) == 3u) & 0xFFFFull) > 4 ? 0xFFu : 0u) : 0u;
          t0 = tmax16(hv.z & 0x7FFFFFFFu);
          STAMPW(6, 38);
        } else bar();
        const unsigned T3 = lay == 0 ? T3A : T3B;
        LOOKAT(T3);
        f4 y;
        int spin = 0;
        for (;; ++spin) {
          OPAQUE();
          const v4u d0 = ldx(rsW, v0, so), d1 = ldx(rsW, v0 + 2048, so), d2 = ldx(rsW, v0, so1), d3 = ldx(rsW, v0 + 2048, so1);
          if (lay == 0 && lane0 == 0) { *(volatile LAS unsigned*)(CT + (L_TM - L_TAG)) = t0; *(volatile LAS unsigned*)(CT + (L_TM - L_TAG) + 16) = hm; *(volatile LAS unsigned*)(CT + (L_TM - L_TAG) + 4) = 1u; }
          SB();
          y = mfma(s8, d0, Z4); y = mfma(s8, d1, y); y = mfma(s8, d2, y); y = mfma(s8, d3, y);
          if (!any(y.x != y.x)) break;
          if (!any(max(max(mx4(d0), mx4(d1)), max(mx4(d2), mx4(d3))) == SENT) || spin > SPIN_CAP) break;
        }
        OPAQUE();
        if (w == 6) { COUNT(43 + lay * 8, spin); COUNT(55, hm); }
        STAMPW(6, 12 + lay * 16);
        xp = pk4(xr + unp(pk4(y)));
        if (lane0 < 32) *(LAS v2u*)xw = xp;
        LATE(T3);
        bar();
        if (w == 6) { if (hasF) STATS(sp); }
        else if (lay == 0 && c == 0 && lane0 == 0) stx2(rsW, *(LAS v2u*)(CT + 0 + 8), 0, WS_EPOCH);
      }
      if (lay == 1 && !isOut) { bar(); return; }
      int so = gat5 + xs + (HOWN ^ HALF), so1 = so + 4096, so2 = so + BLK;
      int oo = hq < 2 ? (pp * DM + slo + TKO) * 2 : 0x40000000;
      f4 xr = unp(xp), bb = unp(b2v);
      KARG();
      rsrc_t rout = mkrs(kp->out, 4 * DM * 2);
      asm volatile("" : "+s"(rout));
      P1PREP();
      PINS(so); PINS(so1); PINS(so2); PINI(xr); PINI(bb); PINI(oo);
      if (lay == 0) { CSQLOAD(); CSQSUM(); PINI(csq); }
      bar();
      const unsigned T5 = lay == 0 ? T5A : T5B;
      LOOKAT(T5);
      f4 y;
      int spin = 0;
      for (;; ++spin) {
        OPAQUE();
        const v4u d0 = ldx(rsW, v0, so), d1 = ldx(rsW, v0 + 2048, so), d2 = ldx(rsW, v0, so1), d3 = ldx(rsW, v0 + 2048, so1), d4 = ldx(rsW, v0, so2), d5 = ldx(rsW, v0 + 2048, so2);
        SB();
        y = mfma(s8, d0, Z4); y = mfma(s8, d1, y); y = mfma(s8, d2, y); y = mfma(s8, d3, y); y = mfma(s8, d4, y); y = mfma(s8, d5, y);
        if (!any(y.x != y.x)) break;
        if (!any(max(max(max(mx4(d0), mx4(d1)), max(mx4(d2), mx4(d3))), max(mx4(d4), mx4(d5))) == SENT) || spin > SPIN_CAP) break;
      }
      OPAQUE();
      if (w == 6) COUNT(45 + lay * 8, spin);
      STAMPW(6, 17 + lay * 16);
      xp = pk4(xr + unp(pk4(unp(pk4(y)) + bb)));
      if (lay == 1) {
        __builtin_amdgcn_raw_buffer_store_b64(xp, rout, oo, 0, OUT_AUX);
        STAMPW(6, 34);
        if (w == 6) { COUNT(61, __builtin_amdgcn_s_memtime()); }
        return;
      }
      if (lane0 < 32) *(LAS v2u*)xw = xp;
      LATE(T5);
      bar();
      P1RUN();
      mso = pub2 + SM_STRIDE + (HOWN ^ HALF);
    }
#pragma unroll
    for (int T = 0; T < 4; ++T) S[T] = Z4;
    O = Z4;
    PIN4(S); PINI(O);
    if (w >= 4) {
      FRESH();
      const unsigned one = (lane & 1) ? 0x3F800000u : 0x00003F80u;
      v4u sel4 = {(col & 2) ? 0u : one, (col & 2) ? one : 0u, (col & 2) ? 0u : one, (col & 2) ? one : 0u};
      v4u sel40 = hq == 0 ? sel4 : v4u{0, 0, 0, 0};
      int so = gat1 + HOWN + ab * BLK, so1 = so + 4096, so2 = gat1 + HOWN + 4 * BLK + ab * 1024, so3 = gat1 + HOWN + 4 * BLK + 4096 + ab * 16;
      int v2 = (16 * (w - 4) + col) * 16, v0 = v2 + hq * 1024, vs = (col < 9 ? col : 8) * 128;
      float sm = col < 9 ? (1.f / DM) : 0.f;
      lptr qw = L + QPW(w - 4);
      PINS(so); PINS(so1); PINS(so2); PINS(so3); PINI(sel4); PINI(sel40); PINI(v0); PINI(v2); PINI(vs); PINI(qw); PINI(sm);
      LOOKAT(T1B);
      f4 d, cc; float rs;
      int spin = 0;
      for (;; ++spin) {
        OPAQUE();
        const v2u st = ldx2(rsW, vs, so3);
        SB();
        const v4u d0 = ldx(rsW, v0, so), d1 = ldx(rsW, v0, so1), d2 = ldx(rsW, v2, so2);
        SB();
        float s1 = asf(st.x) * sm, s2 = asf(st.y) * sm;
        s1 += dppf<XOR1>(s1); s2 += dppf<XOR1>(s2); s1 += dppf<XOR2>(s1); s2 += dppf<XOR2>(s2);
        SB();
        d = mfma(sel4, d0, Z4); d = mfma(sel4, d1, d); d = mfma(sel40, d2, d);
        SB();
        s1 += dppf<ROR4>(s1); s2 += dppf<ROR4>(s2); s1 += dppf<ROR8>(s1); s2 += dppf<ROR8>(s2);
        rs = __builtin_amdgcn_rsqf(fmaxf(s2 - s1 * s1, 0.f) + 1e-5f) * CS;
        cc = (-s1 * rs) * csq;
        d = d * rs + cc;
        if (!any(d.x != d.x)) break;
        if (!any(max(max(max(mx4(d0), mx4(d1)), mx4(d2)), max(st.x, st.y)) == SENT) || spin > SPIN_CAP) break;
      }
      OPAQUE();
      LATE(T1B);
      asm volatile(".Lfwd_end:");
      if (w == 4) COUNT(49, spin);
      STAMPW(4, 22);
      if (lane0 < 16) { LAS unsigned* qp = (LAS unsigned*)qw; qp[0] = pk(d.x, d.y); qp[16] = pk(d.z, d.w); }
    }
    bar();
    STAMPW(0, 23);
  }
}

extern "C" void kernel_launch(void* const* d_in, const int* in_sizes, int n_in, void* d_out, int out_size, void* d_ws, size_t ws_size, hipStream_t stream) {
  Params p;
  p.x = (const u16*)d_in[0]; p.kc = (const u16*)d_in[1]; p.vc = (const u16*)d_in[2]; p.wq = (const u16*)d_in[3]; p.wo = (const u16*)d_in[4];
  p.w1 = (const u16*)d_in[6]; p.b1 = (const u16*)d_in[7]; p.w2 = (const u16*)d_in[8]; p.b2 = (const u16*)d_in[9];
  p.out = (u16*)d_out; p.ws = (unsigned char*)d_ws;
  (void)hipFuncSetAttribute((const void*)fused_fwd, hipFuncAttributeMaxDynamicSharedMemorySize, L_BASE + L_BYTES);
  hipLaunchKernelGGL(fused_fwd, dim3(256), dim3(512), L_BASE + L_BYTES, stream, p);
}
```
